```python
import torch

def _softmax(x, axis=-1):
    return torch.softmax(x, dim=axis)
def _gelu(x):
    return torch.nn.functional.gelu(x, approximate="tanh")
def _silu(x):
    return torch.nn.functional.silu(x)
def _sigmoid(x):
    return torch.sigmoid(x)
def _layernorm(x, axis=-1, eps=1e-5):
    m = x.mean(dim=axis, keepdim=True)
    v = x.var(dim=axis, keepdim=True, unbiased=False)
    return (x - m) / torch.sqrt(v + eps)
def _scatter_add(tgt, idx, src, axis=0):
    return tgt.clone().index_add_(axis, idx, src)
def _segment_sum(src, seg, n):
    out = torch.zeros((n,) + src.shape[1:], dtype=src.dtype)
    return out.index_add_(0, seg, src)
def _pad(x, pw):
    flat = tuple(v for pair in reversed(pw) for v in pair)
    return torch.nn.functional.pad(x, flat)

kernel_name = 'concat_heads__ssm_conv_s4__linear_kernel_hedgehog__prompt'

def setup_inputs():

    _w = (lambda a: (lambda t: t.to(torch.bfloat16) if t.is_floating_point() else t)(torch.as_tensor(a)))
    def _A_setup():
        B, L, D, N = 1, 2048, 640, 128
        A_re = -(lambda _x: torch.exp(torch.as_tensor(_x)))((lambda *s: torch.randn(*s, dtype=torch.bfloat16) * 3.2)(D, N))
        A_im = torch.pi * (lambda *s: torch.randn(*s, dtype=torch.bfloat16) * 0.2)(D, N)
        B_re = (lambda *s: torch.randn(*s, dtype=torch.bfloat16) * 0.4)(D, N)
        B_im = (lambda *s: torch.randn(*s, dtype=torch.bfloat16) * 0.4)(D, N)
        C_re = (lambda *s: torch.randn(*s, dtype=torch.bfloat16) * 0.4)(D, N)
        C_im = (lambda *s: torch.randn(*s, dtype=torch.bfloat16) * 0.4)(D, N)
        log_dt = (lambda *s: torch.randn(*s, dtype=torch.bfloat16) * 0.2)(D) * 0.5 - 4.0
        Dskip = (lambda *s: torch.randn(*s, dtype=torch.bfloat16) * 0.4)(D)
        return {
            "u": _w((lambda *s: torch.randn(*s, dtype=torch.bfloat16) * 0.05)(B, L, D)),
            "A_re": _w(A_re),
            "A_im": _w(A_im),
            "B_re": _w(B_re),
            "B_im": _w(B_im),
            "C_re": _w(C_re),
            "C_im": _w(C_im),
            "log_dt": _w(log_dt),
            "Dskip": _w(Dskip),
        }
    _A_in = _A_setup()

    def _B_setup():
        B, S, D, H = 1, 2048, 640, 32
        Dh = D // H
        return {
            "x": _w((lambda *s: torch.randn(*s, dtype=torch.bfloat16) * 0.4)(B, S, D)),
            "wqkv": _w((lambda *s: torch.randn(*s, dtype=torch.bfloat16) * 0.4)(D, 3 * D)),
            "w_phi": _w((lambda *s: torch.randn(*s, dtype=torch.bfloat16) * 0.4)(H, Dh, Dh) * (Dh ** -0.5)),
            "wo": _w((lambda *s: torch.randn(*s, dtype=torch.bfloat16) * 0.05)(D, D)),
        }
    _B_in = _B_setup()

    return {
        "A_u": _A_in["u"],
        "A_A_re": _A_in["A_re"],
        "A_A_im": _A_in["A_im"],
        "A_B_re": _A_in["B_re"],
        "A_B_im": _A_in["B_im"],
        "A_C_re": _A_in["C_re"],
        "A_C_im": _A_in["C_im"],
        "A_log_dt": _A_in["log_dt"],
        "A_Dskip": _A_in["Dskip"],
        "B_x": _B_in["x"],
        "B_wqkv": _B_in["wqkv"],
        "B_w_phi": _B_in["w_phi"],
        "B_wo": _B_in["wo"],
    }

def reference(A_u, A_A_re, A_A_im, A_B_re, A_B_im, A_C_re, A_C_im, A_log_dt, A_Dskip, B_x, B_wqkv, B_w_phi, B_wo):
    def _A_ref(u, A_re, A_im, B_re, B_im, C_re, C_im, log_dt, Dskip):
        Bsz, L, D = u.shape
        dt = torch.exp(log_dt)[:, None]
        A = A_re + 1j * A_im
        Bc = B_re + 1j * B_im
        Cc = C_re + 1j * C_im
        logdA = dt * A
        dB = dt * Bc
        l = (lambda x, dt: x.to(dt))(torch.arange(L), A_re.dtype)
        P = torch.exp(logdA[:, :, None] * l[None, None, :])
        K = torch.einsum("dn,dnl,dn->dl", Cc, P, dB).real
        L2 = 2 * L
        Kp = _pad(K, ((0, 0), (0, L)))
        up = (lambda x, *d: x.permute(*d))(u, 0, 2, 1)
        up = _pad(up, ((0, 0), (0, 0), (0, L)))
        Kf = (lambda x, axes=(-2,-1): torch.fft.rfft2(x.float(), dim=axes))(Kp[:, None, :], axes=(-2, -1))
        Uf = (lambda x, axes=(-2,-1): torch.fft.rfft2(x.float(), dim=axes))(up[:, :, None, :], axes=(-2, -1))
        Yf = Uf * Kf[None, :, :, :]
        y = (lambda x, s, axes=(-2,-1): torch.fft.irfft2(x, s=s, dim=axes).to(torch.bfloat16))(Yf, (1, L2), axes=(-2, -1))[:, :, 0, :L]
        y = (lambda x, *d: x.permute(*d))(y, 0, 2, 1) + u * Dskip[None, None, :]
        return y

    def _B_ref(x, wqkv, w_phi, wo):
        B, S, D = x.shape
        H = 32; Dh = D // H
        qkv = (lambda x, *d: x.permute(*d))((x @ wqkv).reshape(B, S, 3, H, Dh), 2, 0, 3, 1, 4)
        q, k, v = qkv[0], qkv[1], qkv[2]
        pq = torch.einsum("bhsd,hdf->bhsf", q, w_phi)
        pk = torch.einsum("bhsd,hdf->bhsf", k, w_phi)
        phi_q = torch.concatenate([_softmax(pq, axis=-1), _softmax(-pq, axis=-1)], axis=-1)
        phi_k = torch.concatenate([_softmax(pk, axis=-1), _softmax(-pk, axis=-1)], axis=-1)
        kv = torch.einsum("bhsf,bhsd->bhfd", phi_k, v)
        z  = torch.einsum("bhsf->bhf", phi_k)
        num = torch.einsum("bhqf,bhfd->bhqd", phi_q, kv)
        den = torch.einsum("bhqf,bhf->bhq", phi_q, z)[..., None] + 1e-6
        o = num / den
        return (lambda x, *d: x.permute(*d))(o, 0, 2, 1, 3).reshape(B, S, D) @ wo

    a = _A_ref(A_u, A_A_re, A_A_im, A_B_re, A_B_im, A_C_re, A_C_im, A_log_dt, A_Dskip)
    b = _B_ref(B_x, B_wqkv, B_w_phi, B_wo)
    return torch.concatenate([a, b], axis=-1)
```

```cpp
#include <hip/hip_runtime.h>
#include <hip/hip_bf16.h>
typedef unsigned short u16;
__device__ __forceinline__ float b2f(u16 h) { return __uint_as_float((unsigned)h << 16); }
__device__ __forceinline__ u16 f2b(float f) { __hip_bfloat16 b = __float2bfloat16(f); return *(u16*)&b; }
__device__ __forceinline__ float rb(float f) { return b2f(f2b(f)); }

__global__ void kgen(const u16* Are, const u16* Aim, const u16* Bre, const u16* Bim, const u16* Cre, const u16* Cim,
                     const u16* logdt, float* K) {
    int d = blockIdx.x;
    __shared__ float sa[128], sb[128], wr[128], wi[128];
    float dt = rb(expf(b2f(logdt[d])));
    if (threadIdx.x < 128) {
        int n = threadIdx.x; int i = d * 128 + n;
        sa[n] = dt * b2f(Are[i]); sb[n] = dt * b2f(Aim[i]);
        float br = dt * b2f(Bre[i]), bi = dt * b2f(Bim[i]);
        float cr = b2f(Cre[i]), ci = b2f(Cim[i]);
        wr[n] = cr * br - ci * bi; wi[n] = cr * bi + ci * br;
    }
    __syncthreads();
    for (int l = threadIdx.x; l < 2048; l += 256) {
        float lq = rb((float)l);
        float acc = 0;
        for (int n = 0; n < 128; n++) {
            float e = expf(sa[n] * lq); float s, c; sincosf(sb[n] * lq, &s, &c);
            acc += e * (wr[n] * c - wi[n] * s);
        }
        K[d * 2048 + l] = acc;
    }
}
__global__ void conv(const u16* u, const float* K, const u16* Dskip, u16* out) {
    int d = blockIdx.x;
    __shared__ float su[2048], sk[2048];
    for (int l = threadIdx.x; l < 2048; l += 256) { su[l] = b2f(u[l * 640 + d]); sk[l] = K[d * 2048 + l]; }
    __syncthreads();
    float ds = b2f(Dskip[d]);
    for (int t = threadIdx.x; t < 2048; t += 256) {
        float acc = 0;
        for (int j = 0; j <= t; j++) acc += sk[j] * su[t - j];
        out[t * 1280 + d] = f2b(rb(acc) + rb(su[t] * ds));
    }
}
__global__ void gemm_naive(const u16* A, const u16* B, u16* C, int N, int Kd, int ldc) {
    __shared__ float sA[16][65];
    int r0 = blockIdx.y * 16, c = blockIdx.x * 64 + (threadIdx.x & 63), rr = threadIdx.x >> 6;
    float acc[4] = {0, 0, 0, 0};
    for (int k0 = 0; k0 < Kd; k0 += 64) {
        __syncthreads();
        for (int i = threadIdx.x; i < 16 * 64; i += 256) sA[i >> 6][i & 63] = b2f(A[(r0 + (i >> 6)) * Kd + k0 + (i & 63)]);
        __syncthreads();
        for (int k = 0; k < 64; k++) { float b = b2f(B[(k0 + k) * N + c]);
            for (int j = 0; j < 4; j++) acc[j] += sA[rr * 4 + j][k] * b; }
    }
    for (int j = 0; j < 4; j++) C[(r0 + rr * 4 + j) * ldc + c] = f2b(acc[j]);
}
__global__ void feat(const u16* qkv, const u16* wphi, u16* phiq, u16* phik) {
    int s = blockIdx.x * 4 + (threadIdx.x >> 6); int hh = threadIdx.x & 63; int h = hh & 31, which = hh >> 5;
    const u16* q = qkv + s * 1920 + which * 640 + h * 20;
    float p[20];
    for (int f = 0; f < 20; f++) { float a = 0; for (int dd = 0; dd < 20; dd++) a += b2f(q[dd]) * b2f(wphi[h * 400 + dd * 20 + f]); p[f] = rb(a); }
    float mx = -1e30f, mn = 1e30f; for (int f = 0; f < 20; f++) { mx = fmaxf(mx, p[f]); mn = fminf(mn, p[f]); }
    float s1 = 0, s2 = 0; for (int f = 0; f < 20; f++) { s1 += expf(p[f] - mx); s2 += expf(mn - p[f]); }
    u16* o = (which ? phik : phiq) + (s * 32 + h) * 40;
    for (int f = 0; f < 20; f++) { o[f] = f2b(expf(p[f] - mx) / s1); o[20 + f] = f2b(expf(mn - p[f]) / s2); }
}
__global__ void kvk(const u16* phik, const u16* qkv, float* kv) {
    int h = blockIdx.x; int t = threadIdx.x; if (t >= 840) return;
    int f = t / 21, dd = t % 21; float a = 0;
    for (int s = 0; s < 2048; s++) { float v = dd < 20 ? b2f(qkv[s * 1920 + 1280 + h * 20 + dd]) : 1.f; a += b2f(phik[(s * 32 + h) * 40 + f]) * v; }
    kv[h * 840 + t] = rb(a);
}
__global__ void ok(const u16* phiq, const float* kv, u16* o) {
    int s = blockIdx.x; int t = threadIdx.x; int h = t / 20, dd = t % 20;
    float num = 0, den = 0;
    for (int f = 0; f < 40; f++) { float p = b2f(phiq[(s * 32 + h) * 40 + f]); num += p * kv[h * 840 + f * 21 + dd]; den += p * kv[h * 840 + f * 21 + 20]; }
    o[s * 640 + t] = f2b(rb(num) / rb(rb(den) + 1e-6f));
}
__global__ void emptyk() {}
extern "C" void kernel_launch(void* const* d_in, const int* in_sizes, int n_in,
                              void* d_out, int out_size, void* d_ws, size_t ws_size,
                              hipStream_t stream) {
    const u16* const* in = (const u16* const*)d_in;
    char* ws = (char*)d_ws;
    float* K = (float*)ws; u16* qkv = (u16*)(ws + (8 << 20)); u16* phiq = (u16*)(ws + (16 << 20)); u16* phik = (u16*)(ws + (24 << 20));
    float* kv = (float*)(ws + (32 << 20)); u16* o = (u16*)(ws + (40 << 20));
    u16* out = (u16*)d_out;
    kgen<<<640, 256, 0, stream>>>(in[1], in[2], in[3], in[4], in[5], in[6], in[7], K);
    conv<<<640, 256, 0, stream>>>(in[0], K, in[8], out);
    gemm_naive<<<dim3(30, 128), 256, 0, stream>>>(in[9], in[10], qkv, 1920, 640, 1920);
    feat<<<512, 256, 0, stream>>>(qkv, in[11], phiq, phik);
    kvk<<<32, 896, 0, stream>>>(phik, qkv, kv);
    ok<<<2048, 640, 0, stream>>>(phiq, kv, o);
    gemm_naive<<<dim3(10, 128), 256, 0, stream>>>(o, in[12], out + 640, 640, 640, 1280);
}
```

```cpp
#include <hip/hip_runtime.h>
#include <hip/hip_bf16.h>

typedef unsigned short u16;
typedef unsigned u32;
typedef unsigned long long u64;
typedef __attribute__((ext_vector_type(8))) short s16x8;
typedef __attribute__((ext_vector_type(4))) short s16x4;
typedef __attribute__((ext_vector_type(16))) float f32x16;
typedef __attribute__((ext_vector_type(4))) float f32x4;
typedef __attribute__((ext_vector_type(2))) float f32x2;
typedef __attribute__((ext_vector_type(4))) u32 u32x4;
typedef __attribute__((ext_vector_type(2))) u32 u32x2;
typedef __attribute__((ext_vector_type(2))) __bf16 bf16x2;
#define LAS __attribute__((address_space(3)))
#define GAS __attribute__((address_space(1)))
#define DI __device__ __forceinline__
#define SB __builtin_amdgcn_sched_barrier(0)

constexpr int LDS_BYTES = 81920;
constexpr u32 WS_EPOCH = 0, WS_F1 = 4096, WS_F2 = 4096 + 32768, WS_PART = 131072, WS_O = 2097152;
constexpr u32 WS_STAMP = 8u << 20;
constexpr u32 kSpinCap = 1u << 18;
constexpr u32 CODE_PF_BYTES = 28672;

DI u32 pk(float a, float b) { f32x2 v = {a, b}; return __builtin_bit_cast(u32, __builtin_convertvector(v, bf16x2)); }
DI float lo2f(u32 x) { return __uint_as_float(x << 16); }
DI float hi2f(u32 x) { return __uint_as_float(x & 0xffff0000u); }
DI float b2f(u16 h) { return __uint_as_float((u32)h << 16); }
DI s16x4 trd(u32 a) { return __builtin_amdgcn_ds_read_tr16_b64_v4i16((LAS s16x4*)(size_t)a); }
DI s16x8 cat(s16x4 a, s16x4 b) { return __builtin_shufflevector(a, b, 0, 1, 2, 3, 4, 5, 6, 7); }
DI s16x8 frag(u32 a, u32 b, u32 c, u32 d) { u32x4 v = {a, b, c, d}; return __builtin_bit_cast(s16x8, v); }
template <class T> DI T ldsr(u32 a) { return *(LAS T*)(size_t)a; }
template <class T> DI void ldsw(u32 a, T v) { *(LAS T*)(size_t)a = v; }
template <int N> DI void waitv() { asm volatile("s_waitcnt vmcnt(%0)" ::"i"(N) : "memory"); }
DI void waitl0() { asm volatile("s_waitcnt lgkmcnt(0)" ::: "memory"); }
DI void bar() { asm volatile("s_waitcnt lgkmcnt(0)\n\ts_barrier" ::: "memory"); }
DI f32x16 mfma32(s16x8 a, s16x8 b, f32x16 c) { return __builtin_amdgcn_mfma_f32_32x32x16_bf16(a, b, c, 0, 0, 0); }
DI f32x4 mfma16(s16x8 a, s16x8 b, f32x4 c) { return __builtin_amdgcn_mfma_f32_16x16x32_bf16(a, b, c, 0, 0, 0); }
DI void dma(u32 m0v, u32 voff, const void* sbase) {
    asm volatile("s_mov_b32 m0, %0\n\ts_nop 0\n\tglobal_load_lds_dwordx4 %1, %2" ::"s"(m0v), "v"(voff), "s"(sbase) : "memory");
}
DI void dma_m(u32 m0v, u32 voff, const void* sbase, u32 mlo, u32 mhi) {
    asm volatile("s_mov_b32 exec_lo, %3\n\ts_mov_b32 exec_hi, %4\n\ts_mov_b32 m0, %0\n\ts_nop 0\n\tglobal_load_lds_dwordx4 %1, %2\n\ts_mov_b64 exec, -1"
                 ::"s"(m0v), "v"(voff), "s"(sbase), "i"(mlo), "i"(mhi) : "memory");
}
#define STAMP(i)
#define STAMP_ARG
#define STAMP_PASS

DI void wait_flags(const char* fl, u64 tag, int l) {
    const GAS u64* p = (const GAS u64*)(fl + (l & 15) * 128);
#pragma unroll 1
    for (u32 s = 0; s < kSpinCap; s++) {
        u64 v = __hip_atomic_load(p, __ATOMIC_RELAXED, __HIP_MEMORY_SCOPE_AGENT);
        if (__all(v == tag)) break;
        __builtin_amdgcn_s_sleep(1);
    }
}

DI void features(const float (&v)[20], const s16x8 (&wf)[2][3], u32 (&phi)[20]) {
    u32 b[10];
#pragma unroll
    for (int i = 0; i < 10; i++) b[i] = pk(v[2 * i], v[2 * i + 1]);
    const s16x8 B0 = frag(b[0], b[1], b[2], b[3]), B1 = frag(b[4], b[5], b[6], b[7]), B2 = frag(b[8], b[9], 0, 0);
    f32x16 p0 = {}, p1 = {};
    p0 = mfma32(wf[0][0], B0, p0); p1 = mfma32(wf[1][0], B0, p1);
    p0 = mfma32(wf[0][1], B1, p0); p1 = mfma32(wf[1][1], B1, p1);
    p0 = mfma32(wf[0][2], B2, p0); p1 = mfma32(wf[1][2], B2, p1);
    float p[20];
#pragma unroll
    for (int i = 0; i < 10; i++) {
        u32 t = i < 8 ? pk(p0[2 * i], p0[2 * i + 1]) : pk(p1[2 * i - 16], p1[2 * i - 15]);
        p[2 * i] = lo2f(t); p[2 * i + 1] = hi2f(t);
    }
    float mx = p[0], mn = p[0];
#pragma unroll
    for (int i = 1; i < 20; i++) { mx = fmaxf(mx, p[i]); mn = fminf(mn, p[i]); }
    const float L2E = 1.4426950408889634f;
    const float cx = -mx * L2E, cn = mn * L2E;
    float e[40], s1 = 0.f, s2 = 0.f;
#pragma unroll
    for (int i = 0; i < 20; i++) {
        e[i] = __builtin_amdgcn_exp2f(__builtin_fmaf(p[i], L2E, cx));
        e[20 + i] = __builtin_amdgcn_exp2f(__builtin_fmaf(p[i], -L2E, cn));
        s1 += e[i]; s2 += e[20 + i];
    }
    const float r1 = __builtin_amdgcn_rcpf(s1), r2 = __builtin_amdgcn_rcpf(s2);
#pragma unroll
    for (int i = 0; i < 10; i++) { phi[i] = pk(e[2 * i] * r1, e[2 * i + 1] * r1); phi[10 + i] = pk(e[20 + 2 * i] * r2, e[21 + 2 * i] * r2); }
}

constexpr int LX = 0, LW = 49152, WBLK = 960, WSLOT = 8 * WBLK, LMISC = 79872, LZERO = LMISC + 1600;
constexpr int LIMG = 16384, LTAB = 76800, TROW = 56, THEAD = 40 * TROW, WOSLOT = 25600;

DI void path_b(const u16* __restrict__ x, const u16* __restrict__ W, const u16* __restrict__ wphi, const u16* __restrict__ wo,
               u16* __restrict__ out, char* __restrict__ ws STAMP_ARG) {
    const int bid = blockIdx.x, xcd = bid & 7, jj = bid >> 3;
    const int r = 4 * (xcd >> 1) + (jj & 3), c = 8 * (xcd & 1) + (jj >> 2);
    const int tid = threadIdx.x, l = tid & 63;
    const int w = __builtin_amdgcn_readfirstlane(tid >> 6);
    const int ph = (((jj + (jj >> 2)) & 3) * 10) >> 2;
    const char* xb = (const char*)x + (size_t)r * 128 * 1280;
    const char* wb = (const char*)W + c * 80;
    u32 xo[2];
#pragma unroll
    for (int i = 0; i < 2; i++) { int row = 32 * w + 8 * i + (l >> 3); xo[i] = row * 1280 + (((l & 7) ^ ((row >> 1) & 7)) << 4); }
    u32 wo0;
    {
        int pl = l >> 4, idx = l & 15, q, sg, ch;
        if (pl < 3) { q = idx >> 2; sg = pl; ch = (idx & 3) + ((idx >> 1) & 1); }
        else { q = (idx * 11) >> 5; sg = idx - 3 * q; ch = 2; q &= 3; }
        wo0 = (8 * w + q) * 3840 + sg * 1280 + ch * 16;
    }
    const u32 wo1 = wo0 + 4 * 3840;
    const u32 lx = LX + w * 4096, lwv = LW + w * 2 * WBLK;
    auto issue_x = [&](int hx) {
        int jx = hx >> 1, hh = hx & 1, jr = jx + ph; if (jr >= 10) jr -= 10;
        const char* g = xb + jr * 128 + hh * 16 * 1280;
        dma(lx + (jx % 3) * 16384 + hh * 2048, xo[0], g); dma(lx + (jx % 3) * 16384 + hh * 2048 + 1024, xo[1], g);
    };
    auto issue_w = [&](int i) {
        int ir = i + 2 * ph; if (ir >= 20) ir -= 20;
        const char* g = wb + ir * (32 * 3840);
        dma_m(lwv + (i & 3) * WSLOT, wo0, g, 0xffffffffu, 0x0fffffffu); dma_m(lwv + (i & 3) * WSLOT + WBLK, wo1, g, 0xffffffffu, 0x0fffffffu);
    };
    issue_x(0); issue_x(1); issue_w(0);
    if (w == 0) dma(LMISC, l * 16, (const char*)wphi + c * 1600);
    if (w == 1) dma_m(LMISC + 1024, l * 16, (const char*)wphi + c * 1600 + 1024, 0xffffffffu, 0xfu);
    const u64 tag = __hip_atomic_load((GAS u64*)(ws + WS_EPOCH), __ATOMIC_RELAXED, __HIP_MEMORY_SCOPE_AGENT) + 1;
    issue_x(2); issue_w(1); issue_x(3); issue_w(2); issue_x(4); issue_w(3);
    if (tid < 2) ldsw<u64>(LZERO + tid * 8, 0ull);

    const int g = l >> 4, q = (l >> 2) & 3, p = l & 3, h = l >> 5;
    u32 bA = LW + (g >> 1) * 2 * WBLK + q * 64 + (p & 1) * 32 + (g & 1) * 16 + (p >> 1) * 8;
    u32 bD = LW + (g >> 1) * 2 * WBLK + 768 + (3 * q + 2 * (g & 1) + (p >> 1)) * 16 + (p & 1) * 8;
    asm volatile("" : "+v"(bA), "+v"(bD));
    u32 aX[4];
    const int trow = 32 * w + (l & 31);
#pragma unroll
    for (int k = 0; k < 4; k++) aX[k] = LX + trow * 128 + (((2 * k + h) ^ ((trow >> 1) & 7)) << 4);
    f32x16 acc[4] = {};
    s16x4 fa[2][8]; s16x8 fb[2];
    auto rdB = [&](int s, int i, int ks) { fb[s] = ldsr<s16x8>(aX[(i & 1) * 2 + ks] + ((i >> 1) % 3) * 16384); };
    auto rdA = [&](int s, int i, int ks, int n) {
        u32 o = (i & 3) * WSLOT + ks * 4 * WBLK + (n & 1) * WBLK;
        fa[s][n] = n < 6 ? trd(bA + o + (n >> 1) * 256) : trd(bD + o);
    };
    auto mm = [&](int s, int t) { acc[t] = mfma32(cat(fa[s][2 * t], fa[s][2 * t + 1]), fb[s], acc[t]); };
    waitv<12>();
    bar();
    STAMP(1);
    rdB(0, 0, 0);
#pragma unroll
    for (int n = 0; n < 8; n++) rdA(0, 0, 0, n);
#pragma unroll
    for (int i = 0; i < 20; i++) {
        SB; mm(0, 0); SB; rdB(1, i, 1); rdA(1, i, 1, 0); rdA(1, i, 1, 1);
        SB; mm(0, 1); SB; rdA(1, i, 1, 2); rdA(1, i, 1, 3); rdA(1, i, 1, 4);
        SB; mm(0, 2); SB; rdA(1, i, 1, 5); rdA(1, i, 1, 6); rdA(1, i, 1, 7);
        SB; mm(0, 3); SB;
        if (i >= 1 && i + 3 < 20) issue_w(i + 3);
        SB; mm(1, 0); SB;
        if (i < 19) {
            if (i <= 15) waitv<8>(); else if (i == 16) waitv<6>(); else if (i == 17) waitv<2>(); else waitv<0>();
            bar();
            rdB(0, i + 1, 0); rdA(0, i + 1, 0, 0); rdA(0, i + 1, 0, 1);
        }
        SB; mm(1, 1); SB;
        if (i < 19) { rdA(0, i + 1, 0, 2); rdA(0, i + 1, 0, 3); rdA(0, i + 1, 0, 4); }
        SB; mm(1, 2); SB;
        if (i < 19) { rdA(0, i + 1, 0, 5); rdA(0, i + 1, 0, 6); rdA(0, i + 1, 0, 7); }
        SB; mm(1, 3); SB;
        if (i + 5 < 20) issue_x(i + 5);
    }
    STAMP(2);
    const int rq = 2 * (g & 1) + (p >> 1), hk = g >> 1;
    const bool same = hk == (p & 1);
    s16x8 wf[2][3];
#pragma unroll
    for (int to = 0; to < 2; to++)
#pragma unroll
        for (int s = 0; s < 3; s++) {
            s16x4 t[2];
#pragma unroll
            for (int rd = 0; rd < 2; rd++) {
                const int mu = 2 * s + rd;
                if (mu < 5) {
                    int dq = mu + hk; if (mu == 4) dq = hk ? 0 : 4;
                    const bool ok = same && (to == 0 || rq == 0);
                    t[rd] = trd(ok ? LMISC + hk * 800 + (4 * dq + q) * 40 + (4 * to + rq) * 8 : LZERO);
                } else t[rd] = s16x4{0, 0, 0, 0};
            }
            wf[to][s] = cat(t[0], t[1]);
        }
    {
        float kr[20]; u32 phik[20];
#pragma unroll
        for (int i = 0; i < 20; i++) kr[i] = i < 16 ? acc[1][i] : acc[3][i - 12];
        features(kr, wf, phik);
        const u32 sw = ((trow & 2) << 1) | ((trow & 4) >> 1) | (trow & 1);
        const u32 rowa = LIMG + h * 16384 + trow * 128;
#pragma unroll
        for (int ch = 0; ch < 5; ch++) ldsw<u32x4>(rowa + ((ch ^ sw) << 4), u32x4{phik[4 * ch], phik[4 * ch + 1], phik[4 * ch + 2], phik[4 * ch + 3]});
#pragma unroll
        for (int mu = 0; mu < 5; mu++) {
            const int pc = 10 + (mu == 4 ? (h ? 0 : 4) : mu + h);
            u32x2 v = mu < 4 ? u32x2{pk(acc[2][4 * mu], acc[2][4 * mu + 1]), pk(acc[2][4 * mu + 2], acc[2][4 * mu + 3])}
                             : u32x2{pk(acc[3][8], acc[3][9]), pk(acc[3][10], acc[3][11])};
            ldsw<u32x2>(rowa + (((pc >> 1) ^ sw) << 4) + (pc & 1) * 8, v);
        }
        ldsw<u32x2>(rowa + ((7 ^ sw) << 4) + 8, u32x2{0x3f80u, 0u});
    }
    bar();
    const auto rsw = __builtin_amdgcn_make_buffer_rsrc((void*)ws, 0, 8u << 20, 0x00020000);
    {
        const int pcn = 4 * (g & 1) + p;
        const int pa = 10 + (pcn < 6 ? pcn : 5), pb = 5 * (w & 1) + (pcn < 5 ? pcn : 4);
        u32 aa[2], ab[2];
#pragma unroll
        for (int rd = 0; rd < 2; rd++) {
            const int row = 8 * (g >> 1) + 4 * rd + q;
            const u32 sw = ((row & 2) << 1) | ((row & 4) >> 1) | (row & 1);
            const u32 base = LIMG + (w >> 1) * 16384 + row * 128;
            aa[rd] = base + (((pa >> 1) ^ sw) << 4) + (pa & 1) * 8;
            ab[rd] = base + (((pb >> 1) ^ sw) << 4) + (pb & 1) * 8;
        }
        f32x16 kv = {};
#pragma unroll
        for (int ks = 0; ks < 8; ks++)
            kv = mfma32(cat(trd(aa[0] + ks * 2048), trd(aa[1] + ks * 2048)), cat(trd(ab[0] + ks * 2048), trd(ab[1] + ks * 2048)), kv);
        if ((l & 31) < 20) {
            const u32 lam = h * 20 + (l & 31), o = WS_PART + (c * 16 + r) * 4096 + w * 960;
            __builtin_amdgcn_raw_buffer_store_b128(u32x4{pk(kv[0], kv[1]), pk(kv[2], kv[3]), pk(kv[4], kv[5]), pk(kv[6], kv[7])}, rsw, lam * 16, o, 16);
            __builtin_amdgcn_raw_buffer_store_b64(u32x2{pk(kv[8], kv[9]), pk(kv[10], kv[11])}, rsw, 640 + lam * 8, o, 16);
        }
    }
    waitv<0>();
    bar();
    if (tid == 0) __hip_atomic_store((GAS u64*)(ws + WS_F1 + (c * 16 + r) * 128), tag, __ATOMIC_RELAXED, __HIP_MEMORY_SCOPE_AGENT);
    STAMP(3);
    u32 woo[7];
#pragma unroll
    for (int i = 0; i < 7; i++) { u32 idx = 64 * i + l, row = (idx * 6554u) >> 16; woo[i] = (40 * w + row) * 1280 + (idx - row * 10) * 16; }
    const char* wob = (const char*)wo + (c >> 1) * 160;
    auto issue_wo = [&](int G) {
        const u32 d = (G % 3) * WOSLOT + w * 6400; const char* gp = wob + G * 160 * 1280;
#pragma unroll
        for (int i = 0; i < 6; i++) dma(d + i * 1024, woo[i], gp);
        dma_m(d + 6144, woo[6], gp, 0xffffu, 0u);
    };
    issue_wo(0); issue_wo(1);
    u32 phiq[20];
    {
        float qr[20];
#pragma unroll
        for (int i = 0; i < 20; i++) qr[i] = i < 16 ? acc[0][i] : acc[3][i - 16];
        features(qr, wf, phiq);
#pragma unroll
        for (int i = 0; i < 20; i++) asm volatile("" : "+v"(phiq[i]));
    }
    if (tid < 80) ldsw<u64>(LTAB + tid * TROW + 48, 0ull);
    STAMP(4);
    if (w == 0) wait_flags(ws + WS_F1 + c * 16 * 128, tag, l);
    bar();
    STAMP(5);
    {
        const int t = tid < 240 ? tid : 239;
        const int un = (t * 1093) >> 16, it = t - un * 60;
        u32x4 pv[16];
#pragma unroll
        for (int i = 0; i < 16; i++) pv[i] = __builtin_amdgcn_raw_buffer_load_b128(rsw, t * 16 + i * 4096, WS_PART + c * 65536, 16);
        float s[8] = {};
#pragma unroll
        for (int i = 0; i < 16; i++)
#pragma unroll
            for (int e = 0; e < 4; e++) { s[2 * e] += lo2f(pv[i][e]); s[2 * e + 1] += hi2f(pv[i][e]); }
        const int lam = it < 40 ? it : 2 * (it - 40);
        const int hh = lam >= 20, f = lam - 20 * hh;
        const u32 a0 = LTAB + (un >> 1) * THEAD + ((un & 1) * 20 + f) * TROW + hh * 8 + (it < 40 ? 0 : 32);
        ldsw<u32x2>(a0, u32x2{pk(s[0], s[1]), pk(s[2], s[3])});
        ldsw<u32x2>(a0 + (it < 40 ? 16 : TROW), u32x2{pk(s[4], s[5]), pk(s[6], s[7])});
    }
    bar();
    STAMP(6);
    {
        const u32 tb = LTAB + hk * THEAD + q * TROW;
        const u32 t0 = tb + (same ? rq : 6) * 8, t1 = tb + (same && rq < 2 ? 4 + rq : 6) * 8;
        f32x16 o0 = {}, o1 = {};
#pragma unroll
        for (int s = 0; s < 5; s++) {
            const s16x8 B = frag(phiq[4 * s], phiq[4 * s + 1], phiq[4 * s + 2], phiq[4 * s + 3]);
            o0 = mfma32(cat(trd(t0 + 8 * s * TROW), trd(t0 + (8 * s + 4) * TROW)), B, o0);
            o1 = mfma32(cat(trd(t1 + 8 * s * TROW), trd(t1 + (8 * s + 4) * TROW)), B, o1);
        }
        const float den = lo2f(pk(lo2f(pk(o1[4], 0.f)) + 1e-6f, 0.f));
        const float rden = __builtin_amdgcn_rcpf(den);
        u32 ob[10];
#pragma unroll
        for (int i = 0; i < 10; i++) {
            const u32 t = i < 8 ? pk(o0[2 * i], o0[2 * i + 1]) : pk(o1[2 * i - 16], o1[2 * i - 15]);
            ob[i] = pk(lo2f(t) * rden, hi2f(t) * rden);
        }
        const u32 o = WS_O + (r * 16 + c) * 10240 + w * 2560;
        __builtin_amdgcn_raw_buffer_store_b128(u32x4{ob[0], ob[1], ob[2], ob[3]}, rsw, l * 16, o, 16);
        __builtin_amdgcn_raw_buffer_store_b128(u32x4{ob[4], ob[5], ob[6], ob[7]}, rsw, l * 16 + 1024, o, 16);
        __builtin_amdgcn_raw_buffer_store_b64(u32x2{ob[8], ob[9]}, rsw, l * 8 + 2048, o, 16);
    }
    waitv<0>();
    bar();
    if (tid == 0) __hip_atomic_store((GAS u64*)(ws + WS_F2 + (r * 16 + c) * 128), tag, __ATOMIC_RELAXED, __HIP_MEMORY_SCOPE_AGENT);
    STAMP(7);
    issue_wo(2);
    if (w == 0) wait_flags(ws + WS_F2 + r * 16 * 128, tag, l);
    waitv<0>();
    bar();
    STAMP(8);
    if (bid == 0 && tid == 0) __hip_atomic_store((GAS u64*)(ws + WS_EPOCH), tag, __ATOMIC_RELAXED, __HIP_MEMORY_SCOPE_AGENT);
    {
        const int th = c & 1, cb = c >> 1, hp = g & 1, P = g >> 1;
        const int ti = 16 * (w & 1) + (l & 15);
        const u32 ob = WS_O + r * 16 * 10240 + (2 * th + (w >> 1)) * 2560;
        const u32 v1 = P * 1024 + (hp * 32 + ti) * 16, v2 = 2048 + 2 * P * 10240 + (hp * 32 + ti) * 8;
        u32x4 of[16]; u32x2 ot[8];
#pragma unroll
        for (int G = 0; G < 4; G++) {
#pragma unroll
            for (int i = 0; i < 4; i++) of[4 * G + i] = __builtin_amdgcn_raw_buffer_load_b128(rsw, v1 + (4 * G + i) * 10240, ob, 16);
#pragma unroll
            for (int i = 0; i < 2; i++) ot[2 * G + i] = __builtin_amdgcn_raw_buffer_load_b64(rsw, v2 + (4 * G + i) * 10240, ob, 16);
        }
        u32 a1 = (20 * hp + 8 * P + q) * 160 + p * 8, a2 = (80 * P + 20 * hp + 16 + q) * 160 + p * 8;
        f32x4 oc[5] = {};
#pragma unroll
        for (int G = 0; G < 4; G++) {
            const u32 sl = (G % 3) * WOSLOT;
            u32 b1 = a1 + sl, b2 = a2 + sl;
            asm volatile("" : "+v"(b1), "+v"(b2));
            if (G == 3) { waitv<0>(); bar(); }
#pragma unroll
            for (int i = 0; i < 4; i++) {
                const s16x8 B = __builtin_bit_cast(s16x8, of[4 * G + i]);
#pragma unroll
                for (int t = 0; t < 5; t++) oc[t] = mfma16(cat(trd(b1 + 40 * i * 160 + 32 * t), trd(b1 + (40 * i + 4) * 160 + 32 * t)), B, oc[t]);
            }
            const s16x8 B = frag(ot[2 * G][0], ot[2 * G][1], ot[2 * G + 1][0], ot[2 * G + 1][1]);
#pragma unroll
            for (int t = 0; t < 5; t++) oc[t] = mfma16(cat(trd(b2 + 32 * t), trd(b2 + 40 * 160 + 32 * t)), B, oc[t]);
            if (G == 0) { bar(); issue_wo(3); }
        }
        u16* op = out + (size_t)(128 * r + 64 * th + 16 * w + (l & 15)) * 1280 + 640 + 80 * cb + 4 * g;
#pragma unroll
        for (int t = 0; t < 5; t++) *(u32x2*)(op + 16 * t) = u32x2{pk(oc[t][0], oc[t][1]), pk(oc[t][2], oc[t][3])};
    }
    STAMP(9);
}

DI f32x2 cmul(f32x2 a, f32x2 b) { const f32x2 t = a.xx * b; const f32x2 s = {-b.y, b.x}; return __builtin_elementwise_fma(a.yy, s, t); }
DI f32x2 csq(f32x2 a) { return f32x2{__builtin_fmaf(a.x, a.x, -a.y * a.y), 2.f * a.x * a.y}; }
DI f32x2 cexp(float a, float b) {
    const float m = __builtin_amdgcn_exp2f(a * 1.4426950408889634f);
    const float c1 = 0.15915494309189535f, c2 = 6.4206382e-9f;
    const float n = __builtin_rintf(b * c1);
    const float f = __builtin_fmaf(b, c2, __builtin_fmaf(b, c1, -n));
    return f32x2{m * __builtin_amdgcn_cosf(f), m * __builtin_amdgcn_sinf(f)};
}
constexpr int LU = 65536;
DI u32 kaddr(u32 e) { return ((e >> 5) << 8) + ((e & 31) << 1); }

DI void path_a(const u16* __restrict__ u, const u16* __restrict__ Are, const u16* __restrict__ Aim, const u16* __restrict__ Bre,
               const u16* __restrict__ Bim, const u16* __restrict__ Cre, const u16* __restrict__ Cim, const u16* __restrict__ logdt,
               const u16* __restrict__ Dskip, u16* __restrict__ out, char* __restrict__ ws STAMP_ARG) {
    const int ia = blockIdx.x - 256, mg = ia >> 3, d0 = 80 * (ia & 7) + 4 * mg;
    const int tid = threadIdx.x, l = tid & 63;
    const int w = __builtin_amdgcn_readfirstlane(tid >> 6);
    const int d = d0 + w;
    const u32 KB = w * 16384;
    u16 prm[2][6];
#pragma unroll
    for (int ps = 0; ps < 2; ps++) {
        const int i = d * 128 + ps * 64 + l;
        prm[ps][0] = Are[i]; prm[ps][1] = Aim[i]; prm[ps][2] = Bre[i]; prm[ps][3] = Bim[i]; prm[ps][4] = Cre[i]; prm[ps][5] = Cim[i];
    }
    const float dt = lo2f(pk(expf(b2f(logdt[d])), 0.f));
    const float dsk = b2f(Dskip[d]);
    const int g = l >> 4, q = (l >> 2) & 3, p = l & 3, h = l >> 5;
    f32x16 kd = {};
    {
        u32 ra[2];
#pragma unroll
        for (int rd = 0; rd < 2; rd++) ra[rd] = KB + (8 * (g >> 1) + 4 * rd + q) * 64 + (((2 * (g & 1) + (p >> 1)) ^ ((2 * rd + (q >> 1)) & 3)) << 4) + (p & 1) * 8;
        const u32 wa = KB + l * 64, wsw = (l >> 1) & 3;
#pragma unroll 1
        for (int ps = 0; ps < 2; ps++) {
            const u16* pr = ps ? prm[1] : prm[0];
            const float a = dt * b2f(ps ? prm[1][0] : prm[0][0]), b = dt * b2f(ps ? prm[1][1] : prm[0][1]);
            const f32x2 dB = {dt * b2f(ps ? prm[1][2] : prm[0][2]), dt * b2f(ps ? prm[1][3] : prm[0][3])};
            const f32x2 Wc = cmul(f32x2{b2f(ps ? prm[1][4] : prm[0][4]), b2f(ps ? prm[1][5] : prm[0][5])}, dB);
            (void)pr;
            const f32x2 e1 = cexp(a, b), e16 = cexp(16.f * a, 16.f * b), e256 = cexp(256.f * a, 256.f * b);
            const f32x2 e2 = csq(e1), e4 = csq(e2), e8 = csq(e4), e32 = csq(e16), e64 = csq(e32), e128 = csq(e64), e512 = csq(e256), e1024 = csq(e512);
            f32x2 X[32];
            X[0] = Wc; X[1] = cmul(Wc, e1); X[2] = cmul(Wc, e2); X[3] = cmul(X[1], e2);
#pragma unroll
            for (int i = 0; i < 4; i++) X[4 + i] = cmul(X[i], e4);
            X[8] = cmul(Wc, e256); X[9] = cmul(X[8], e2); X[10] = cmul(X[8], e4);
            X[12] = X[10]; X[13] = cmul(X[9], e4); X[14] = cmul(X[8], e8);
            X[11] = cmul(Wc, e512); X[16] = cmul(X[11], e4); X[17] = cmul(X[11], e8);
            X[15] = cmul(X[8], e512); X[20] = cmul(X[15], e4); X[21] = cmul(X[15], e8);
            X[18] = cmul(Wc, e1024); X[19] = cmul(X[18], e8);
            X[24] = cmul(X[8], e1024); X[25] = cmul(X[24], e8);
            X[22] = cmul(X[11], e1024); X[23] = cmul(X[22], e8);
            X[28] = cmul(X[15], e1024); X[29] = cmul(X[28], e8);
            X[26] = X[27] = X[30] = X[31] = f32x2{0.f, 0.f};
#pragma unroll
            for (int ch = 0; ch < 4; ch++) {
                const u32 o = (ch ^ wsw) << 4;
                ldsw<u32x4>(wa + o, u32x4{pk(X[8 * ch].x, X[8 * ch + 1].x), pk(X[8 * ch + 2].x, X[8 * ch + 3].x), pk(X[8 * ch + 4].x, X[8 * ch + 5].x), pk(X[8 * ch + 6].x, X[8 * ch + 7].x)});
                ldsw<u32x4>(wa + o + 4096, u32x4{pk(X[8 * ch].y, X[8 * ch + 1].y), pk(X[8 * ch + 2].y, X[8 * ch + 3].y), pk(X[8 * ch + 4].y, X[8 * ch + 5].y), pk(X[8 * ch + 6].y, X[8 * ch + 7].y)} ^ 0x80008000u);
            }
            f32x2 Y[8];
            Y[0] = f32x2{1.f, 0.f}; Y[1] = e8; Y[2] = e16; Y[3] = cmul(e8, e16); Y[4] = e32; Y[5] = cmul(e8, e32); Y[6] = cmul(e16, e32); Y[7] = cmul(Y[3], e32);
            const f32x2 e192 = cmul(e64, e128);
#pragma unroll
            for (int ch = 0; ch < 4; ch++) {
                f32x2 Z[8];
                const f32x2 m = ch == 1 ? e64 : ch == 2 ? e128 : e192;
#pragma unroll
                for (int i = 0; i < 8; i++) Z[i] = ch == 0 ? Y[i] : i == 0 ? m : cmul(Y[i], m);
                const u32 o = (ch ^ wsw) << 4;
                ldsw<u32x4>(wa + o + 8192, u32x4{pk(Z[0].x, Z[1].x), pk(Z[2].x, Z[3].x), pk(Z[4].x, Z[5].x), pk(Z[6].x, Z[7].x)});
                ldsw<u32x4>(wa + o + 12288, u32x4{pk(Z[0].y, Z[1].y), pk(Z[2].y, Z[3].y), pk(Z[4].y, Z[5].y), pk(Z[6].y, Z[7].y)});
            }
#pragma unroll
            for (int ks = 0; ks < 8; ks++)
                kd = mfma32(cat(trd(ra[0] + ks * 1024), trd(ra[1] + ks * 1024)), cat(trd(ra[0] + ks * 1024 + 8192), trd(ra[1] + ks * 1024 + 8192)), kd);
        }
    }
    STAMP(1);
    u32x2 uv[8];
    const u32 urow = (512 * w + l + 128 * mg) & 2047;
    (void)urow;
#pragma unroll
    for (int n = 0; n < 8; n++) uv[n] = *(const u32x2*)(u + (size_t)((512 * w + 64 * n + l + 128 * mg) & 2047) * 640 + d0);
    {
        const int j = l & 31;
        ldsw<u32x2>(KB + kaddr(2044 - 8 * j - 4 * h), u32x2{pk(kd[3], kd[2]), pk(kd[1], kd[0])});
        ldsw<u32x2>(KB + kaddr(2044 - 256 - 8 * j - 4 * h), u32x2{pk(kd[6], kd[5]), pk(kd[4], kd[4])});
        ldsw<u32x4>(KB + kaddr(2040 - 512 - 256 * h - 8 * j), u32x4{pk(kd[9], kd[9]), pk(kd[8], kd[8]), pk(kd[8], kd[7]), pk(kd[7], kd[7])});
#pragma unroll
        for (int sp = 0; sp < 2; sp++) {
            const float a = kd[10 + 2 * sp], b = kd[11 + 2 * sp];
            const u32 aa = pk(a, a), bb = pk(b, b);
            ldsw<u32x4>(KB + kaddr(2040 - 1024 - 512 * h - 256 * sp - 8 * j), u32x4{bb, (j & 1) ? bb : pk(b, a), aa, aa});
        }
#pragma unroll
        for (int it = 0; it < 4; it++) {
            const u32 i = it * 64 + l, a0 = KB + kaddr(8 * i);
            const u32x4 v = ldsr<u32x4>(a0);
            const u32x2 n = ldsr<u32x2>(KB + kaddr(8 * i + 8));
            const u32 s0 = __builtin_amdgcn_alignbit(v[1], v[0], 16), s1 = __builtin_amdgcn_alignbit(v[2], v[1], 16), s2 = __builtin_amdgcn_alignbit(v[3], v[2], 16),
                      s3 = __builtin_amdgcn_alignbit(n[0], v[3], 16), s4 = __builtin_amdgcn_alignbit(n[1], n[0], 16);
            ldsw<u32x4>(a0 + 64, u32x4{s0, s1, s2, s3});
            ldsw<u32x4>(a0 + 128, u32x4{v[1], v[2], v[3], n[0]});
            ldsw<u32x4>(a0 + 192, u32x4{s1, s2, s3, s4});
        }
    }
    STAMP(8);
#pragma unroll
    for (int n = 0; n < 8; n++) {
        const u32 a = LU + ((512 * w + 64 * n + l + 128 * mg) & 2047) * 2;
        ldsw<u16>(a, (u16)uv[n][0]); ldsw<u16>(a + 4096, (u16)(uv[n][0] >> 16));
        ldsw<u16>(a + 8192, (u16)uv[n][1]); ldsw<u16>(a + 12288, (u16)(uv[n][1] >> 16));
    }
    bar();
    STAMP(2);
    f32x4 y[8] = {};
    const int rr = l & 15;
    {
        const u32 e0 = 2047 - 16 - rr + 8 * g, sh = e0 & 3;
        u32 ka0 = KB + kaddr(e0 - sh) + sh * 64 - 7 * 2048, ka1 = KB + kaddr(e0 - sh + 4) + sh * 64 - 7 * 2048;
        u32 ua = LU + w * 4096 + 32 * rr + 16 * g - 32;
        u32 mk[4];
#pragma unroll
        for (int i = 0; i < 4; i++) mk[i] = (2 * i > 16 + rr - 8 * g ? 0u : 0xffffu) | (2 * i + 1 > 16 + rr - 8 * g ? 0u : 0xffff0000u);
        int lim = 2 * rr + g - 2;
#pragma unroll 1
        for (int gg = 0; gg < 8; gg++) {
            u32x4 A[8], B[8];
#pragma unroll
            for (int m = 0; m < 8; m++) {
                const u32x2 t0 = ldsr<u32x2>(ka0 + (7 - m) * 2048), t1 = ldsr<u32x2>(ka1 + (7 - m) * 2048);
                A[m] = u32x4{t0[0], t0[1], t1[0], t1[1]};
                B[m] = ldsr<u32x4>(ua + m * 512);
            }
            A[0] &= u32x4{mk[0], mk[1], mk[2], mk[3]};
            if (lim < 0) B[0] = u32x4{0, 0, 0, 0};
#pragma unroll
            for (int m = 7; m >= 0; m--)
#pragma unroll
                for (int ct = 7 - m; ct >= 0; ct--) y[ct + m] = mfma16(__builtin_bit_cast(s16x8, A[m]), __builtin_bit_cast(s16x8, B[ct]), y[ct + m]);
            ka0 -= 256; ka1 -= 256; ua -= 64; lim -= 4;
            mk[0] = mk[1] = mk[2] = mk[3] = 0xffffffffu;
        }
    }
    STAMP(3);
#pragma unroll
    for (int ct = 0; ct < 8; ct++) {
        const u32 a = LU + w * 4096 + ct * 512 + rr * 32 + g * 8;
        const u32x2 uu = ldsr<u32x2>(a);
        const u32 y0 = pk(y[ct][0], y[ct][1]), y1 = pk(y[ct][2], y[ct][3]);
        const u32 s0 = pk(lo2f(uu[0]) * dsk, hi2f(uu[0]) * dsk), s1 = pk(lo2f(uu[1]) * dsk, hi2f(uu[1]) * dsk);
        ldsw<u32x2>(a, u32x2{pk(lo2f(y0) + lo2f(s0), hi2f(y0) + hi2f(s0)), pk(lo2f(y1) + lo2f(s1), hi2f(y1) + hi2f(s1))});
    }
    bar();
    {
        u32x4 c[4];
#pragma unroll
        for (int i = 0; i < 4; i++) c[i] = ldsr<u32x4>(LU + i * 4096 + tid * 16);
        u16* op = out + (size_t)tid * 8 * 1280 + d0;
#pragma unroll
        for (int i = 0; i < 4; i++) {
            *(u32x2*)(op + (2 * i) * 1280) = u32x2{__builtin_amdgcn_perm(c[1][i], c[0][i], 0x05040100u), __builtin_amdgcn_perm(c[3][i], c[2][i], 0x05040100u)};
            *(u32x2*)(op + (2 * i + 1) * 1280) = u32x2{__builtin_amdgcn_perm(c[1][i], c[0][i], 0x07060302u), __builtin_amdgcn_perm(c[3][i], c[2][i], 0x07060302u)};
        }
    }
    STAMP(4);
}

#if defined(__HIP_DEVICE_COMPILE__)
__attribute__((target("no-packed-fp32-ops")))
#endif
__global__ void __launch_bounds__(256, 2)
fused_kernel(const u16* __restrict__ x, const u16* __restrict__ W, const u16* __restrict__ wphi, const u16* __restrict__ wo,
             u16* __restrict__ out, char* __restrict__ ws, const u16* __restrict__ u, const u16* __restrict__ Are,
             const u16* __restrict__ Aim, const u16* __restrict__ Bre, const u16* __restrict__ Bim, const u16* __restrict__ Cre,
             const u16* __restrict__ Cim, const u16* __restrict__ logdt, const u16* __restrict__ Dskip) {
    __shared__ __attribute__((aligned(16))) char smem[LDS_BYTES];
    asm volatile("" ::"s"((u32)(size_t)(LAS char*)smem));
    STAMP(0);
    u32 pfd = 0;
    {
        u64 pc; asm volatile("s_getpc_b64 %0" : "=s"(pc));
        asm volatile("global_load_dword %0, %1, %2" : "+v"(pfd) : "v"(min(threadIdx.x * 128, CODE_PF_BYTES)), "s"(pc));
    }
    if (__builtin_expect(blockIdx.x < 256, 1)) {
        __builtin_amdgcn_s_setprio(3);
        path_b(x, W, wphi, wo, out, ws STAMP_PASS);
    } else {
        path_a(u, Are, Aim, Bre, Bim, Cre, Cim, logdt, Dskip, out, ws STAMP_PASS);
    }
    asm volatile("" ::"v"(pfd));
}

extern "C" void kernel_launch(void* const* d_in, const int* in_sizes, int n_in, void* d_out, int out_size, void* d_ws, size_t ws_size,
                              hipStream_t stream) {
    const u16* const* in = (const u16* const*)d_in;
    fused_kernel<<<416, 256, 0, stream>>>(in[9], in[10], in[11], in[12], (u16*)d_out, (char*)d_ws, in[0], in[1], in[2], in[3], in[4], in[5],
                                          in[6], in[7], in[8]);
}
```

```cpp
#include <hip/hip_runtime.h>
#include <hip/hip_bf16.h>

typedef unsigned short u16;
typedef unsigned u32;
typedef unsigned long long u64;
typedef __attribute__((ext_vector_type(8))) short s16x8;
typedef __attribute__((ext_vector_type(4))) short s16x4;
typedef __attribute__((ext_vector_type(16))) float f32x16;
typedef __attribute__((ext_vector_type(4))) float f32x4;
typedef __attribute__((ext_vector_type(2))) float f32x2;
typedef __attribute__((ext_vector_type(4))) u32 u32x4;
typedef __attribute__((ext_vector_type(2))) u32 u32x2;
typedef __attribute__((ext_vector_type(2))) __bf16 bf16x2;
#define LAS __attribute__((address_space(3)))
#define GAS __attribute__((address_space(1)))
#define DI __device__ __forceinline__
#define SB __builtin_amdgcn_sched_barrier(0)

#define HSLEEP 16
#define APRIO 0
constexpr int LDS_BYTES = 81920;
constexpr u32 WS_EPOCH = 0, WS_EPOCH_A = 4096, WS_F1 = 65536, WS_F2 = 131072, WS_F3 = 196608, WS_PART = 524288, WS_O = 2097152,
              WS_UT = 5u << 20, WS_F0 = 262144, WS_HINT = 327680, WS_WP = 9u << 20, WS_END = 12u << 20;
constexpr u32 WS_STAMP = 16u << 20;
constexpr u32 kSpinCap = 1u << 18;
constexpr u32 CODE_PF_BYTES = 30720;

DI u32 pk(float a, float b) { f32x2 v = {a, b}; return __builtin_bit_cast(u32, __builtin_convertvector(v, bf16x2)); }
DI float lo2f(u32 x) { return __uint_as_float(x << 16); }
DI float hi2f(u32 x) { return __uint_as_float(x & 0xffff0000u); }
DI float b2f(u16 h) { return __uint_as_float((u32)h << 16); }
DI s16x4 trd(u32 a) { return __builtin_amdgcn_ds_read_tr16_b64_v4i16((LAS s16x4*)(size_t)a); }
DI s16x8 cat(s16x4 a, s16x4 b) { return __builtin_shufflevector(a, b, 0, 1, 2, 3, 4, 5, 6, 7); }
DI s16x8 frag(u32 a, u32 b, u32 c, u32 d) { u32x4 v = {a, b, c, d}; return __builtin_bit_cast(s16x8, v); }
template <class T> DI T ldsr(u32 a) { return *(LAS T*)(size_t)a; }
template <class T> DI void ldsw(u32 a, T v) { *(LAS T*)(size_t)a = v; }
template <int N> DI void waitv() { asm volatile("s_waitcnt vmcnt(%0)" ::"i"(N) : "memory"); }
DI void waitl0() { asm volatile("s_waitcnt lgkmcnt(0)" ::: "memory"); }
DI void bar() { asm volatile("s_waitcnt lgkmcnt(0)\n\ts_barrier" ::: "memory"); }
DI f32x16 mfma32(s16x8 a, s16x8 b, f32x16 c) { return __builtin_amdgcn_mfma_f32_32x32x16_bf16(a, b, c, 0, 0, 0); }
DI f32x4 mfma16(s16x8 a, s16x8 b, f32x4 c) { return __builtin_amdgcn_mfma_f32_16x16x32_bf16(a, b, c, 0, 0, 0); }
DI float max3(float a, float b, float c) { float r; asm("v_max3_f32 %0, %1, %2, %3" : "=v"(r) : "v"(a), "v"(b), "v"(c)); return r; }
DI float min3(float a, float b, float c) { float r; asm("v_min3_f32 %0, %1, %2, %3" : "=v"(r) : "v"(a), "v"(b), "v"(c)); return r; }
DI float dot2(u32 x, u32 sel, float c) { return __builtin_amdgcn_fdot2_f32_bf16(__builtin_bit_cast(bf16x2, sel), __builtin_bit_cast(bf16x2, x), c, false); }
DI void dma(u32 m0v, u32 voff, const void* sbase) {
    asm volatile("s_mov_b32 m0, %0\n\ts_nop 0\n\tglobal_load_lds_dwordx4 %1, %2" ::"s"(m0v), "v"(voff), "s"(sbase) : "memory");
}
DI void dma_m(u32 m0v, u32 voff, const void* sbase, u32 mlo, u32 mhi) {
    asm volatile("s_mov_b32 exec_lo, %3\n\ts_mov_b32 exec_hi, %4\n\ts_mov_b32 m0, %0\n\ts_nop 0\n\tglobal_load_lds_dwordx4 %1, %2\n\ts_mov_b64 exec, -1"
                 ::"s"(m0v), "v"(voff), "s"(sbase), "i"(mlo), "i"(mhi) : "memory");
}
DI void dma4(u32 m0v, u32 voff, const void* sbase) {
    asm volatile("s_mov_b32 m0, %0\n\ts_nop 0\n\tglobal_load_lds_dword %1, %2" ::"s"(m0v), "v"(voff), "s"(sbase) : "memory");
}
#define WSC " sc1"
DI void dma_s(u32 m0v, u32 voff, const void* sbase) {
    asm volatile("s_mov_b32 m0, %0\n\ts_nop 0\n\tglobal_load_lds_dwordx4 %1, %2" WSC ::"s"(m0v), "v"(voff), "s"(sbase) : "memory");
}
DI void dma_sm(u32 m0v, u32 voff, const void* sbase, u32 mhi) {
    asm volatile("s_mov_b32 exec_hi, %3\n\ts_mov_b32 m0, %0\n\ts_nop 0\n\tglobal_load_lds_dwordx4 %1, %2 offset:1024" WSC "\n\ts_mov_b32 exec_hi, -1"
                 ::"s"(m0v), "v"(voff), "s"(sbase), "i"(mhi) : "memory");
}
DI void mfma16i(f32x4& c, s16x8 a, s16x8 b) { c = mfma16(a, b, c); }
#define STAMP(i)
#define STAMP_ARG
#define STAMP_PASS

DI void post_flag(char* fl, u64 tag, int tid, int n, int me) {
    if (tid < n) __hip_atomic_store((GAS u64*)(fl + tid * 256 + me * 8), tag, __ATOMIC_RELAXED, __HIP_MEMORY_SCOPE_AGENT);
}
DI void wait_flags(const char* fl, u64 tag, int l, int n) {
    const GAS u64* p = (const GAS u64*)(fl + (l < n ? l : 0) * 8);
#pragma unroll 1
    for (u32 s = 0; s < kSpinCap; s++) {
        u64 v = __hip_atomic_load(p, __ATOMIC_RELAXED, __HIP_MEMORY_SCOPE_AGENT);
        if (__all(v == tag)) break;
        __builtin_amdgcn_s_sleep(1);
    }
}

DI void features(const float (&v)[20], const s16x8 (&wf)[2][3], u32 (&phi)[20]) {
    u32 b[10];
#pragma unroll
    for (int i = 0; i < 10; i++) b[i] = pk(v[2 * i], v[2 * i + 1]);
    const s16x8 B0 = frag(b[0], b[1], b[2], b[3]), B1 = frag(b[4], b[5], b[6], b[7]), B2 = frag(b[8], b[9], 0, 0);
    f32x16 p0 = {}, p1 = {};
    p0 = mfma32(wf[0][0], B0, p0); p1 = mfma32(wf[1][0], B0, p1);
    p0 = mfma32(wf[0][1], B1, p0); p1 = mfma32(wf[1][1], B1, p1);
    p0 = mfma32(wf[0][2], B2, p0); p1 = mfma32(wf[1][2], B2, p1);
    asm volatile("s_nop 15" : "+v"(p0), "+v"(p1));
    float p[20];
#pragma unroll
    for (int i = 0; i < 20; i++) p[i] = i < 16 ? p0[i] : p1[i - 16];
    float mx = max3(p[0], p[1], p[2]), mn = min3(p[0], p[1], p[2]);
#pragma unroll
    for (int i = 3; i < 19; i += 2) { mx = max3(mx, p[i], p[i + 1]); mn = min3(mn, p[i], p[i + 1]); }
    mx = max3(mx, p[19], p[19]); mn = min3(mn, p[19], p[19]);
    const float L2E = 1.4426950408889634f;
    const float cx = -mx * L2E, cn = mn * L2E;
    float e[40], s1 = 0.f, s2 = 0.f;
#pragma unroll
    for (int i = 0; i < 20; i++) {
        e[i] = __builtin_amdgcn_exp2f(__builtin_fmaf(p[i], L2E, cx));
        e[20 + i] = __builtin_amdgcn_exp2f(__builtin_fmaf(p[i], -L2E, cn));
        s1 += e[i]; s2 += e[20 + i];
    }
    const float r1 = __builtin_amdgcn_rcpf(s1), r2 = __builtin_amdgcn_rcpf(s2);
#pragma unroll
    for (int i = 0; i < 10; i++) { phi[i] = pk(e[2 * i] * r1, e[2 * i + 1] * r1); phi[10 + i] = pk(e[20 + 2 * i] * r2, e[21 + 2 * i] * r2); }
}

constexpr int LX = 0, LW = 49152, WBLK = 960, WSLOT = 8 * WBLK, LMISC = 79872, LZERO = LMISC + 1600;
constexpr int LIMG = 16384, LTAB = 76800, TROW = 56, THEAD = 40 * TROW, WOSLOT = 25600;

DI void prologue_b(const u16* __restrict__ W) {
    const int bid = blockIdx.x, xcd = bid & 7, jj = bid >> 3;
    const int r = 8 * (xcd >> 2) + (jj & 7), c = 4 * (xcd & 3) + (jj >> 3);
    const int tid = threadIdx.x, l = tid & 63, w = __builtin_amdgcn_readfirstlane(tid >> 6);
    const int pl = l >> 4, idx = l & 15, q3 = (idx * 11) >> 5;
    const int q = pl < 3 ? idx >> 2 : q3 & 3, sg = pl < 3 ? pl : idx - 3 * q3, ch = pl < 3 ? (idx & 3) + ((idx >> 1) & 1) : 2;
    const u32 wl = q * 3840 + sg * 1280 + ch * 16;
    const char* src = (const char*)W + ((c & ~1) + (w & 1)) * 80 + (20 * (2 * r + (c & 1)) + 4 * (w >> 1)) * 3840;
    dma(LW + w * 3072, wl, src); dma(LW + w * 3072 + 1024, wl, src + 8 * 3840);
    if (w < 2) dma(LW + w * 3072 + 2048, wl, src + 16 * 3840);
}

DI void path_b(const u16* __restrict__ x, const u16* __restrict__ W, const u16* __restrict__ wphi, const u16* __restrict__ wo,
               u16* __restrict__ out, char* __restrict__ ws, u32& pfd STAMP_ARG) {
    const int bid = blockIdx.x, xcd = bid & 7, jj = bid >> 3;
    const int r = 8 * (xcd >> 2) + (jj & 7), c = 4 * (xcd & 3) + (jj >> 3);
    const int r2 = 2 * xcd + (jj >> 4), c2 = jj & 15;
    const int tid = threadIdx.x, l = tid & 63;
    const int w = __builtin_amdgcn_readfirstlane(tid >> 6);
#define ROTG 4
    const int ph = ((((jj & 7) + (jj >> 3)) % ROTG) * 10) / ROTG;
    const auto rsw = __builtin_amdgcn_make_buffer_rsrc((void*)ws, 0, WS_END, 0x00020000);
    const char* xb = (const char*)x + (size_t)r * 128 * 1280;
    const char* wb = ws + WS_WP + c * 153600 + w * 2 * WBLK;
    u32 xo[2];
#pragma unroll
    for (int i = 0; i < 2; i++) { int row = 32 * w + 8 * i + (l >> 3); xo[i] = row * 1280 + (((l & 7) ^ ((row >> 1) & 7)) << 4); }
    const u32 lx = LX + w * 4096, lwv = LW + w * 2 * WBLK, l16 = l * 16;
    auto issue_x = [&](int hx) {
        int jx = hx >> 1, hh = hx & 1, jr = jx + ph; if (jr >= 10) jr -= 10;
        const char* g = xb + jr * 128 + hh * 16 * 1280;
        dma(lx + (jx % 3) * 16384 + hh * 2048, xo[0], g); dma(lx + (jx % 3) * 16384 + hh * 2048 + 1024, xo[1], g);
    };
    auto issue_w = [&](int i) {
        int ir = i + 2 * ph; if (ir >= 20) ir -= 20;
        const char* g = wb + ir * WSLOT;
        dma_s(lwv + (i & 3) * WSLOT, l16, g); dma_sm(lwv + (i & 3) * WSLOT, l16, g, 0x00ffffffu);
    };
    STAMP(15);
        u64 tag;
    asm volatile("s_load_dwordx2 %0, %1, 0\n\ts_waitcnt vmcnt(0) lgkmcnt(0)" : "=&s"(tag) : "s"(ws + WS_EPOCH) : "memory");
    tag += 1;
    STAMP(19);
    if (w == 2) dma(LMISC, l * 16, (const char*)wphi + c * 1600);
    if (w == 3) dma_m(LMISC + 1024, l * 16, (const char*)wphi + c * 1600 + 1024, 0xffffffffu, 0xfu);
    {
        const u32 o = WS_WP + ((c & ~1) + (w & 1)) * 153600 + (5 * (2 * r + (c & 1)) + (w >> 1)) * WBLK, a = LW + w * 3072 + l16;
        const u32x4 v0 = ldsr<u32x4>(a), v1 = ldsr<u32x4>(a + 1024), v2 = ldsr<u32x4>(a + 2048);
        if (l < 60) {
            __builtin_amdgcn_raw_buffer_store_b128(v0, rsw, l16, o, 16);
            __builtin_amdgcn_raw_buffer_store_b128(v1, rsw, l16 + 2 * WBLK, o, 16);
            if (w < 2) __builtin_amdgcn_raw_buffer_store_b128(v2, rsw, l16 + 4 * WBLK, o, 16);
        }
    }
    STAMP(20);
    issue_x(0); issue_x(1); issue_x(2); issue_x(3);
    if (tid < 2) ldsw<u64>(LZERO + tid * 8, 0ull);
    STAMP(16);

    const int g = l >> 4, q = (l >> 2) & 3, p = l & 3, h = l >> 5;
    u32 bA = LW + (g >> 1) * 2 * WBLK + q * 64 + (p & 1) * 32 + (g & 1) * 16 + (p >> 1) * 8;
    u32 bD = LW + (g >> 1) * 2 * WBLK + 768 + (3 * q + 2 * (g & 1) + (p >> 1)) * 16 + (p & 1) * 8;
    asm volatile("" : "+v"(bA), "+v"(bD));
    u32 aX[4];
    const int trow = 32 * w + (l & 31);
#pragma unroll
    for (int k = 0; k < 4; k++) aX[k] = LX + trow * 128 + (((2 * k + h) ^ ((trow >> 1) & 7)) << 4);
    f32x16 acc[4] = {};
    s16x4 fa[2][8]; s16x8 fb[2];
    auto rdB = [&](int s, int i, int ks) { fb[s] = ldsr<s16x8>(aX[(i & 1) * 2 + ks] + ((i >> 1) % 3) * 16384); };
    auto rdA = [&](int s, int i, int ks, int n) {
        u32 o = (i & 3) * WSLOT + ks * 4 * WBLK + (n & 1) * WBLK;
        fa[s][n] = n < 6 ? trd(bA + o + (n >> 1) * 256) : trd(bD + o);
    };
    auto mm = [&](int s, int t) { acc[t] = mfma32(cat(fa[s][2 * t], fa[s][2 * t + 1]), fb[s], acc[t]); };
    waitv<8>();
    bar();
    post_flag(ws + WS_F0 + (c >> 1) * 8192, tag, tid, 32, 2 * r + (c & 1));
    STAMP(22);
    const int rq = 2 * (g & 1) + (p >> 1), hk = g >> 1;
    const bool same = hk == (p & 1);
    s16x8 wf[2][3];
#pragma unroll
    for (int to = 0; to < 2; to++)
#pragma unroll
        for (int s = 0; s < 3; s++) {
            s16x4 t[2];
#pragma unroll
            for (int rd = 0; rd < 2; rd++) {
                const int mu = 2 * s + rd;
                if (mu < 5) {
                    int dq = mu + hk; if (mu == 4) dq = hk ? 0 : 4;
                    const bool ok = same && (to == 0 || rq == 0);
                    t[rd] = trd(ok ? LMISC + hk * 800 + (4 * dq + q) * 40 + (4 * to + rq) * 8 : LZERO);
                } else t[rd] = s16x4{0, 0, 0, 0};
            }
            wf[to][s] = cat(t[0], t[1]);
        }
#pragma unroll
    for (int i = 0; i < 6; i++) asm volatile("" : "+v"(wf[i / 3][i % 3]));
    if (w == 0) wait_flags(ws + WS_F0 + (c >> 1) * 8192 + (2 * r + (c & 1)) * 256, tag, l, 32);
    bar();
    STAMP(14);
    issue_w(0); issue_w(1); issue_x(4); issue_w(2); issue_x(5); issue_w(3);
    waitv<10>();
    bar();
    STAMP(1);
    rdB(0, 0, 0);
#pragma unroll
    for (int n = 0; n < 8; n++) rdA(0, 0, 0, n);
#pragma unroll
    for (int i = 0; i < 20; i++) {
        SB; mm(0, 0); SB; rdB(1, i, 1); rdA(1, i, 1, 0); rdA(1, i, 1, 1);
        SB; mm(0, 1); SB; rdA(1, i, 1, 2); rdA(1, i, 1, 3); rdA(1, i, 1, 4);
        SB; mm(0, 2); SB; rdA(1, i, 1, 5); rdA(1, i, 1, 6); rdA(1, i, 1, 7);
        SB; mm(0, 3); SB;
        if (i >= 1 && i + 3 < 20) issue_w(i + 3);
        if (i == 17) {
            const u32 ln = jj * 200 + (tid < 200 ? tid : 199);
            asm volatile("global_load_dword %0, %1, %2" : "+v"(pfd) : "v"(ln * 128), "s"(wo));
        }
        SB; mm(1, 0); SB;
        if (i < 19) {
            if (i == 1 || i == 2) waitv<6>(); else if (i <= 15) waitv<8>(); else if (i == 16) waitv<6>(); else if (i == 17) waitv<3>(); else waitv<1>();
            bar();
            rdB(0, i + 1, 0); rdA(0, i + 1, 0, 0); rdA(0, i + 1, 0, 1);
        }
        SB; mm(1, 1); SB;
        if (i < 19) { rdA(0, i + 1, 0, 2); rdA(0, i + 1, 0, 3); rdA(0, i + 1, 0, 4); }
        SB; mm(1, 2); SB;
        if (i < 19) { rdA(0, i + 1, 0, 5); rdA(0, i + 1, 0, 6); rdA(0, i + 1, 0, 7); }
        SB; mm(1, 3); SB;
        if (i >= 1 && i + 5 < 20) issue_x(i + 5);
    }
    if (tid == 0) __hip_atomic_store((GAS u64*)(ws + WS_HINT + bid * 128), tag, __ATOMIC_RELAXED, __HIP_MEMORY_SCOPE_AGENT);
    STAMP(2);
    {
        float kr[20]; u32 phik[20];
#pragma unroll
        for (int i = 0; i < 20; i++) kr[i] = i < 16 ? acc[1][i] : acc[3][i - 12];
        features(kr, wf, phik);
        const u32 sw = ((trow & 2) << 1) | ((trow & 4) >> 1) | (trow & 1);
        const u32 rowa = LIMG + h * 16384 + trow * 128;
#pragma unroll
        for (int ch = 0; ch < 5; ch++) ldsw<u32x4>(rowa + ((ch ^ sw) << 4), u32x4{phik[4 * ch], phik[4 * ch + 1], phik[4 * ch + 2], phik[4 * ch + 3]});
#pragma unroll
        for (int mu = 0; mu < 5; mu++) {
            const int pc = 10 + (mu == 4 ? (h ? 0 : 4) : mu + h);
            u32x2 v = mu < 4 ? u32x2{pk(acc[2][4 * mu], acc[2][4 * mu + 1]), pk(acc[2][4 * mu + 2], acc[2][4 * mu + 3])}
                             : u32x2{pk(acc[3][8], acc[3][9]), pk(acc[3][10], acc[3][11])};
            ldsw<u32x2>(rowa + (((pc >> 1) ^ sw) << 4) + (pc & 1) * 8, v);
        }
        ldsw<u32x2>(rowa + ((7 ^ sw) << 4) + 8, u32x2{0x3f80u, 0u});
    }
    STAMP(10);
    u32 woo[7];
#pragma unroll
    for (int i = 0; i < 7; i++) { u32 idx = 64 * i + l, row = (idx * 6554u) >> 16; woo[i] = (40 * w + row) * 1280 + (idx - row * 10) * 16; }
    const char* wob = (const char*)wo + (c2 >> 1) * 160;
    auto issue_wo = [&](int G) {
        const u32 d = (G % 3) * WOSLOT + w * 6400; const char* gp = wob + G * 160 * 1280;
#pragma unroll
        for (int i = 0; i < 6; i++) dma(d + i * 1024, woo[i], gp);
        dma_m(d + 6144, woo[6], gp, 0xffffu, 0u);
    };
    bar();
    issue_wo(2);
    {
        const int pcn = 4 * (g & 1) + p;
        const int pa = 10 + (pcn < 6 ? pcn : 5), pb = 5 * (w & 1) + (pcn < 5 ? pcn : 4);
        u32 aa[2], ab[2];
#pragma unroll
        for (int rd = 0; rd < 2; rd++) {
            const int row = 8 * (g >> 1) + 4 * rd + q;
            const u32 sw = ((row & 2) << 1) | ((row & 4) >> 1) | (row & 1);
            const u32 base = LIMG + (w >> 1) * 16384 + row * 128;
            aa[rd] = base + (((pa >> 1) ^ sw) << 4) + (pa & 1) * 8;
            ab[rd] = base + (((pb >> 1) ^ sw) << 4) + (pb & 1) * 8;
        }
        f32x16 kv = {};
#pragma unroll
        for (int ks = 0; ks < 8; ks++)
            kv = mfma32(cat(trd(aa[0] + ks * 2048), trd(aa[1] + ks * 2048)), cat(trd(ab[0] + ks * 2048), trd(ab[1] + ks * 2048)), kv);
        if ((l & 31) < 20) {
            const u32 lam = h * 20 + (l & 31), o = WS_PART + (c * 16 + r) * 4096 + w * 960;
            __builtin_amdgcn_raw_buffer_store_b128(u32x4{pk(kv[0], kv[1]), pk(kv[2], kv[3]), pk(kv[4], kv[5]), pk(kv[6], kv[7])}, rsw, lam * 16, o, 16);
            __builtin_amdgcn_raw_buffer_store_b64(u32x2{pk(kv[8], kv[9]), pk(kv[10], kv[11])}, rsw, 640 + lam * 8, o, 16);
        }
    }
    STAMP(11);
    u32 phiq[20];
    {
        float qr[20];
#pragma unroll
        for (int i = 0; i < 20; i++) qr[i] = i < 16 ? acc[0][i] : acc[3][i - 16];
        features(qr, wf, phiq);
#pragma unroll
        for (int i = 0; i < 20; i++) asm volatile("" : "+v"(phiq[i]));
    }
    STAMP(17);
    waitv<0>();
    asm volatile("" ::"v"(pfd));
    bar();
    post_flag(ws + WS_F1 + c * 4096, tag, tid, 16, r);
    STAMP(3);
    issue_wo(0); issue_wo(1);
    if (tid < 80) ldsw<u64>(LTAB + tid * TROW + 48, 0ull);
    STAMP(4);
    if (w == 0) wait_flags(ws + WS_F1 + c * 4096 + r * 256, tag, l, 16);
    bar();
    STAMP(5);
    {
        const int t = tid < 240 ? tid : 239;
        const int un = (t * 1093) >> 16, it = t - un * 60;
        u32x4 pv[16];
#pragma unroll
        for (int i = 0; i < 16; i++) pv[i] = __builtin_amdgcn_raw_buffer_load_b128(rsw, t * 16 + i * 4096, WS_PART + c * 65536, 16);
        float s[8] = {};
        u32 slo = 0x00003f80u, shi = 0x3f800000u;
        asm volatile("" : "+s"(slo), "+s"(shi));
#pragma unroll
        for (int i = 0; i < 16; i++)
#pragma unroll
            for (int e = 0; e < 4; e++) { s[2 * e] = dot2(pv[i][e], slo, s[2 * e]); s[2 * e + 1] = dot2(pv[i][e], shi, s[2 * e + 1]); }
        const int lam = it < 40 ? it : 2 * (it - 40);
        const int hh = lam >= 20, f = lam - 20 * hh;
        const u32 a0 = LTAB + (un >> 1) * THEAD + ((un & 1) * 20 + f) * TROW + hh * 8 + (it < 40 ? 0 : 32);
        ldsw<u32x2>(a0, u32x2{pk(s[0], s[1]), pk(s[2], s[3])});
        ldsw<u32x2>(a0 + (it < 40 ? 16 : TROW), u32x2{pk(s[4], s[5]), pk(s[6], s[7])});
    }
    bar();
    STAMP(6);
    {
        const u32 tb = LTAB + hk * THEAD + q * TROW;
        const u32 t0 = tb + (same ? rq : 6) * 8, t1 = tb + (same && rq < 2 ? 4 + rq : 6) * 8;
        f32x16 o0 = {}, o1 = {};
#pragma unroll
        for (int s = 0; s < 5; s++) {
            const s16x8 B = frag(phiq[4 * s], phiq[4 * s + 1], phiq[4 * s + 2], phiq[4 * s + 3]);
            o0 = mfma32(cat(trd(t0 + 8 * s * TROW), trd(t0 + (8 * s + 4) * TROW)), B, o0);
            o1 = mfma32(cat(trd(t1 + 8 * s * TROW), trd(t1 + (8 * s + 4) * TROW)), B, o1);
        }
        const float rden = __builtin_amdgcn_rcpf(o1[4] + 1e-6f);
        u32 ob[10];
#pragma unroll
        for (int i = 0; i < 10; i++) ob[i] = i < 8 ? pk(o0[2 * i] * rden, o0[2 * i + 1] * rden) : pk(o1[2 * i - 16] * rden, o1[2 * i - 15] * rden);
        const u32 o = WS_O + (r * 16 + c) * 10240 + w * 2560;
        __builtin_amdgcn_raw_buffer_store_b128(u32x4{ob[0], ob[1], ob[2], ob[3]}, rsw, l * 16, o, 16);
        __builtin_amdgcn_raw_buffer_store_b128(u32x4{ob[4], ob[5], ob[6], ob[7]}, rsw, l * 16 + 1024, o, 16);
        __builtin_amdgcn_raw_buffer_store_b64(u32x2{ob[8], ob[9]}, rsw, l * 8 + 2048, o, 16);
    }
    STAMP(12);
    waitv<0>();
    bar();
    post_flag(ws + WS_F2 + r * 4096, tag, tid, 16, c);
    STAMP(7);
    {
        constexpr int NPRE = 6;
        const int th = c2 & 1, cb = c2 >> 1, hp = g & 1, P = g >> 1;
        u32 b1 = (20 * hp + 8 * P + q) * 160 + p * 8, b2 = (80 * P + 20 * hp + 16 + q) * 160 + p * 8;
        u32 b1h = b1 + 2 * WOSLOT, b2h = b2 + 2 * WOSLOT;
        asm volatile("" : "+v"(b1), "+v"(b2), "+v"(b1h), "+v"(b2h));
        s16x4 wfr[NPRE][10];
        auto rdw = [&](int ks, int n) {
            const int G = ks / 5, i = ks % 5, sl = G % 3, t = n >> 1, rd = n & 1;
            const u32 so = sl == 2 ? 0 : sl * WOSLOT;
            wfr[ks % NPRE][n] = i < 4 ? trd((sl == 2 ? b1h : b1) + so + (40 * i + 4 * rd) * 160 + 32 * t) : trd((sl == 2 ? b2h : b2) + so + 40 * rd * 160 + 32 * t);
        };
#pragma unroll
        for (int ks = 0; ks < NPRE; ks++)
#pragma unroll
            for (int n = 0; n < 10; n++) rdw(ks, n);
        bar();
        issue_wo(3);
        if (w == 0) wait_flags(ws + WS_F2 + r2 * 4096 + c2 * 256, tag, l, 16);
        waitv<0>();
        bar();
        STAMP(8);
        const int ti = 16 * (w & 1) + (l & 15);
        const u32 ob = WS_O + r2 * 16 * 10240 + (2 * th + (w >> 1)) * 2560;
        const u32 v1 = P * 1024 + (hp * 32 + ti) * 16, v2 = 2048 + 2 * P * 10240 + (hp * 32 + ti) * 8;
        u32x4 of[16]; u32x2 ot[8];
#pragma unroll
        for (int G = 0; G < 4; G++) {
#pragma unroll
            for (int i = 0; i < 4; i++) of[4 * G + i] = __builtin_amdgcn_raw_buffer_load_b128(rsw, v1 + (4 * G + i) * 10240, ob, 16);
#pragma unroll
            for (int i = 0; i < 2; i++) ot[2 * G + i] = __builtin_amdgcn_raw_buffer_load_b64(rsw, v2 + (4 * G + i) * 10240, ob, 16);
        }
        if (bid == 0 && tid == 0) __hip_atomic_store((GAS u64*)(ws + WS_EPOCH), tag, __ATOMIC_RELAXED, __HIP_MEMORY_SCOPE_AGENT);
        f32x4 oc[5] = {};
#pragma unroll
        for (int ks = 0; ks < 20; ks++) {
            const int G = ks / 5, i = ks % 5;
            const s16x8 B = i < 4 ? __builtin_bit_cast(s16x8, of[4 * G + i]) : frag(ot[2 * G][0], ot[2 * G][1], ot[2 * G + 1][0], ot[2 * G + 1][1]);
#pragma unroll
            for (int t = 0; t < 5; t++) {
                SB;
                mfma16i(oc[t], cat(wfr[ks % NPRE][2 * t], wfr[ks % NPRE][2 * t + 1]), B);
                SB;
                if (ks + NPRE < 20) { rdw(ks + NPRE, 2 * t); rdw(ks + NPRE, 2 * t + 1); }
            }
        }
        SB;
        u16* op = out + (size_t)(128 * r2 + 64 * th + 16 * w + (l & 15)) * 1280 + 640 + 80 * cb + 4 * g;
#pragma unroll
        for (int t = 0; t < 5; t++) *(u32x2*)(op + 16 * t) = u32x2{pk(oc[t][0], oc[t][1]), pk(oc[t][2], oc[t][3])};
    }
    STAMP(9);
}

DI f32x2 cmul(f32x2 a, f32x2 b) { const f32x2 t = a.xx * b; const f32x2 s = {-b.y, b.x}; return __builtin_elementwise_fma(a.yy, s, t); }
DI f32x2 csq(f32x2 a) { return f32x2{__builtin_fmaf(a.x, a.x, -a.y * a.y), 2.f * a.x * a.y}; }
DI f32x2 cexp(float a, float b) {
    const float m = __builtin_amdgcn_exp2f(a * 1.4426950408889634f);
    const float c1 = 0.15915494309189535f, c2 = 6.4206382e-9f;
    const float n = __builtin_rintf(b * c1);
    const float f = __builtin_fmaf(b, c2, __builtin_fmaf(b, c1, -n));
    return f32x2{m * __builtin_amdgcn_cosf(f), m * __builtin_amdgcn_sinf(f)};
}
constexpr int LU = 65536;
DI u32 kaddr(u32 e) { return ((e >> 5) << 8) + ((e & 31) << 1); }

constexpr int LPRM = LU;
DI void prologue_a(const u16* __restrict__ Are, const u16* __restrict__ Aim, const u16* __restrict__ Bre,
                   const u16* __restrict__ Bim, const u16* __restrict__ Cre, const u16* __restrict__ Cim,
                   const u16* __restrict__ logdt, const u16* __restrict__ Dskip) {
    const int ia = blockIdx.x - 256, mg = ia >> 3, xg = ia & 7;
    const int l = threadIdx.x & 63, w = __builtin_amdgcn_readfirstlane(threadIdx.x >> 6);
    const u32 po = (80 * xg + 4 * mg + w) * 256 + l * 4, pd = LPRM + w * 4096;
    dma4(pd, po, Are); dma4(pd + 256, po, Aim); dma4(pd + 512, po, Bre); dma4(pd + 768, po, Bim); dma4(pd + 1024, po, Cre); dma4(pd + 1280, po, Cim);
    const u32 so = (80 * xg + 4 * mg) * 2 + (l & 1) * 4;
    dma4(pd + 1536, so, logdt); dma4(pd + 1792, so, Dskip);
}

DI void path_a(const u16* __restrict__ u, u16* __restrict__ out, char* __restrict__ ws, u32 pfd STAMP_ARG) {
    const int ia = blockIdx.x - 256, mg = ia >> 3, d0 = 80 * (ia & 7) + 4 * mg;
    const int tid = threadIdx.x, l = tid & 63;
    const int w = __builtin_amdgcn_readfirstlane(tid >> 6);
    const int d = d0 + w;
    const u32 KB = w * 16384;
    const int tg = d0 >> 6, trb = (d0 & 63) >> 2;
    const auto rsw = __builtin_amdgcn_make_buffer_rsrc((void*)ws, 0, WS_END, 0x00020000);
    const int g = l >> 4, q = (l >> 2) & 3, p = l & 3, h = l >> 5;
    STAMP(1);
    u64 tag, hint;
    asm volatile("s_load_dwordx2 %0, %2, 0\n\ts_load_dwordx2 %1, %3, 0\n\ts_waitcnt vmcnt(0) lgkmcnt(0)" : "=&s"(tag), "=&s"(hint)
                 : "s"(ws + WS_EPOCH_A + tg * 128), "s"(ws + WS_EPOCH), "v"(pfd) : "memory");
    tag += 1; hint += 1;
    const float ldt = b2f(ldsr<u16>(LPRM + w * 4096 + 1536 + w * 2)), dsk = b2f(ldsr<u16>(LPRM + w * 4096 + 1792 + w * 2));
    u32 prm[6];
#pragma unroll
    for (int i = 0; i < 6; i++) prm[i] = ldsr<u32>(LPRM + w * 4096 + i * 256 + l * 4);
    {
        const char* ub = (const char*)u + (size_t)(128 * trb + 32 * w) * 1280 + tg * 128;
        waitl0();
#pragma unroll
        for (int i = 0; i < 4; i++) dma(LU + w * 4096 + i * 1024, (l >> 3) * 1280 + (((l & 7) ^ ((4 * w + i) & 7)) << 4), ub + i * 8 * 1280);
    }
    const float dt = lo2f(pk(expf(ldt), 0.f));
    STAMP(2);
    f32x16 kd = {};
    {
        u32 ra[2];
#pragma unroll
        for (int rd = 0; rd < 2; rd++) ra[rd] = KB + (8 * (g >> 1) + 4 * rd + q) * 64 + (((2 * (g & 1) + (p >> 1)) ^ ((2 * rd + (q >> 1)) & 3)) << 4) + (p & 1) * 8;
        const u32 wa = KB + l * 64, wsw = (l >> 1) & 3;
#pragma unroll 1
        for (int ps = 0; ps < 2; ps++) {
            const float a = dt * lo2f(prm[0]), b = dt * lo2f(prm[1]);
            const f32x2 dB = {dt * lo2f(prm[2]), dt * lo2f(prm[3])};
            const f32x2 Wc = cmul(f32x2{lo2f(prm[4]), lo2f(prm[5])}, dB);
#pragma unroll
            for (int i = 0; i < 6; i++) prm[i] >>= 16;
            const f32x2 e1 = cexp(a, b), e16 = cexp(16.f * a, 16.f * b), e256 = cexp(256.f * a, 256.f * b);
            const f32x2 e2 = csq(e1), e4 = csq(e2), e8 = csq(e4), e32 = csq(e16), e64 = csq(e32), e128 = csq(e64), e512 = csq(e256), e1024 = csq(e512);
            if (ps == 1) {
                waitv<0>();
                bar();
                STAMP(14);
#pragma unroll
                for (int it = 0; it < 4; it++) {
                    const int cq = 4 * w + it, eta = 4 * g + p;
                    const u32 a = LU + (8 * eta + q) * 128 + (((cq >> 1) ^ (eta & 7)) << 4) + (cq & 1) * 8;
                    __builtin_amdgcn_raw_buffer_store_b128(__builtin_bit_cast(u32x4, cat(trd(a), trd(a + 512))), rsw,
                                                           (l & 3) * 256 + (l >> 2) * 16 + cq * 1024, WS_UT + (tg * 16 + trb) * 16384, 16);
                }
            }
            f32x2 X[32];
            X[0] = Wc; X[1] = cmul(Wc, e1); X[2] = cmul(Wc, e2); X[3] = cmul(X[1], e2);
#pragma unroll
            for (int i = 0; i < 4; i++) X[4 + i] = cmul(X[i], e4);
            X[8] = cmul(Wc, e256); X[9] = cmul(X[8], e2); X[10] = cmul(X[8], e4);
            X[12] = X[10]; X[13] = cmul(X[9], e4); X[14] = cmul(X[8], e8);
            X[11] = cmul(Wc, e512); X[16] = cmul(X[11], e4); X[17] = cmul(X[11], e8);
            X[15] = cmul(X[8], e512); X[20] = cmul(X[15], e4); X[21] = cmul(X[15], e8);
            X[18] = cmul(Wc, e1024); X[19] = cmul(X[18], e8);
            X[24] = cmul(X[8], e1024); X[25] = cmul(X[24], e8);
            X[22] = cmul(X[11], e1024); X[23] = cmul(X[22], e8);
            X[28] = cmul(X[15], e1024); X[29] = cmul(X[28], e8);
            X[26] = X[27] = X[30] = X[31] = f32x2{0.f, 0.f};
#pragma unroll
            for (int ch = 0; ch < 4; ch++) {
                const u32 o = (ch ^ wsw) << 4;
                ldsw<u32x4>(wa + o, u32x4{pk(X[8 * ch].x, X[8 * ch + 1].x), pk(X[8 * ch + 2].x, X[8 * ch + 3].x), pk(X[8 * ch + 4].x, X[8 * ch + 5].x), pk(X[8 * ch + 6].x, X[8 * ch + 7].x)});
                ldsw<u32x4>(wa + o + 4096, u32x4{pk(X[8 * ch].y, X[8 * ch + 1].y), pk(X[8 * ch + 2].y, X[8 * ch + 3].y), pk(X[8 * ch + 4].y, X[8 * ch + 5].y), pk(X[8 * ch + 6].y, X[8 * ch + 7].y)} ^ 0x80008000u);
            }
            f32x2 Y[8];
            Y[0] = f32x2{1.f, 0.f}; Y[1] = e8; Y[2] = e16; Y[3] = cmul(e8, e16); Y[4] = e32; Y[5] = cmul(e8, e32); Y[6] = cmul(e16, e32); Y[7] = cmul(Y[3], e32);
            const f32x2 e192 = cmul(e64, e128);
#pragma unroll
            for (int ch = 0; ch < 4; ch++) {
                f32x2 Z[8];
                const f32x2 m = ch == 1 ? e64 : ch == 2 ? e128 : e192;
#pragma unroll
                for (int i = 0; i < 8; i++) Z[i] = ch == 0 ? Y[i] : i == 0 ? m : cmul(Y[i], m);
                const u32 o = (ch ^ wsw) << 4;
                ldsw<u32x4>(wa + o + 8192, u32x4{pk(Z[0].x, Z[1].x), pk(Z[2].x, Z[3].x), pk(Z[4].x, Z[5].x), pk(Z[6].x, Z[7].x)});
                ldsw<u32x4>(wa + o + 12288, u32x4{pk(Z[0].y, Z[1].y), pk(Z[2].y, Z[3].y), pk(Z[4].y, Z[5].y), pk(Z[6].y, Z[7].y)});
            }
#pragma unroll
            for (int ks = 0; ks < 8; ks++)
                kd = mfma32(cat(trd(ra[0] + ks * 1024), trd(ra[1] + ks * 1024)), cat(trd(ra[0] + ks * 1024 + 8192), trd(ra[1] + ks * 1024 + 8192)), kd);
        }
    }
    STAMP(7);
    waitv<0>();
    bar();
    post_flag(ws + WS_F3 + tg * 4096, tag, tid, 16, trb);
    STAMP(3);
    {
        const int j = l & 31;
        ldsw<u32x2>(KB + kaddr(2044 - 8 * j - 4 * h), u32x2{pk(kd[3], kd[2]), pk(kd[1], kd[0])});
        ldsw<u32x2>(KB + kaddr(2044 - 256 - 8 * j - 4 * h), u32x2{pk(kd[6], kd[5]), pk(kd[4], kd[4])});
        ldsw<u32x4>(KB + kaddr(2040 - 512 - 256 * h - 8 * j), u32x4{pk(kd[9], kd[9]), pk(kd[8], kd[8]), pk(kd[8], kd[7]), pk(kd[7], kd[7])});
#pragma unroll
        for (int sp = 0; sp < 2; sp++) {
            const float a = kd[10 + 2 * sp], b = kd[11 + 2 * sp];
            const u32 aa = pk(a, a), bb = pk(b, b);
            ldsw<u32x4>(KB + kaddr(2040 - 1024 - 512 * h - 256 * sp - 8 * j), u32x4{bb, (j & 1) ? bb : pk(b, a), aa, aa});
        }
#pragma unroll
        for (int it = 0; it < 4; it++) {
            const u32 i = it * 64 + l, a0 = KB + kaddr(8 * i);
            const u32x4 v = ldsr<u32x4>(a0);
            const u32x2 n = ldsr<u32x2>(KB + kaddr(8 * i + 8));
            const u32 s0 = __builtin_amdgcn_alignbit(v[1], v[0], 16), s1 = __builtin_amdgcn_alignbit(v[2], v[1], 16), s2 = __builtin_amdgcn_alignbit(v[3], v[2], 16),
                      s3 = __builtin_amdgcn_alignbit(n[0], v[3], 16), s4 = __builtin_amdgcn_alignbit(n[1], n[0], 16);
            ldsw<u32x4>(a0 + 64, u32x4{s0, s1, s2, s3});
            ldsw<u32x4>(a0 + 128, u32x4{v[1], v[2], v[3], n[0]});
            ldsw<u32x4>(a0 + 192, u32x4{s1, s2, s3, s4});
        }
    }
    STAMP(8);
    if (w == 0) wait_flags(ws + WS_F3 + tg * 4096 + trb * 256, tag, l, 16);
    bar();
    STAMP(9);
    {
        u32x4 uv[4];
#pragma unroll
        for (int it = 0; it < 4; it++)
            uv[it] = __builtin_amdgcn_raw_buffer_load_b128(rsw, (4 * it + g) * 16384 + (l & 15) * 16, WS_UT + tg * 262144 + (d & 63) * 256, 16);
        if (trb == 0 && tid == 0) __hip_atomic_store((GAS u64*)(ws + WS_EPOCH_A + tg * 128), tag, __ATOMIC_RELAXED, __HIP_MEMORY_SCOPE_AGENT);
#pragma unroll
        for (int it = 0; it < 4; it++) ldsw<u32x4>(LU + w * 4096 + it * 1024 + l * 16, uv[it]);
    }
    STAMP(10);
    f32x4 y[8] = {};
    const int rr = l & 15;
    {
        const u32 e0 = 2047 - 16 - rr + 8 * g, sh = e0 & 3;
        u32 ka0 = KB + kaddr(e0 - sh) + sh * 64 - 7 * 2048, ka1 = KB + kaddr(e0 - sh + 4) + sh * 64 - 7 * 2048;
        u32 ua = LU + w * 4096 + 32 * rr + 16 * g - 32;
        u32 mk[4];
#pragma unroll
        for (int i = 0; i < 4; i++) mk[i] = (2 * i > 16 + rr - 8 * g ? 0u : 0xffffu) | (2 * i + 1 > 16 + rr - 8 * g ? 0u : 0xffff0000u);
        int lim = 2 * rr + g - 2;
        u32x2 A[2][16]; u32x4 B[2][8];
        auto rdc = [&](int s, int n) {
            const int m = n / 3, k = n % 3;
            if (k == 0) B[s][m] = ldsr<u32x4>(ua + m * 512);
            else if (k == 1) A[s][2 * (7 - m)] = ldsr<u32x2>(ka0 + m * 2048);
            else A[s][2 * (7 - m) + 1] = ldsr<u32x2>(ka1 + m * 2048);
        };
        auto group = [&](int s, bool next) {
            A[s][0] &= u32x2{mk[0], mk[1]}; A[s][1] &= u32x2{mk[2], mk[3]};
            if (lim < 0) B[s][0] = u32x4{0, 0, 0, 0};
            ka0 -= 256; ka1 -= 256; ua -= 64; lim -= 4;
            mk[0] = mk[1] = mk[2] = mk[3] = 0xffffffffu;
            asm volatile("" : "+v"(ka0), "+v"(ka1), "+v"(ua));
            int n = 0;
#pragma unroll
            for (int m = 7; m >= 0; m--)
#pragma unroll
                for (int ct = 7 - m; ct >= 0; ct--) {
                    SB;
                    mfma16i(y[ct + m], frag(A[s][2 * m][0], A[s][2 * m][1], A[s][2 * m + 1][0], A[s][2 * m + 1][1]), __builtin_bit_cast(s16x8, B[s][ct]));
                    SB;
                    if (next && n < 24) rdc(s ^ 1, n);
                    n++;
                }
        };
        if (w == 0) {
            const GAS u64* hp = (const GAS u64*)(ws + WS_HINT + ia * 128);
#pragma unroll 1
            for (int s = 0; s < 10; s++) {
                if (__hip_atomic_load(hp, __ATOMIC_RELAXED, __HIP_MEMORY_SCOPE_AGENT) == hint) break;
                __builtin_amdgcn_s_sleep(HSLEEP);
            }
        }
        bar();
        STAMP(13);
        asm volatile("" : "+v"(ka0), "+v"(ka1), "+v"(ua));
#pragma unroll
        for (int n = 0; n < 24; n++) rdc(0, n);
#pragma unroll 1
        for (int gg = 0; gg < 3; gg++) { group(0, true); group(1, true); }
        group(0, true); group(1, false);
    }
    STAMP(11);
#pragma unroll
    for (int ct = 0; ct < 8; ct++) {
        const u32 a = LU + w * 4096 + ct * 512 + rr * 32 + g * 8;
        const u32x2 uu = ldsr<u32x2>(a);
        const u32 y0 = pk(y[ct][0], y[ct][1]), y1 = pk(y[ct][2], y[ct][3]);
        const u32 s0 = pk(lo2f(uu[0]) * dsk, hi2f(uu[0]) * dsk), s1 = pk(lo2f(uu[1]) * dsk, hi2f(uu[1]) * dsk);
        ldsw<u32x2>(a, u32x2{pk(lo2f(y0) + lo2f(s0), hi2f(y0) + hi2f(s0)), pk(lo2f(y1) + lo2f(s1), hi2f(y1) + hi2f(s1))});
    }
    bar();
    {
        u32x4 c[4];
#pragma unroll
        for (int i = 0; i < 4; i++) c[i] = ldsr<u32x4>(LU + i * 4096 + tid * 16);
        u16* op = out + (size_t)tid * 8 * 1280 + d0;
#pragma unroll
        for (int i = 0; i < 4; i++) {
            *(u32x2*)(op + (2 * i) * 1280) = u32x2{__builtin_amdgcn_perm(c[1][i], c[0][i], 0x05040100u), __builtin_amdgcn_perm(c[3][i], c[2][i], 0x05040100u)};
            *(u32x2*)(op + (2 * i + 1) * 1280) = u32x2{__builtin_amdgcn_perm(c[1][i], c[0][i], 0x07060302u), __builtin_amdgcn_perm(c[3][i], c[2][i], 0x07060302u)};
        }
    }
    STAMP(12);
}

#if defined(__HIP_DEVICE_COMPILE__)
__attribute__((target("no-packed-fp32-ops")))
#endif
__global__ void __launch_bounds__(256, 2)
fused_kernel(const u16* __restrict__ x, const u16* __restrict__ W, const u16* __restrict__ wphi, const u16* __restrict__ wo,
             u16* __restrict__ out, char* __restrict__ ws, const u16* __restrict__ u, const u16* __restrict__ Are,
             const u16* __restrict__ Aim, const u16* __restrict__ Bre, const u16* __restrict__ Bim, const u16* __restrict__ Cre,
             const u16* __restrict__ Cim, const u16* __restrict__ logdt, const u16* __restrict__ Dskip) {
    __shared__ __attribute__((aligned(16))) char smem[LDS_BYTES];
    asm volatile("" ::"s"((u32)(size_t)(LAS char*)smem));
    STAMP(0);
    u32 pfd = 0;
    if (threadIdx.x < 8) {
        u64 pc; asm volatile("s_getpc_b64 %0" : "=s"(pc));
        asm volatile("global_load_dword %0, %1, %2" : "+v"(pfd) : "v"(min((((blockIdx.x >> 3) & 31) * 8 + threadIdx.x) * 128, CODE_PF_BYTES)), "s"(pc));
    }
    const bool is_a = blockIdx.x >= 256;
    if (is_a) prologue_a(Are, Aim, Bre, Bim, Cre, Cim, logdt, Dskip); else prologue_b(W);
    u32 bsel = blockIdx.x;
    asm volatile("" : "+s"(bsel));
    if (bsel >= 256) {
        __builtin_amdgcn_s_setprio(APRIO);
        path_a(u, out, ws, pfd STAMP_PASS);
    } else {
        __builtin_amdgcn_s_setprio(2);
        path_b(x, W, wphi, wo, out, ws, pfd STAMP_PASS);
    }
}

extern "C" void kernel_launch(void* const* d_in, const int* in_sizes, int n_in, void* d_out, int out_size, void* d_ws, size_t ws_size,
                              hipStream_t stream) {
    const u16* const* in = (const u16* const*)d_in;
    fused_kernel<<<416, 256, 0, stream>>>(in[9], in[10], in[11], in[12], (u16*)d_out, (char*)d_ws, in[0], in[1], in[2], in[3], in[4], in[5],
                                          in[6], in[7], in[8]);
}
```

```cpp
#define STAMPS
#include <hip/hip_runtime.h>
#include <hip/hip_bf16.h>

typedef unsigned short u16;
typedef unsigned u32;
typedef unsigned long long u64;
typedef __attribute__((ext_vector_type(8))) short s16x8;
typedef __attribute__((ext_vector_type(4))) short s16x4;
typedef __attribute__((ext_vector_type(16))) float f32x16;
typedef __attribute__((ext_vector_type(4))) float f32x4;
typedef __attribute__((ext_vector_type(2))) float f32x2;
typedef __attribute__((ext_vector_type(4))) u32 u32x4;
typedef __attribute__((ext_vector_type(2))) u32 u32x2;
typedef __attribute__((ext_vector_type(2))) __bf16 bf16x2;
#define LAS __attribute__((address_space(3)))
#define GAS __attribute__((address_space(1)))
#define DI __device__ __forceinline__
#define SB __builtin_amdgcn_sched_barrier(0)

#define HSLEEP 16
#define APRIO 0
constexpr int LDS_BYTES = 81920;
constexpr u32 WS_EPOCH = 0, WS_EPOCH_A = 4096, WS_F1 = 65536, WS_F2 = 131072, WS_F3 = 196608, WS_PART = 524288, WS_O = 2097152,
              WS_UT = 5u << 20, WS_F0 = 262144, WS_HINT = 327680, WS_WP = 9u << 20, WS_END = 12u << 20;
constexpr u32 WS_STAMP = 16u << 20;
constexpr u32 kSpinCap = 1u << 18;
constexpr u32 CODE_PF_BYTES = 30720;

DI u32 pk(float a, float b) { f32x2 v = {a, b}; return __builtin_bit_cast(u32, __builtin_convertvector(v, bf16x2)); }
DI float lo2f(u32 x) { return __uint_as_float(x << 16); }
DI float hi2f(u32 x) { return __uint_as_float(x & 0xffff0000u); }
DI float b2f(u16 h) { return __uint_as_float((u32)h << 16); }
DI s16x4 trd(u32 a) { return __builtin_amdgcn_ds_read_tr16_b64_v4i16((LAS s16x4*)(size_t)a); }
DI s16x8 cat(s16x4 a, s16x4 b) { return __builtin_shufflevector(a, b, 0, 1, 2, 3, 4, 5, 6, 7); }
DI s16x8 frag(u32 a, u32 b, u32 c, u32 d) { u32x4 v = {a, b, c, d}; return __builtin_bit_cast(s16x8, v); }
template <class T> DI T ldsr(u32 a) { return *(LAS T*)(size_t)a; }
template <class T> DI void ldsw(u32 a, T v) { *(LAS T*)(size_t)a = v; }
template <int N> DI void waitv() { asm volatile("s_waitcnt vmcnt(%0)" ::"i"(N) : "memory"); }
DI void waitl0() { asm volatile("s_waitcnt lgkmcnt(0)" ::: "memory"); }
DI void bar() { asm volatile("s_waitcnt lgkmcnt(0)\n\ts_barrier" ::: "memory"); }
DI f32x16 mfma32(s16x8 a, s16x8 b, f32x16 c) { return __builtin_amdgcn_mfma_f32_32x32x16_bf16(a, b, c, 0, 0, 0); }
DI f32x4 mfma16(s16x8 a, s16x8 b, f32x4 c) { return __builtin_amdgcn_mfma_f32_16x16x32_bf16(a, b, c, 0, 0, 0); }
DI float max3(float a, float b, float c) { float r; asm("v_max3_f32 %0, %1, %2, %3" : "=v"(r) : "v"(a), "v"(b), "v"(c)); return r; }
DI float min3(float a, float b, float c) { float r; asm("v_min3_f32 %0, %1, %2, %3" : "=v"(r) : "v"(a), "v"(b), "v"(c)); return r; }
DI float dot2(u32 x, u32 sel, float c) { return __builtin_amdgcn_fdot2_f32_bf16(__builtin_bit_cast(bf16x2, sel), __builtin_bit_cast(bf16x2, x), c, false); }
DI void dma(u32 m0v, u32 voff, const void* sbase) {
    asm volatile("s_mov_b32 m0, %0\n\ts_nop 0\n\tglobal_load_lds_dwordx4 %1, %2" ::"s"(m0v), "v"(voff), "s"(sbase) : "memory");
}
DI void dma_m(u32 m0v, u32 voff, const void* sbase, u32 mlo, u32 mhi) {
    asm volatile("s_mov_b32 exec_lo, %3\n\ts_mov_b32 exec_hi, %4\n\ts_mov_b32 m0, %0\n\ts_nop 0\n\tglobal_load_lds_dwordx4 %1, %2\n\ts_mov_b64 exec, -1"
                 ::"s"(m0v), "v"(voff), "s"(sbase), "i"(mlo), "i"(mhi) : "memory");
}
DI void dma4(u32 m0v, u32 voff, const void* sbase) {
    asm volatile("s_mov_b32 m0, %0\n\ts_nop 0\n\tglobal_load_lds_dword %1, %2" ::"s"(m0v), "v"(voff), "s"(sbase) : "memory");
}
#define WSC " sc1"
DI void dma_s(u32 m0v, u32 voff, const void* sbase) {
    asm volatile("s_mov_b32 m0, %0\n\ts_nop 0\n\tglobal_load_lds_dwordx4 %1, %2" WSC ::"s"(m0v), "v"(voff), "s"(sbase) : "memory");
}
DI void dma_sm(u32 m0v, u32 voff, const void* sbase, u32 mhi) {
    asm volatile("s_mov_b32 exec_hi, %3\n\ts_mov_b32 m0, %0\n\ts_nop 0\n\tglobal_load_lds_dwordx4 %1, %2 offset:1024" WSC "\n\ts_mov_b32 exec_hi, -1"
                 ::"s"(m0v), "v"(voff), "s"(sbase), "i"(mhi) : "memory");
}
DI void mfma16i(f32x4& c, s16x8 a, s16x8 b) { c = mfma16(a, b, c); }
#define STAMP(i) asm volatile("s_memrealtime %0\n\ts_waitcnt lgkmcnt(0)" : "=s"(stv[i])::"memory")
#define STAMP_ARG , u64 (&stv)[24]
#define STAMP_PASS , stv

DI void post_flag(char* fl, u64 tag, int tid, int n, int me) {
    if (tid < n) __hip_atomic_store((GAS u64*)(fl + tid * 256 + me * 8), tag, __ATOMIC_RELAXED, __HIP_MEMORY_SCOPE_AGENT);
}
DI void wait_flags(const char* fl, u64 tag, int l, int n) {
    const GAS u64* p = (const GAS u64*)(fl + (l < n ? l : 0) * 8);
#pragma unroll 1
    for (u32 s = 0; s < kSpinCap; s++) {
        u64 v = __hip_atomic_load(p, __ATOMIC_RELAXED, __HIP_MEMORY_SCOPE_AGENT);
        if (__all(v == tag)) break;
        __builtin_amdgcn_s_sleep(1);
    }
}

DI void features(const float (&v)[20], const s16x8 (&wf)[2][3], u32 (&phi)[20]) {
    u32 b[10];
#pragma unroll
    for (int i = 0; i < 10; i++) b[i] = pk(v[2 * i], v[2 * i + 1]);
    const s16x8 B0 = frag(b[0], b[1], b[2], b[3]), B1 = frag(b[4], b[5], b[6], b[7]), B2 = frag(b[8], b[9], 0, 0);
    f32x16 p0 = {}, p1 = {};
    p0 = mfma32(wf[0][0], B0, p0); p1 = mfma32(wf[1][0], B0, p1);
    p0 = mfma32(wf[0][1], B1, p0); p1 = mfma32(wf[1][1], B1, p1);
    p0 = mfma32(wf[0][2], B2, p0); p1 = mfma32(wf[1][2], B2, p1);
    asm volatile("s_nop 15" : "+v"(p0), "+v"(p1));
    float p[20];
#pragma unroll
    for (int i = 0; i < 20; i++) p[i] = i < 16 ? p0[i] : p1[i - 16];
    float mx = max3(p[0], p[1], p[2]), mn = min3(p[0], p[1], p[2]);
#pragma unroll
    for (int i = 3; i < 19; i += 2) { mx = max3(mx, p[i], p[i + 1]); mn = min3(mn, p[i], p[i + 1]); }
    mx = max3(mx, p[19], p[19]); mn = min3(mn, p[19], p[19]);
    const float L2E = 1.4426950408889634f;
    const float cx = -mx * L2E, cn = mn * L2E;
    float e[40], s1 = 0.f, s2 = 0.f;
#pragma unroll
    for (int i = 0; i < 20; i++) {
        e[i] = __builtin_amdgcn_exp2f(__builtin_fmaf(p[i], L2E, cx));
        e[20 + i] = __builtin_amdgcn_exp2f(__builtin_fmaf(p[i], -L2E, cn));
        s1 += e[i]; s2 += e[20 + i];
    }
    const float r1 = __builtin_amdgcn_rcpf(s1), r2 = __builtin_amdgcn_rcpf(s2);
#pragma unroll
    for (int i = 0; i < 10; i++) { phi[i] = pk(e[2 * i] * r1, e[2 * i + 1] * r1); phi[10 + i] = pk(e[20 + 2 * i] * r2, e[21 + 2 * i] * r2); }
}

constexpr int LX = 0, LW = 49152, WBLK = 960, WSLOT = 8 * WBLK, LMISC = 79872, LZERO = LMISC + 1600;
constexpr int LIMG = 16384, LTAB = 76800, TROW = 56, THEAD = 40 * TROW, WOSLOT = 25600;

DI void prologue_b(const u16* __restrict__ W) {
    const int bid = blockIdx.x, xcd = bid & 7, jj = bid >> 3;
    const int r = 8 * (xcd >> 2) + (jj & 7), c = 4 * (xcd & 3) + (jj >> 3);
    const int tid = threadIdx.x, l = tid & 63, w = __builtin_amdgcn_readfirstlane(tid >> 6);
    const int pl = l >> 4, idx = l & 15, q3 = (idx * 11) >> 5;
    const int q = pl < 3 ? idx >> 2 : q3 & 3, sg = pl < 3 ? pl : idx - 3 * q3, ch = pl < 3 ? (idx & 3) + ((idx >> 1) & 1) : 2;
    const u32 wl = q * 3840 + sg * 1280 + ch * 16;
    const char* src = (const char*)W + ((c & ~1) + (w & 1)) * 80 + (20 * (2 * r + (c & 1)) + 4 * (w >> 1)) * 3840;
    dma(LW + w * 3072, wl, src); dma(LW + w * 3072 + 1024, wl, src + 8 * 3840);
    if (w < 2) dma(LW + w * 3072 + 2048, wl, src + 16 * 3840);
}

DI void path_b(const u16* __restrict__ x, const u16* __restrict__ W, const u16* __restrict__ wphi, const u16* __restrict__ wo,
               u16* __restrict__ out, char* __restrict__ ws, u32& pfd STAMP_ARG) {
    const int bid = blockIdx.x, xcd = bid & 7, jj = bid >> 3;
    const int r = 8 * (xcd >> 2) + (jj & 7), c = 4 * (xcd & 3) + (jj >> 3);
    const int r2 = 2 * xcd + (jj >> 4), c2 = jj & 15;
    const int tid = threadIdx.x, l = tid & 63;
    const int w = __builtin_amdgcn_readfirstlane(tid >> 6);
#define ROTG 4
    const int ph = ((((jj & 7) + (jj >> 3)) % ROTG) * 10) / ROTG;
    const auto rsw = __builtin_amdgcn_make_buffer_rsrc((void*)ws, 0, WS_END, 0x00020000);
    const char* xb = (const char*)x + (size_t)r * 128 * 1280;
    const char* wb = ws + WS_WP + c * 153600 + w * 2 * WBLK;
    u32 xo[2];
#pragma unroll
    for (int i = 0; i < 2; i++) { int row = 32 * w + 8 * i + (l >> 3); xo[i] = row * 1280 + (((l & 7) ^ ((row >> 1) & 7)) << 4); }
    const u32 lx = LX + w * 4096, lwv = LW + w * 2 * WBLK, l16 = l * 16;
    auto issue_x = [&](int hx) {
        int jx = hx >> 1, hh = hx & 1, jr = jx + ph; if (jr >= 10) jr -= 10;
        const char* g = xb + jr * 128 + hh * 16 * 1280;
        dma(lx + (jx % 3) * 16384 + hh * 2048, xo[0], g); dma(lx + (jx % 3) * 16384 + hh * 2048 + 1024, xo[1], g);
    };
    auto issue_w = [&](int i) {
        int ir = i + 2 * ph; if (ir >= 20) ir -= 20;
        const char* g = wb + ir * WSLOT;
        dma_s(lwv + (i & 3) * WSLOT, l16, g); dma_sm(lwv + (i & 3) * WSLOT, l16, g, 0x00ffffffu);
    };
    STAMP(15);
        u64 tag;
    asm volatile("s_load_dwordx2 %0, %1, 0\n\ts_waitcnt vmcnt(0) lgkmcnt(0)" : "=&s"(tag) : "s"(ws + WS_EPOCH) : "memory");
    tag += 1;
    STAMP(19);
    if (w == 2) dma(LMISC, l * 16, (const char*)wphi + c * 1600);
    if (w == 3) dma_m(LMISC + 1024, l * 16, (const char*)wphi + c * 1600 + 1024, 0xffffffffu, 0xfu);
    {
        const u32 o = WS_WP + ((c & ~1) + (w & 1)) * 153600 + (5 * (2 * r + (c & 1)) + (w >> 1)) * WBLK, a = LW + w * 3072 + l16;
        const u32x4 v0 = ldsr<u32x4>(a), v1 = ldsr<u32x4>(a + 1024), v2 = ldsr<u32x4>(a + 2048);
        if (l < 60) {
            __builtin_amdgcn_raw_buffer_store_b128(v0, rsw, l16, o, 16);
            __builtin_amdgcn_raw_buffer_store_b128(v1, rsw, l16 + 2 * WBLK, o, 16);
            if (w < 2) __builtin_amdgcn_raw_buffer_store_b128(v2, rsw, l16 + 4 * WBLK, o, 16);
        }
    }
    STAMP(20);
    issue_x(0); issue_x(1); issue_x(2); issue_x(3);
    if (tid < 2) ldsw<u64>(LZERO + tid * 8, 0ull);
    STAMP(16);

    const int g = l >> 4, q = (l >> 2) & 3, p = l & 3, h = l >> 5;
    u32 bA = LW + (g >> 1) * 2 * WBLK + q * 64 + (p & 1) * 32 + (g & 1) * 16 + (p >> 1) * 8;
    u32 bD = LW + (g >> 1) * 2 * WBLK + 768 + (3 * q + 2 * (g & 1) + (p >> 1)) * 16 + (p & 1) * 8;
    asm volatile("" : "+v"(bA), "+v"(bD));
    u32 aX[4];
    const int trow = 32 * w + (l & 31);
#pragma unroll
    for (int k = 0; k < 4; k++) aX[k] = LX + trow * 128 + (((2 * k + h) ^ ((trow >> 1) & 7)) << 4);
    f32x16 acc[4] = {};
    s16x4 fa[2][8]; s16x8 fb[2];
    auto rdB = [&](int s, int i, int ks) { fb[s] = ldsr<s16x8>(aX[(i & 1) * 2 + ks] + ((i >> 1) % 3) * 16384); };
    auto rdA = [&](int s, int i, int ks, int n) {
        u32 o = (i & 3) * WSLOT + ks * 4 * WBLK + (n & 1) * WBLK;
        fa[s][n] = n < 6 ? trd(bA + o + (n >> 1) * 256) : trd(bD + o);
    };
    auto mm = [&](int s, int t) { acc[t] = mfma32(cat(fa[s][2 * t], fa[s][2 * t + 1]), fb[s], acc[t]); };
    waitv<8>();
    bar();
    post_flag(ws + WS_F0 + (c >> 1) * 8192, tag, tid, 32, 2 * r + (c & 1));
    STAMP(22);
    const int rq = 2 * (g & 1) + (p >> 1), hk = g >> 1;
    const bool same = hk == (p & 1);
    s16x8 wf[2][3];
#pragma unroll
    for (int to = 0; to < 2; to++)
#pragma unroll
        for (int s = 0; s < 3; s++) {
            s16x4 t[2];
#pragma unroll
            for (int rd = 0; rd < 2; rd++) {
                const int mu = 2 * s + rd;
                if (mu < 5) {
                    int dq = mu + hk; if (mu == 4) dq = hk ? 0 : 4;
                    const bool ok = same && (to == 0 || rq == 0);
                    t[rd] = trd(ok ? LMISC + hk * 800 + (4 * dq + q) * 40 + (4 * to + rq) * 8 : LZERO);
                } else t[rd] = s16x4{0, 0, 0, 0};
            }
            wf[to][s] = cat(t[0], t[1]);
        }
#pragma unroll
    for (int i = 0; i < 6; i++) asm volatile("" : "+v"(wf[i / 3][i % 3]));
    if (w == 0) wait_flags(ws + WS_F0 + (c >> 1) * 8192 + (2 * r + (c & 1)) * 256, tag, l, 32);
    bar();
    STAMP(14);
    issue_w(0); issue_w(1); issue_x(4); issue_w(2); issue_x(5); issue_w(3);
    waitv<10>();
    bar();
    STAMP(1);
    u64 cy0; asm volatile("s_memtime %0\n\ts_waitcnt lgkmcnt(0)" : "=s"(cy0)::"memory");
    rdB(0, 0, 0);
#pragma unroll
    for (int n = 0; n < 8; n++) rdA(0, 0, 0, n);
#pragma unroll
    for (int i = 0; i < 20; i++) {
        if (i == 2) STAMP(13); if (i == 8) STAMP(18);
        SB; mm(0, 0); SB; rdB(1, i, 1); rdA(1, i, 1, 0); rdA(1, i, 1, 1);
        SB; mm(0, 1); SB; rdA(1, i, 1, 2); rdA(1, i, 1, 3); rdA(1, i, 1, 4);
        SB; mm(0, 2); SB; rdA(1, i, 1, 5); rdA(1, i, 1, 6); rdA(1, i, 1, 7);
        SB; mm(0, 3); SB;
        if (i >= 1 && i + 3 < 20) issue_w(i + 3);
        if (i == 17) {
            const u32 ln = jj * 200 + (tid < 200 ? tid : 199);
            asm volatile("global_load_dword %0, %1, %2" : "+v"(pfd) : "v"(ln * 128), "s"(wo));
        }
        SB; mm(1, 0); SB;
        if (i < 19) {
            if (i == 1 || i == 2) waitv<6>(); else if (i <= 15) waitv<8>(); else if (i == 16) waitv<6>(); else if (i == 17) waitv<3>(); else waitv<1>();
            bar();
            rdB(0, i + 1, 0); rdA(0, i + 1, 0, 0); rdA(0, i + 1, 0, 1);
        }
        SB; mm(1, 1); SB;
        if (i < 19) { rdA(0, i + 1, 0, 2); rdA(0, i + 1, 0, 3); rdA(0, i + 1, 0, 4); }
        SB; mm(1, 2); SB;
        if (i < 19) { rdA(0, i + 1, 0, 5); rdA(0, i + 1, 0, 6); rdA(0, i + 1, 0, 7); }
        SB; mm(1, 3); SB;
        if (i >= 1 && i + 5 < 20) issue_x(i + 5);
    }
    if (tid == 0) __hip_atomic_store((GAS u64*)(ws + WS_HINT + bid * 128), tag, __ATOMIC_RELAXED, __HIP_MEMORY_SCOPE_AGENT);
    STAMP(2);
    { u64 cy1; asm volatile("s_memtime %0\n\ts_waitcnt lgkmcnt(0)" : "=s"(cy1)::"memory"); stv[21] = cy1 - cy0; }
    {
        float kr[20]; u32 phik[20];
#pragma unroll
        for (int i = 0; i < 20; i++) kr[i] = i < 16 ? acc[1][i] : acc[3][i - 12];
        features(kr, wf, phik);
        const u32 sw = ((trow & 2) << 1) | ((trow & 4) >> 1) | (trow & 1);
        const u32 rowa = LIMG + h * 16384 + trow * 128;
#pragma unroll
        for (int ch = 0; ch < 5; ch++) ldsw<u32x4>(rowa + ((ch ^ sw) << 4), u32x4{phik[4 * ch], phik[4 * ch + 1], phik[4 * ch + 2], phik[4 * ch + 3]});
#pragma unroll
        for (int mu = 0; mu < 5; mu++) {
            const int pc = 10 + (mu == 4 ? (h ? 0 : 4) : mu + h);
            u32x2 v = mu < 4 ? u32x2{pk(acc[2][4 * mu], acc[2][4 * mu + 1]), pk(acc[2][4 * mu + 2], acc[2][4 * mu + 3])}
                             : u32x2{pk(acc[3][8], acc[3][9]), pk(acc[3][10], acc[3][11])};
            ldsw<u32x2>(rowa + (((pc >> 1) ^ sw) << 4) + (pc & 1) * 8, v);
        }
        ldsw<u32x2>(rowa + ((7 ^ sw) << 4) + 8, u32x2{0x3f80u, 0u});
    }
    STAMP(10);
    u32 woo[7];
#pragma unroll
    for (int i = 0; i < 7; i++) { u32 idx = 64 * i + l, row = (idx * 6554u) >> 16; woo[i] = (40 * w + row) * 1280 + (idx - row * 10) * 16; }
    const char* wob = (const char*)wo + (c2 >> 1) * 160;
    auto issue_wo = [&](int G) {
        const u32 d = (G % 3) * WOSLOT + w * 6400; const char* gp = wob + G * 160 * 1280;
#pragma unroll
        for (int i = 0; i < 6; i++) dma(d + i * 1024, woo[i], gp);
        dma_m(d + 6144, woo[6], gp, 0xffffu, 0u);
    };
    bar();
    issue_wo(2);
    {
        const int pcn = 4 * (g & 1) + p;
        const int pa = 10 + (pcn < 6 ? pcn : 5), pb = 5 * (w & 1) + (pcn < 5 ? pcn : 4);
        u32 aa[2], ab[2];
#pragma unroll
        for (int rd = 0; rd < 2; rd++) {
            const int row = 8 * (g >> 1) + 4 * rd + q;
            const u32 sw = ((row & 2) << 1) | ((row & 4) >> 1) | (row & 1);
            const u32 base = LIMG + (w >> 1) * 16384 + row * 128;
            aa[rd] = base + (((pa >> 1) ^ sw) << 4) + (pa & 1) * 8;
            ab[rd] = base + (((pb >> 1) ^ sw) << 4) + (pb & 1) * 8;
        }
        f32x16 kv = {};
#pragma unroll
        for (int ks = 0; ks < 8; ks++)
            kv = mfma32(cat(trd(aa[0] + ks * 2048), trd(aa[1] + ks * 2048)), cat(trd(ab[0] + ks * 2048), trd(ab[1] + ks * 2048)), kv);
        if ((l & 31) < 20) {
            const u32 lam = h * 20 + (l & 31), o = WS_PART + (c * 16 + r) * 4096 + w * 960;
            __builtin_amdgcn_raw_buffer_store_b128(u32x4{pk(kv[0], kv[1]), pk(kv[2], kv[3]), pk(kv[4], kv[5]), pk(kv[6], kv[7])}, rsw, lam * 16, o, 16);
            __builtin_amdgcn_raw_buffer_store_b64(u32x2{pk(kv[8], kv[9]), pk(kv[10], kv[11])}, rsw, 640 + lam * 8, o, 16);
        }
    }
    STAMP(11);
    u32 phiq[20];
    {
        float qr[20];
#pragma unroll
        for (int i = 0; i < 20; i++) qr[i] = i < 16 ? acc[0][i] : acc[3][i - 16];
        features(qr, wf, phiq);
#pragma unroll
        for (int i = 0; i < 20; i++) asm volatile("" : "+v"(phiq[i]));
    }
    STAMP(17);
    waitv<0>();
    asm volatile("" ::"v"(pfd));
    bar();
    post_flag(ws + WS_F1 + c * 4096, tag, tid, 16, r);
    STAMP(3);
    issue_wo(0); issue_wo(1);
    if (tid < 80) ldsw<u64>(LTAB + tid * TROW + 48, 0ull);
    STAMP(4);
    if (w == 0) wait_flags(ws + WS_F1 + c * 4096 + r * 256, tag, l, 16);
    bar();
    STAMP(5);
    {
        const int t = tid < 240 ? tid : 239;
        const int un = (t * 1093) >> 16, it = t - un * 60;
        u32x4 pv[16];
#pragma unroll
        for (int i = 0; i < 16; i++) pv[i] = __builtin_amdgcn_raw_buffer_load_b128(rsw, t * 16 + i * 4096, WS_PART + c * 65536, 16);
        float s[8] = {};
        u32 slo = 0x00003f80u, shi = 0x3f800000u;
        asm volatile("" : "+s"(slo), "+s"(shi));
#pragma unroll
        for (int i = 0; i < 16; i++)
#pragma unroll
            for (int e = 0; e < 4; e++) { s[2 * e] = dot2(pv[i][e], slo, s[2 * e]); s[2 * e + 1] = dot2(pv[i][e], shi, s[2 * e + 1]); }
        const int lam = it < 40 ? it : 2 * (it - 40);
        const int hh = lam >= 20, f = lam - 20 * hh;
        const u32 a0 = LTAB + (un >> 1) * THEAD + ((un & 1) * 20 + f) * TROW + hh * 8 + (it < 40 ? 0 : 32);
        ldsw<u32x2>(a0, u32x2{pk(s[0], s[1]), pk(s[2], s[3])});
        ldsw<u32x2>(a0 + (it < 40 ? 16 : TROW), u32x2{pk(s[4], s[5]), pk(s[6], s[7])});
    }
    bar();
    STAMP(6);
    {
        const u32 tb = LTAB + hk * THEAD + q * TROW;
        const u32 t0 = tb + (same ? rq : 6) * 8, t1 = tb + (same && rq < 2 ? 4 + rq : 6) * 8;
        f32x16 o0 = {}, o1 = {};
#pragma unroll
        for (int s = 0; s < 5; s++) {
            const s16x8 B = frag(phiq[4 * s], phiq[4 * s + 1], phiq[4 * s + 2], phiq[4 * s + 3]);
            o0 = mfma32(cat(trd(t0 + 8 * s * TROW), trd(t0 + (8 * s + 4) * TROW)), B, o0);
            o1 = mfma32(cat(trd(t1 + 8 * s * TROW), trd(t1 + (8 * s + 4) * TROW)), B, o1);
        }
        const float rden = __builtin_amdgcn_rcpf(o1[4] + 1e-6f);
        u32 ob[10];
#pragma unroll
        for (int i = 0; i < 10; i++) ob[i] = i < 8 ? pk(o0[2 * i] * rden, o0[2 * i + 1] * rden) : pk(o1[2 * i - 16] * rden, o1[2 * i - 15] * rden);
        const u32 o = WS_O + (r * 16 + c) * 10240 + w * 2560;
        __builtin_amdgcn_raw_buffer_store_b128(u32x4{ob[0], ob[1], ob[2], ob[3]}, rsw, l * 16, o, 16);
        __builtin_amdgcn_raw_buffer_store_b128(u32x4{ob[4], ob[5], ob[6], ob[7]}, rsw, l * 16 + 1024, o, 16);
        __builtin_amdgcn_raw_buffer_store_b64(u32x2{ob[8], ob[9]}, rsw, l * 8 + 2048, o, 16);
    }
    STAMP(12);
    waitv<0>();
    bar();
    post_flag(ws + WS_F2 + r * 4096, tag, tid, 16, c);
    STAMP(7);
    {
        constexpr int NPRE = 6;
        const int th = c2 & 1, cb = c2 >> 1, hp = g & 1, P = g >> 1;
        u32 b1 = (20 * hp + 8 * P + q) * 160 + p * 8, b2 = (80 * P + 20 * hp + 16 + q) * 160 + p * 8;
        u32 b1h = b1 + 2 * WOSLOT, b2h = b2 + 2 * WOSLOT;
        asm volatile("" : "+v"(b1), "+v"(b2), "+v"(b1h), "+v"(b2h));
        s16x4 wfr[NPRE][10];
        auto rdw = [&](int ks, int n) {
            const int G = ks / 5, i = ks % 5, sl = G % 3, t = n >> 1, rd = n & 1;
            const u32 so = sl == 2 ? 0 : sl * WOSLOT;
            wfr[ks % NPRE][n] = i < 4 ? trd((sl == 2 ? b1h : b1) + so + (40 * i + 4 * rd) * 160 + 32 * t) : trd((sl == 2 ? b2h : b2) + so + 40 * rd * 160 + 32 * t);
        };
#pragma unroll
        for (int ks = 0; ks < NPRE; ks++)
#pragma unroll
            for (int n = 0; n < 10; n++) rdw(ks, n);
        bar();
        issue_wo(3);
        if (w == 0) wait_flags(ws + WS_F2 + r2 * 4096 + c2 * 256, tag, l, 16);
        waitv<0>();
        bar();
        STAMP(8);
        const int ti = 16 * (w & 1) + (l & 15);
        const u32 ob = WS_O + r2 * 16 * 10240 + (2 * th + (w >> 1)) * 2560;
        const u32 v1 = P * 1024 + (hp * 32 + ti) * 16, v2 = 2048 + 2 * P * 10240 + (hp * 32 + ti) * 8;
        u32x4 of[16]; u32x2 ot[8];
#pragma unroll
        for (int G = 0; G < 4; G++) {
#pragma unroll
            for (int i = 0; i < 4; i++) of[4 * G + i] = __builtin_amdgcn_raw_buffer_load_b128(rsw, v1 + (4 * G + i) * 10240, ob, 16);
#pragma unroll
            for (int i = 0; i < 2; i++) ot[2 * G + i] = __builtin_amdgcn_raw_buffer_load_b64(rsw, v2 + (4 * G + i) * 10240, ob, 16);
        }
        if (bid == 0 && tid == 0) __hip_atomic_store((GAS u64*)(ws + WS_EPOCH), tag, __ATOMIC_RELAXED, __HIP_MEMORY_SCOPE_AGENT);
        f32x4 oc[5] = {};
#pragma unroll
        for (int ks = 0; ks < 20; ks++) {
            const int G = ks / 5, i = ks % 5;
            const s16x8 B = i < 4 ? __builtin_bit_cast(s16x8, of[4 * G + i]) : frag(ot[2 * G][0], ot[2 * G][1], ot[2 * G + 1][0], ot[2 * G + 1][1]);
#pragma unroll
            for (int t = 0; t < 5; t++) {
                SB;
                mfma16i(oc[t], cat(wfr[ks % NPRE][2 * t], wfr[ks % NPRE][2 * t + 1]), B);
                SB;
                if (ks + NPRE < 20) { rdw(ks + NPRE, 2 * t); rdw(ks + NPRE, 2 * t + 1); }
            }
        }
        SB;
        u16* op = out + (size_t)(128 * r2 + 64 * th + 16 * w + (l & 15)) * 1280 + 640 + 80 * cb + 4 * g;
#pragma unroll
        for (int t = 0; t < 5; t++) *(u32x2*)(op + 16 * t) = u32x2{pk(oc[t][0], oc[t][1]), pk(oc[t][2], oc[t][3])};
    }
    STAMP(9);
}

DI f32x2 cmul(f32x2 a, f32x2 b) { const f32x2 t = a.xx * b; const f32x2 s = {-b.y, b.x}; return __builtin_elementwise_fma(a.yy, s, t); }
DI f32x2 csq(f32x2 a) { return f32x2{__builtin_fmaf(a.x, a.x, -a.y * a.y), 2.f * a.x * a.y}; }
DI f32x2 cexp(float a, float b) {
    const float m = __builtin_amdgcn_exp2f(a * 1.4426950408889634f);
    const float c1 = 0.15915494309189535f, c2 = 6.4206382e-9f;
    const float n = __builtin_rintf(b * c1);
    const float f = __builtin_fmaf(b, c2, __builtin_fmaf(b, c1, -n));
    return f32x2{m * __builtin_amdgcn_cosf(f), m * __builtin_amdgcn_sinf(f)};
}
constexpr int LU = 65536;
DI u32 kaddr(u32 e) { return ((e >> 5) << 8) + ((e & 31) << 1); }

constexpr int LPRM = LU;
DI void prologue_a(const u16* __restrict__ Are, const u16* __restrict__ Aim, const u16* __restrict__ Bre,
                   const u16* __restrict__ Bim, const u16* __restrict__ Cre, const u16* __restrict__ Cim,
                   const u16* __restrict__ logdt, const u16* __restrict__ Dskip) {
    const int ia = blockIdx.x - 256, mg = ia >> 3, xg = ia & 7;
    const int l = threadIdx.x & 63, w = __builtin_amdgcn_readfirstlane(threadIdx.x >> 6);
    const u32 po = (80 * xg + 4 * mg + w) * 256 + l * 4, pd = LPRM + w * 4096;
    dma4(pd, po, Are); dma4(pd + 256, po, Aim); dma4(pd + 512, po, Bre); dma4(pd + 768, po, Bim); dma4(pd + 1024, po, Cre); dma4(pd + 1280, po, Cim);
    const u32 so = (80 * xg + 4 * mg) * 2 + (l & 1) * 4;
    dma4(pd + 1536, so, logdt); dma4(pd + 1792, so, Dskip);
}

DI void path_a(const u16* __restrict__ u, u16* __restrict__ out, char* __restrict__ ws, u32 pfd STAMP_ARG) {
    const int ia = blockIdx.x - 256, mg = ia >> 3, d0 = 80 * (ia & 7) + 4 * mg;
    const int tid = threadIdx.x, l = tid & 63;
    const int w = __builtin_amdgcn_readfirstlane(tid >> 6);
    const int d = d0 + w;
    const u32 KB = w * 16384;
    const int tg = d0 >> 6, trb = (d0 & 63) >> 2;
    const auto rsw = __builtin_amdgcn_make_buffer_rsrc((void*)ws, 0, WS_END, 0x00020000);
    const int g = l >> 4, q = (l >> 2) & 3, p = l & 3, h = l >> 5;
    STAMP(1);
    u64 tag, hint;
    asm volatile("s_load_dwordx2 %0, %2, 0\n\ts_load_dwordx2 %1, %3, 0\n\ts_waitcnt vmcnt(0) lgkmcnt(0)" : "=&s"(tag), "=&s"(hint)
                 : "s"(ws + WS_EPOCH_A + tg * 128), "s"(ws + WS_EPOCH), "v"(pfd) : "memory");
    tag += 1; hint += 1;
    const float ldt = b2f(ldsr<u16>(LPRM + w * 4096 + 1536 + w * 2)), dsk = b2f(ldsr<u16>(LPRM + w * 4096 + 1792 + w * 2));
    u32 prm[6];
#pragma unroll
    for (int i = 0; i < 6; i++) prm[i] = ldsr<u32>(LPRM + w * 4096 + i * 256 + l * 4);
    {
        const char* ub = (const char*)u + (size_t)(128 * trb + 32 * w) * 1280 + tg * 128;
        waitl0();
#pragma unroll
        for (int i = 0; i < 4; i++) dma(LU + w * 4096 + i * 1024, (l >> 3) * 1280 + (((l & 7) ^ ((4 * w + i) & 7)) << 4), ub + i * 8 * 1280);
    }
    const float dt = lo2f(pk(expf(ldt), 0.f));
    STAMP(2);
    f32x16 kd = {};
    {
        u32 ra[2];
#pragma unroll
        for (int rd = 0; rd < 2; rd++) ra[rd] = KB + (8 * (g >> 1) + 4 * rd + q) * 64 + (((2 * (g & 1) + (p >> 1)) ^ ((2 * rd + (q >> 1)) & 3)) << 4) + (p & 1) * 8;
        const u32 wa = KB + l * 64, wsw = (l >> 1) & 3;
#pragma unroll
        for (int ps = 0; ps < 2; ps++) {
            const float a = dt * lo2f(prm[0]), b = dt * lo2f(prm[1]);
            const f32x2 dB = {dt * lo2f(prm[2]), dt * lo2f(prm[3])};
            const f32x2 Wc = cmul(f32x2{lo2f(prm[4]), lo2f(prm[5])}, dB);
#pragma unroll
            for (int i = 0; i < 6; i++) prm[i] >>= 16;
            if (ps == 0) { asm volatile("" :: "v"(a), "v"(Wc.x), "v"(Wc.y)); STAMP(4); }
            const f32x2 e1 = cexp(a, b), e16 = cexp(16.f * a, 16.f * b), e256 = cexp(256.f * a, 256.f * b);
            const f32x2 e2 = csq(e1), e4 = csq(e2), e8 = csq(e4), e32 = csq(e16), e64 = csq(e32), e128 = csq(e64), e512 = csq(e256), e1024 = csq(e512);
            if (ps == 1) {
                waitv<0>();
                bar();
                STAMP(14);
#pragma unroll
                for (int it = 0; it < 4; it++) {
                    const int cq = 4 * w + it, eta = 4 * g + p;
                    const u32 a = LU + (8 * eta + q) * 128 + (((cq >> 1) ^ (eta & 7)) << 4) + (cq & 1) * 8;
                    __builtin_amdgcn_raw_buffer_store_b128(__builtin_bit_cast(u32x4, cat(trd(a), trd(a + 512))), rsw,
                                                           (l & 3) * 256 + (l >> 2) * 16 + cq * 1024, WS_UT + (tg * 16 + trb) * 16384, 16);
                }
            }
            f32x2 X[32];
            X[0] = Wc; X[1] = cmul(Wc, e1); X[2] = cmul(Wc, e2); X[3] = cmul(X[1], e2);
#pragma unroll
            for (int i = 0; i < 4; i++) X[4 + i] = cmul(X[i], e4);
            X[8] = cmul(Wc, e256); X[9] = cmul(X[8], e2); X[10] = cmul(X[8], e4);
            X[12] = X[10]; X[13] = cmul(X[9], e4); X[14] = cmul(X[8], e8);
            X[11] = cmul(Wc, e512); X[16] = cmul(X[11], e4); X[17] = cmul(X[11], e8);
            X[15] = cmul(X[8], e512); X[20] = cmul(X[15], e4); X[21] = cmul(X[15], e8);
            X[18] = cmul(Wc, e1024); X[19] = cmul(X[18], e8);
            X[24] = cmul(X[8], e1024); X[25] = cmul(X[24], e8);
            X[22] = cmul(X[11], e1024); X[23] = cmul(X[22], e8);
            X[28] = cmul(X[15], e1024); X[29] = cmul(X[28], e8);
            X[26] = X[27] = X[30] = X[31] = f32x2{0.f, 0.f};
#pragma unroll
            for (int ch = 0; ch < 4; ch++) {
                const u32 o = (ch ^ wsw) << 4;
                ldsw<u32x4>(wa + o, u32x4{pk(X[8 * ch].x, X[8 * ch + 1].x), pk(X[8 * ch + 2].x, X[8 * ch + 3].x), pk(X[8 * ch + 4].x, X[8 * ch + 5].x), pk(X[8 * ch + 6].x, X[8 * ch + 7].x)});
                ldsw<u32x4>(wa + o + 4096, u32x4{pk(X[8 * ch].y, X[8 * ch + 1].y), pk(X[8 * ch + 2].y, X[8 * ch + 3].y), pk(X[8 * ch + 4].y, X[8 * ch + 5].y), pk(X[8 * ch + 6].y, X[8 * ch + 7].y)} ^ 0x80008000u);
            }
            f32x2 Y[8];
            Y[0] = f32x2{1.f, 0.f}; Y[1] = e8; Y[2] = e16; Y[3] = cmul(e8, e16); Y[4] = e32; Y[5] = cmul(e8, e32); Y[6] = cmul(e16, e32); Y[7] = cmul(Y[3], e32);
            const f32x2 e192 = cmul(e64, e128);
#pragma unroll
            for (int ch = 0; ch < 4; ch++) {
                f32x2 Z[8];
                const f32x2 m = ch == 1 ? e64 : ch == 2 ? e128 : e192;
#pragma unroll
                for (int i = 0; i < 8; i++) Z[i] = ch == 0 ? Y[i] : i == 0 ? m : cmul(Y[i], m);
                const u32 o = (ch ^ wsw) << 4;
                ldsw<u32x4>(wa + o + 8192, u32x4{pk(Z[0].x, Z[1].x), pk(Z[2].x, Z[3].x), pk(Z[4].x, Z[5].x), pk(Z[6].x, Z[7].x)});
                ldsw<u32x4>(wa + o + 12288, u32x4{pk(Z[0].y, Z[1].y), pk(Z[2].y, Z[3].y), pk(Z[4].y, Z[5].y), pk(Z[6].y, Z[7].y)});
            }
            if (ps == 0) STAMP(5);
#pragma unroll
            for (int ks = 0; ks < 8; ks++)
                kd = mfma32(cat(trd(ra[0] + ks * 1024), trd(ra[1] + ks * 1024)), cat(trd(ra[0] + ks * 1024 + 8192), trd(ra[1] + ks * 1024 + 8192)), kd);
            if (ps == 0) { asm volatile("" :: "v"(kd[0])); STAMP(6); }
        }
    }
    STAMP(7);
    waitv<0>();
    bar();
    post_flag(ws + WS_F3 + tg * 4096, tag, tid, 16, trb);
    STAMP(3);
    {
        const int j = l & 31;
        ldsw<u32x2>(KB + kaddr(2044 - 8 * j - 4 * h), u32x2{pk(kd[3], kd[2]), pk(kd[1], kd[0])});
        ldsw<u32x2>(KB + kaddr(2044 - 256 - 8 * j - 4 * h), u32x2{pk(kd[6], kd[5]), pk(kd[4], kd[4])});
        ldsw<u32x4>(KB + kaddr(2040 - 512 - 256 * h - 8 * j), u32x4{pk(kd[9], kd[9]), pk(kd[8], kd[8]), pk(kd[8], kd[7]), pk(kd[7], kd[7])});
#pragma unroll
        for (int sp = 0; sp < 2; sp++) {
            const float a = kd[10 + 2 * sp], b = kd[11 + 2 * sp];
            const u32 aa = pk(a, a), bb = pk(b, b);
            ldsw<u32x4>(KB + kaddr(2040 - 1024 - 512 * h - 256 * sp - 8 * j), u32x4{bb, (j & 1) ? bb : pk(b, a), aa, aa});
        }
#pragma unroll
        for (int it = 0; it < 4; it++) {
            const u32 i = it * 64 + l, a0 = KB + kaddr(8 * i);
            const u32x4 v = ldsr<u32x4>(a0);
            const u32x2 n = ldsr<u32x2>(KB + kaddr(8 * i + 8));
            const u32 s0 = __builtin_amdgcn_alignbit(v[1], v[0], 16), s1 = __builtin_amdgcn_alignbit(v[2], v[1], 16), s2 = __builtin_amdgcn_alignbit(v[3], v[2], 16),
                      s3 = __builtin_amdgcn_alignbit(n[0], v[3], 16), s4 = __builtin_amdgcn_alignbit(n[1], n[0], 16);
            ldsw<u32x4>(a0 + 64, u32x4{s0, s1, s2, s3});
            ldsw<u32x4>(a0 + 128, u32x4{v[1], v[2], v[3], n[0]});
            ldsw<u32x4>(a0 + 192, u32x4{s1, s2, s3, s4});
        }
    }
    STAMP(8);
    if (w == 0) wait_flags(ws + WS_F3 + tg * 4096 + trb * 256, tag, l, 16);
    bar();
    STAMP(9);
    {
        u32x4 uv[4];
#pragma unroll
        for (int it = 0; it < 4; it++)
            uv[it] = __builtin_amdgcn_raw_buffer_load_b128(rsw, (4 * it + g) * 16384 + (l & 15) * 16, WS_UT + tg * 262144 + (d & 63) * 256, 16);
        if (trb == 0 && tid == 0) __hip_atomic_store((GAS u64*)(ws + WS_EPOCH_A + tg * 128), tag, __ATOMIC_RELAXED, __HIP_MEMORY_SCOPE_AGENT);
#pragma unroll
        for (int it = 0; it < 4; it++) ldsw<u32x4>(LU + w * 4096 + it * 1024 + l * 16, uv[it]);
    }
    STAMP(10);
    f32x4 y[8] = {};
    const int rr = l & 15;
    {
        const u32 e0 = 2047 - 16 - rr + 8 * g, sh = e0 & 3;
        u32 ka0 = KB + kaddr(e0 - sh) + sh * 64 - 7 * 2048, ka1 = KB + kaddr(e0 - sh + 4) + sh * 64 - 7 * 2048;
        u32 ua = LU + w * 4096 + 32 * rr + 16 * g - 32;
        u32 mk[4];
#pragma unroll
        for (int i = 0; i < 4; i++) mk[i] = (2 * i > 16 + rr - 8 * g ? 0u : 0xffffu) | (2 * i + 1 > 16 + rr - 8 * g ? 0u : 0xffff0000u);
        int lim = 2 * rr + g - 2;
        u32x2 A[2][16]; u32x4 B[2][8];
        auto rdc = [&](int s, int n) {
            const int m = n / 3, k = n % 3;
            if (k == 0) B[s][m] = ldsr<u32x4>(ua + m * 512);
            else if (k == 1) A[s][2 * (7 - m)] = ldsr<u32x2>(ka0 + m * 2048);
            else A[s][2 * (7 - m) + 1] = ldsr<u32x2>(ka1 + m * 2048);
        };
        auto group = [&](int s, bool next) {
            A[s][0] &= u32x2{mk[0], mk[1]}; A[s][1] &= u32x2{mk[2], mk[3]};
            if (lim < 0) B[s][0] = u32x4{0, 0, 0, 0};
            ka0 -= 256; ka1 -= 256; ua -= 64; lim -= 4;
            mk[0] = mk[1] = mk[2] = mk[3] = 0xffffffffu;
            asm volatile("" : "+v"(ka0), "+v"(ka1), "+v"(ua));
            int n = 0;
#pragma unroll
            for (int m = 7; m >= 0; m--)
#pragma unroll
                for (int ct = 7 - m; ct >= 0; ct--) {
                    SB;
                    mfma16i(y[ct + m], frag(A[s][2 * m][0], A[s][2 * m][1], A[s][2 * m + 1][0], A[s][2 * m + 1][1]), __builtin_bit_cast(s16x8, B[s][ct]));
                    SB;
                    if (next && n < 24) rdc(s ^ 1, n);
                    n++;
                }
        };
        if (w == 0) {
            const GAS u64* hp = (const GAS u64*)(ws + WS_HINT + ia * 128);
#pragma unroll 1
            for (int s = 0; s < 10; s++) {
                if (__hip_atomic_load(hp, __ATOMIC_RELAXED, __HIP_MEMORY_SCOPE_AGENT) == hint) break;
                __builtin_amdgcn_s_sleep(HSLEEP);
            }
        }
        bar();
        STAMP(13);
        asm volatile("" : "+v"(ka0), "+v"(ka1), "+v"(ua));
#pragma unroll
        for (int n = 0; n < 24; n++) rdc(0, n);
#pragma unroll 1
        for (int gg = 0; gg < 3; gg++) { group(0, true); group(1, true); }
        group(0, true); group(1, false);
    }
    STAMP(11);
#pragma unroll
    for (int ct = 0; ct < 8; ct++) {
        const u32 a = LU + w * 4096 + ct * 512 + rr * 32 + g * 8;
        const u32x2 uu = ldsr<u32x2>(a);
        const u32 y0 = pk(y[ct][0], y[ct][1]), y1 = pk(y[ct][2], y[ct][3]);
        const u32 s0 = pk(lo2f(uu[0]) * dsk, hi2f(uu[0]) * dsk), s1 = pk(lo2f(uu[1]) * dsk, hi2f(uu[1]) * dsk);
        ldsw<u32x2>(a, u32x2{pk(lo2f(y0) + lo2f(s0), hi2f(y0) + hi2f(s0)), pk(lo2f(y1) + lo2f(s1), hi2f(y1) + hi2f(s1))});
    }
    bar();
    {
        u32x4 c[4];
#pragma unroll
        for (int i = 0; i < 4; i++) c[i] = ldsr<u32x4>(LU + i * 4096 + tid * 16);
        u16* op = out + (size_t)tid * 8 * 1280 + d0;
#pragma unroll
        for (int i = 0; i < 4; i++) {
            *(u32x2*)(op + (2 * i) * 1280) = u32x2{__builtin_amdgcn_perm(c[1][i], c[0][i], 0x05040100u), __builtin_amdgcn_perm(c[3][i], c[2][i], 0x05040100u)};
            *(u32x2*)(op + (2 * i + 1) * 1280) = u32x2{__builtin_amdgcn_perm(c[1][i], c[0][i], 0x07060302u), __builtin_amdgcn_perm(c[3][i], c[2][i], 0x07060302u)};
        }
    }
    STAMP(12);
}

#if defined(__HIP_DEVICE_COMPILE__)
__attribute__((target("no-packed-fp32-ops")))
#endif
__global__ void __launch_bounds__(256, 2)
fused_kernel(const u16* __restrict__ x, const u16* __restrict__ W, const u16* __restrict__ wphi, const u16* __restrict__ wo,
             u16* __restrict__ out, char* __restrict__ ws, const u16* __restrict__ u, const u16* __restrict__ Are,
             const u16* __restrict__ Aim, const u16* __restrict__ Bre, const u16* __restrict__ Bim, const u16* __restrict__ Cre,
             const u16* __restrict__ Cim, const u16* __restrict__ logdt, const u16* __restrict__ Dskip) {
    __shared__ __attribute__((aligned(16))) char smem[LDS_BYTES];
    asm volatile("" ::"s"((u32)(size_t)(LAS char*)smem));
    u64 stv[24] = {};
    STAMP(0);
    u32 pfd = 0;
    if (threadIdx.x < 8) {
        u64 pc; asm volatile("s_getpc_b64 %0" : "=s"(pc));
        asm volatile("global_load_dword %0, %1, %2" : "+v"(pfd) : "v"(min((((blockIdx.x >> 3) & 31) * 8 + threadIdx.x) * 128, CODE_PF_BYTES)), "s"(pc));
    }
    const bool is_a = blockIdx.x >= 256;
    if (is_a) prologue_a(Are, Aim, Bre, Bim, Cre, Cim, logdt, Dskip); else prologue_b(W);
    u32 bsel = blockIdx.x;
    asm volatile("" : "+s"(bsel));
    if (bsel >= 256) {
        __builtin_amdgcn_s_setprio(APRIO);
        path_a(u, out, ws, pfd STAMP_PASS);
    } else {
        __builtin_amdgcn_s_setprio(2);
        path_b(x, W, wphi, wo, out, ws, pfd STAMP_PASS);
    }
    if (blockIdx.x == 0) { u64 now; const u64 until = stv[9] + 5 * (stv[0] - stv[0]);
        do { asm volatile("s_memrealtime %0\n\ts_waitcnt lgkmcnt(0)" : "=s"(now)::"memory"); } while (now < until && now - stv[0] < 100000); }
}

extern "C" void kernel_launch(void* const* d_in, const int* in_sizes, int n_in, void* d_out, int out_size, void* d_ws, size_t ws_size,
                              hipStream_t stream) {
    const u16* const* in = (const u16* const*)d_in;
    fused_kernel<<<416, 256, 0, stream>>>(in[9], in[10], in[11], in[12], (u16*)d_out, (char*)d_ws, in[0], in[1], in[2], in[3], in[4], in[5],
                                          in[6], in[7], in[8]);
}
```

```cpp
#include <hip/hip_runtime.h>
#include <hip/hip_bf16.h>

typedef unsigned short u16;
typedef unsigned u32;
typedef unsigned long long u64;
typedef __attribute__((ext_vector_type(8))) short s16x8;
typedef __attribute__((ext_vector_type(4))) short s16x4;
typedef __attribute__((ext_vector_type(16))) float f32x16;
typedef __attribute__((ext_vector_type(4))) float f32x4;
typedef __attribute__((ext_vector_type(2))) float f32x2;
typedef __attribute__((ext_vector_type(4))) u32 u32x4;
typedef __attribute__((ext_vector_type(2))) u32 u32x2;
typedef __attribute__((ext_vector_type(2))) __bf16 bf16x2;
#define LAS __attribute__((address_space(3)))
#define GAS __attribute__((address_space(1)))
#define DI __device__ __forceinline__
#define SB __builtin_amdgcn_sched_barrier(0)
DI unsigned sbid() { return __builtin_amdgcn_workgroup_id_x(); }

#define PSLEEP 4
#define STW 0
#define UPOS 2
#define WOPOS 0
#define YMODE 0
constexpr int LDS_BYTES = 163840;
constexpr u32 WS_EPOCH = 0, WS_ACK = 4096, WS_F1 = 65536, WS_F2 = 131072, WS_PART = 524288, WS_O = 2u << 20, WS_END = 5u << 20;
constexpr u32 WS_STAMP = 16u << 20;
constexpr u32 kSpinCap = 1u << 18;
constexpr u32 CODE_PF_BYTES = 34816;

DI u32 pk(float a, float b) { f32x2 v = {a, b}; return __builtin_bit_cast(u32, __builtin_convertvector(v, bf16x2)); }
DI float lo2f(u32 x) { return __uint_as_float(x << 16); }
DI float hi2f(u32 x) { return __uint_as_float(x & 0xffff0000u); }
DI float b2f(u16 h) { return __uint_as_float((u32)h << 16); }
DI s16x4 trd(u32 a) { return __builtin_amdgcn_ds_read_tr16_b64_v4i16((LAS s16x4*)(size_t)a); }
DI s16x8 cat(s16x4 a, s16x4 b) { return __builtin_shufflevector(a, b, 0, 1, 2, 3, 4, 5, 6, 7); }
DI s16x8 frag(u32 a, u32 b, u32 c, u32 d) { u32x4 v = {a, b, c, d}; return __builtin_bit_cast(s16x8, v); }
template <class T> DI T ldsr(u32 a) { return *(LAS T*)(size_t)a; }
template <class T> DI void ldsw(u32 a, T v) { *(LAS T*)(size_t)a = v; }
template <int N> DI void waitv() { asm volatile("s_waitcnt vmcnt(%0)" ::"i"(N) : "memory"); }
DI void bar() { asm volatile("s_waitcnt lgkmcnt(0)\n\ts_barrier" ::: "memory"); }
DI f32x16 mfma32(s16x8 a, s16x8 b, f32x16 c) { return __builtin_amdgcn_mfma_f32_32x32x16_bf16(a, b, c, 0, 0, 0); }
DI f32x4 mfma16(s16x8 a, s16x8 b, f32x4 c) { return __builtin_amdgcn_mfma_f32_16x16x32_bf16(a, b, c, 0, 0, 0); }
DI float max3(float a, float b, float c) { float r; asm("v_max3_f32 %0, %1, %2, %3" : "=v"(r) : "v"(a), "v"(b), "v"(c)); return r; }
DI float min3(float a, float b, float c) { float r; asm("v_min3_f32 %0, %1, %2, %3" : "=v"(r) : "v"(a), "v"(b), "v"(c)); return r; }
DI float dot2(u32 x, u32 sel, float c) { return __builtin_amdgcn_fdot2_f32_bf16(__builtin_bit_cast(bf16x2, sel), __builtin_bit_cast(bf16x2, x), c, false); }
template <class R> DI void st128(u32x4 v, R rs, u32 voff, u32 soff, int) { __builtin_amdgcn_raw_buffer_store_b128(v, rs, voff + soff, 0, 16); }
DI void dma(u32 m0v, u32 voff, const void* sbase) {
    asm volatile("s_mov_b32 m0, %0\n\ts_nop 0\n\tglobal_load_lds_dwordx4 %1, %2" ::"s"(m0v), "v"(voff), "s"(sbase) : "memory");
}
DI void dma_m(u32 m0v, u32 voff, const void* sbase, u32 mlo, u32 mhi) {
    asm volatile("s_mov_b32 exec_lo, %3\n\ts_mov_b32 exec_hi, %4\n\ts_mov_b32 m0, %0\n\ts_nop 0\n\tglobal_load_lds_dwordx4 %1, %2\n\ts_mov_b64 exec, -1"
                 ::"s"(m0v), "v"(voff), "s"(sbase), "i"(mlo), "i"(mhi) : "memory");
}
#define STAMP(i)
#define STAMP_ARG
#define STAMP_PASS

DI void post_flag(char* fl, u64 tag, int tid, int n, int me) {
    if (tid < n) __hip_atomic_store((GAS u64*)(fl + tid * 256 + me * 8), tag, __ATOMIC_RELAXED, __HIP_MEMORY_SCOPE_AGENT);
}
DI void wait_flags(const char* fl, u64 tag, int l, int n) {
    const GAS u64* p = (const GAS u64*)(fl + (l < n ? l : 0) * 8);
    u64 v0 = __hip_atomic_load(p, __ATOMIC_RELAXED, __HIP_MEMORY_SCOPE_AGENT);
#pragma unroll 1
    for (u32 s = 0; s < kSpinCap; s++) {
        __builtin_amdgcn_s_sleep(PSLEEP);
        const u64 v1 = __hip_atomic_load(p, __ATOMIC_RELAXED, __HIP_MEMORY_SCOPE_AGENT);
        if (__all(v0 == tag)) break;
        v0 = v1;
    }
}

template <class F> DI void features(const float (&v)[20], const s16x8 (&wf)[2][3], u32 (&phi)[20], F filler) {
    u32 b[10];
#pragma unroll
    for (int i = 0; i < 10; i++) b[i] = pk(v[2 * i], v[2 * i + 1]);
    const s16x8 B0 = frag(b[0], b[1], b[2], b[3]), B1 = frag(b[4], b[5], b[6], b[7]), B2 = frag(b[8], b[9], 0, 0);
    f32x16 p0 = {}, p1 = {};
    p0 = mfma32(wf[0][0], B0, p0); p1 = mfma32(wf[1][0], B0, p1);
    p0 = mfma32(wf[0][1], B1, p0); p1 = mfma32(wf[1][1], B1, p1);
    p0 = mfma32(wf[0][2], B2, p0); p1 = mfma32(wf[1][2], B2, p1);
    SB; filler(); SB;
    asm volatile("s_nop 11" : "+v"(p0), "+v"(p1));
    float p[20];
#pragma unroll
    for (int i = 0; i < 20; i++) p[i] = i < 16 ? p0[i] : p1[i - 16];
    float mxa = max3(p[0], p[1], p[2]), mna = min3(p[0], p[1], p[2]), mxb = max3(p[3], p[4], p[5]), mnb = min3(p[3], p[4], p[5]);
#pragma unroll
    for (int i = 6; i < 18; i += 4) {
        mxa = max3(mxa, p[i], p[i + 1]); mna = min3(mna, p[i], p[i + 1]);
        mxb = max3(mxb, p[i + 2], p[i + 3]); mnb = min3(mnb, p[i + 2], p[i + 3]);
    }
    mxa = max3(mxa, p[18], p[19]); mna = min3(mna, p[18], p[19]);
    const float mx = max3(mxa, mxb, mxb), mn = min3(mna, mnb, mnb);
    const float L2E = 1.4426950408889634f;
    const float cx = -mx * L2E, cn = mn * L2E;
    float e[40], s1, s2;
#pragma unroll
    for (int i = 0; i < 20; i++) {
        e[i] = __builtin_amdgcn_exp2f(__builtin_fmaf(p[i], L2E, cx));
        e[20 + i] = __builtin_amdgcn_exp2f(__builtin_fmaf(p[i], -L2E, cn));
        if (i == 0) { s1 = e[0]; s2 = e[20]; } else { s1 += e[i]; s2 += e[20 + i]; }
    }
    const float r1 = __builtin_amdgcn_rcpf(s1), r2 = __builtin_amdgcn_rcpf(s2);
#pragma unroll
    for (int i = 0; i < 10; i++) { phi[i] = pk(e[2 * i] * r1, e[2 * i + 1] * r1); phi[10 + i] = pk(e[20 + 2 * i] * r2, e[21 + 2 * i] * r2); }
}

constexpr int WBLK = 960, WSLOT = 8 * WBLK, XSLOT = 24576;
constexpr int LW = XSLOT, LIMG = 102400, LMISC = LIMG + 2 * XSLOT, LZERO = LMISC + 3200, LTAB = LZERO + 64, TROW = 56, THEAD = 40 * TROW;
DI int xslot(int j) { return j % 3 == 0 ? 0 : LIMG + (j % 3 - 1) * XSLOT; }
static_assert(LTAB + 80 * TROW <= LDS_BYTES, "LDS map");

struct Role { bool is_a; int r, c, ia; };
DI Role role_of(u32 bid) {
    const int xcd = bid & 7, j = bid >> 3, e = xcd & 1, m = xcd >> 1, nb = e ? 20 : 24, cq = e ? (j * 52) >> 8 : (j * 43) >> 8;
    Role R;
    R.is_a = j >= nb;
    R.r = __builtin_amdgcn_readfirstlane(e ? 6 + j - 5 * cq : j - 6 * cq); R.c = __builtin_amdgcn_readfirstlane(4 * m + cq);
    R.ia = __builtin_amdgcn_readfirstlane(20 * m + (e ? 8 : 0) + j - nb);
    return R;
}

DI void load_wo(const u16* wo, int cb, int wi, int nw, int l) {
    u32 off[5];
#pragma unroll
    for (int i = 0; i < 5; i++) { const u32 idx = 64 * i + l, row = (idx * 6554u) >> 16; off[i] = row * 1120 + idx * 16; }
#pragma unroll 1
    for (int gq = wi; gq < 20; gq += nw) {
        const char* gp = (const char*)wo + cb * 160 + gq * (32 * 1280);
#pragma unroll
        for (int i = 0; i < 5; i++) dma(gq * 5120 + i * 1024, off[i], gp);
    }
}

DI void out_proj(u16* __restrict__ out, char* __restrict__ ws, u64 tag STAMP_ARG) {
    const int bid = sbid(), tid = threadIdx.x, l = tid & 63, w = __builtin_amdgcn_readfirstlane(tid >> 6);
    const int t = 4 * (bid & 7) + (bid >> 6), cb = (bid >> 3) & 7, rs = (t * 43) >> 7, sub = t - 3 * rs;
    waitv<0>();
    bar();
    if (w >= 4) {
        return;
    }
    constexpr int NPRE = 6;
    const auto rsw = __builtin_amdgcn_make_buffer_rsrc((void*)ws, 0, WS_END, 0x00020000);
    const int g = l >> 4, q = (l >> 2) & 3, p = l & 3, hp = g & 1, P = g >> 1;
    u32 b1 = (20 * hp + 8 * P + q) * 160 + p * 8, b2 = (80 * P + 20 * hp + 16 + q) * 160 + p * 8;
    u32 b1h = b1 + 51200, b2h = b2 + 51200;
    asm volatile("" : "+v"(b1), "+v"(b2), "+v"(b1h), "+v"(b2h));
    s16x4 wfr[NPRE][10];
    auto rdw = [&](int ks, int n) {
        const int G = ks / 5, i = ks % 5, tt = n >> 1, rd = n & 1;
        const u32 so = (G & 1) * 25600;
        wfr[ks % NPRE][n] = i < 4 ? trd((G >> 1 ? b1h : b1) + so + (40 * i + 4 * rd) * 160 + 32 * tt) : trd((G >> 1 ? b2h : b2) + so + 40 * rd * 160 + 32 * tt);
    };
#pragma unroll
    for (int ks = 0; ks < NPRE; ks++)
#pragma unroll
        for (int n = 0; n < 10; n++) rdw(ks, n);
    if (w == 0) wait_flags(ws + WS_F2 + (rs * 24 + sub * 8 + cb) * 256, tag, l, 16);
    bar();
    STAMP(8);
    const int ti = 16 * (w & 1) + (l & 15);
    const u32 ob = WS_O + (rs * 96 + 2 * sub + (w >> 1)) * 2560;
    const u32 v1 = P * 1024 + (hp * 32 + ti) * 16, v2 = 2048 + 2 * P * 15360 + (hp * 32 + ti) * 8;
    u32x4 of[16]; u32x2 ot[8];
    auto ldo = [&](int n) {
        const int G = n / 6, i = n % 6;
        if (i < 4) of[4 * G + i] = __builtin_amdgcn_raw_buffer_load_b128(rsw, v1, ob + (4 * G + i) * 15360, 16);
        else ot[2 * G + i - 4] = __builtin_amdgcn_raw_buffer_load_b64(rsw, v2, ob + (4 * G + i - 4) * 15360, 16);
    };
#pragma unroll
    for (int n = 0; n < 10; n++) ldo(n);
    f32x4 oc[5] = {};
#pragma unroll
    for (int ks = 0; ks < 20; ks++) {
        const int G = ks / 5, i = ks % 5;
        const s16x8 B = i < 4 ? __builtin_bit_cast(s16x8, of[4 * G + i]) : frag(ot[2 * G][0], ot[2 * G][1], ot[2 * G + 1][0], ot[2 * G + 1][1]);
#pragma unroll
        for (int tt = 0; tt < 5; tt++) {
            SB;
            oc[tt] = mfma16(cat(wfr[ks % NPRE][2 * tt], wfr[ks % NPRE][2 * tt + 1]), B, oc[tt]);
            SB;
            if (ks + NPRE < 20) { rdw(ks + NPRE, 2 * tt); rdw(ks + NPRE, 2 * tt + 1); }
            if ((tt == 0 || tt == 2) && 10 + 2 * ks + (tt >> 1) < 24) ldo(10 + 2 * ks + (tt >> 1));
        }
    }
    SB;
    u16* op = out + (size_t)(64 * t + 16 * w + (l & 15)) * 1280 + 640 + 80 * cb + 4 * g;
#pragma unroll
    for (int tt = 0; tt < 5; tt++) *(u32x2*)(op + 16 * tt) = u32x2{pk(oc[tt][0], oc[tt][1]), pk(oc[tt][2], oc[tt][3])};
    STAMP(9);
    if (bid == 0 && w == 0) {
        wait_flags(ws + WS_ACK, tag, l, 64);
        wait_flags(ws + WS_ACK + 512, tag, l, 16);
        if (l == 0) __hip_atomic_store((GAS u64*)(ws + WS_EPOCH), tag, __ATOMIC_RELAXED, __HIP_MEMORY_SCOPE_AGENT);
    }
}

DI void loader_b(const u16* __restrict__ x, const u16* __restrict__ W, const u16* __restrict__ wo, int r, int c, int lw, u32& pfd) {
    const int l = threadIdx.x & 63;
    const int ph = (((r + 4 * c) & 15) * 10) >> 4;
    const int pl = l >> 4, idx = l & 15, q3 = (idx * 11) >> 5;
    const int q = pl < 3 ? idx >> 2 : q3 & 3, sg = pl < 3 ? pl : idx - 3 * q3, ch = pl < 3 ? (idx & 3) + ((idx >> 1) & 1) : 2;
    const u32 wl = q * 3840 + sg * 1280 + ch * 16 + (sg == 0 ? ((c & 1) ? 0 : 160) : 80);
    const u32 xo0 = (l >> 3) * 1280 + (((l & 7) ^ (l >> 4)) << 4), xo1 = ((l >> 3) + 8) * 1280 + (((l & 7) ^ ((l >> 4) | 4)) << 4);
    u32 xr[2][3];
#pragma unroll
    for (int i = 0; i < 6; i++) { const int row = 192 * r + 96 * (i / 3) + 48 * lw + 16 * (i % 3); xr[i / 3][i % 3] = (row < 2048 ? row : row - 64) * 1280; }
    const char* wb = (const char*)W + c * 80 - 80 + lw * (16 * 3840);
    auto ld_x = [&](int hx) {
        const int jx = hx >> 1, hh = hx & 1;
        int jr = jx + ph; if (jr >= 10) jr -= 10;
        const char* g = (const char*)x + jr * 128;
        const u32 d = xslot(jx) + (96 * hh + 48 * lw) * 128;
#pragma unroll
        for (int i = 0; i < 3; i++) { dma(d + i * 2048, xo0, g + xr[hh][i]); dma(d + i * 2048 + 1024, xo1, g + xr[hh][i]); }
    };
    auto ld_w = [&](int i) {
        int ir = i + 2 * ph; if (ir >= 20) ir -= 20;
        const char* g = wb + ir * (32 * 3840);
        const u32 d = LW + (i & 3) * WSLOT + lw * 4 * WBLK;
#pragma unroll
        for (int b = 0; b < 4; b++) dma_m(d + b * WBLK, wl, g + b * (4 * 3840), 0xffffffffu, 0x0fffffffu);
    };
    ld_w(0); ld_x(0); ld_x(1); ld_w(1); ld_x(2); ld_x(3);
    waitv<16>();
    bar();
    ld_w(2); ld_w(3); ld_x(4);
#pragma unroll
    for (int i = 0; i < 19; i++) {
        if (i == 0 || i == 2) waitv<26>(); else if (i <= 15) waitv<20>(); else if (i == 16) waitv<14>(); else if (i == 17) waitv<4>(); else waitv<2>();
        bar();
        if (i + 5 < 20) ld_x(i + 5);
        if (i + 4 < 20) ld_w(i + 4);
        if (i == 17) {
            const u32 ln = __builtin_amdgcn_readfirstlane(min((sbid() >> 3) * 256 + lw * 64, 6208u)) + l;
            asm volatile("global_load_dword %0, %1, %2\n\tglobal_load_dword %0, %1, %3" : "+v"(pfd) : "v"(ln * 128), "s"(wo), "s"((const char*)wo + 128 * 128));
        }
    }
}

DI u64 path_b(const u16* __restrict__ x, const u16* __restrict__ W, const u16* __restrict__ wphi, const u16* __restrict__ wo,
               u16* __restrict__ out, char* __restrict__ ws, int r, int c, u32& pfd STAMP_ARG) {
    const int tid = threadIdx.x, l = tid & 63;
    const int w = __builtin_amdgcn_readfirstlane(tid >> 6);
    const auto rsw = __builtin_amdgcn_make_buffer_rsrc((void*)ws, 0, WS_END, 0x00020000);
    const int g = l >> 4, q = (l >> 2) & 3, p = l & 3, h = l >> 5;
    const int rq = 2 * (g & 1) + (p >> 1), hk = g >> 1;
    const bool same = hk == (p & 1);
    u64 tag;
    u32 phiq[20];
    f32x16 acc[4] = {};
    s16x8 wfq[2][3];
    if (w >= 6) {
        loader_b(x, W, wo, r, c, w - 6, pfd);
        asm volatile("s_load_dwordx2 %0, %1, 0\n\ts_waitcnt lgkmcnt(0)" : "=&s"(tag) : "s"(ws + WS_EPOCH) : "memory");
    } else {
        if (w < 4) dma_m(LMISC + w * 800, l * 16, (const char*)wphi + ((c ^ (w >> 1)) * 2 + (w & 1)) * 800, 0xffffffffu, 0x3ffffu);
        if (tid < 2) ldsw<u64>(LZERO + tid * 8, 0ull);
        u32 bA = LW + (g >> 1) * 2 * WBLK + q * 64 + (p & 1) * 32 + (g & 1) * 16 + (p >> 1) * 8;
        u32 bD = LW + (g >> 1) * 2 * WBLK + 768 + (3 * q + 2 * (g & 1) + (p >> 1)) * 16 + (p & 1) * 8;
        asm volatile("" : "+v"(bA), "+v"(bD));
        u32 aX[4], aXh[4];
        const int trow = 32 * w + (l & 31);
#pragma unroll
        for (int k = 0; k < 4; k++) {
            aX[k] = trow * 128 + (((2 * k + h) ^ ((trow >> 1) & 7)) << 4); aXh[k] = aX[k] + LIMG;
            asm volatile("" : "+v"(aX[k]), "+v"(aXh[k]));
        }
        s16x4 fa[2][8]; s16x8 fb[2];
        auto rdB = [&](int s, int i, int ks) {
            const int sl = (i >> 1) % 3, k = (i & 1) * 2 + ks;
            fb[s] = sl == 0 ? ldsr<s16x8>(aX[k]) : ldsr<s16x8>(aXh[k] + (sl - 1) * XSLOT);
        };
        auto rdA = [&](int s, int i, int ks, int n) {
            const u32 o = (i & 3) * WSLOT + ks * 4 * WBLK + (n & 1) * WBLK;
            fa[s][n] = n < 6 ? trd(bA + o + (n >> 1) * 256) : trd(bD + o);
        };
        auto mm = [&](int s, int t) { acc[t] = mfma32(cat(fa[s][2 * t], fa[s][2 * t + 1]), fb[s], acc[t]); };
        s16x8 wfk[2][3];
        auto load_wf = [&](s16x8 (&wf)[2][3], u32 base) {
#pragma unroll
            for (int to = 0; to < 2; to++)
#pragma unroll
                for (int s = 0; s < 3; s++) {
                    s16x4 t[2];
#pragma unroll
                    for (int rd = 0; rd < 2; rd++) {
                        const int mu = 2 * s + rd;
                        if (mu < 5) {
                            int dq = mu + hk; if (mu == 4) dq = hk ? 0 : 4;
                            const bool ok = same && (to == 0 || rq == 0);
                            t[rd] = trd(ok ? base + hk * 800 + (4 * dq + q) * 40 + (4 * to + rq) * 8 : LZERO);
                        } else t[rd] = s16x4{0, 0, 0, 0};
                    }
                    wf[to][s] = cat(t[0], t[1]);
                }
        };
        asm volatile("s_load_dwordx2 %0, %1, 0\n\ts_waitcnt vmcnt(0) lgkmcnt(0)" : "=&s"(tag) : "s"(ws + WS_EPOCH), "v"(pfd) : "memory");
        bar();
        STAMP(1);
        rdB(0, 0, 0);
#pragma unroll
        for (int n = 0; n < 8; n++) rdA(0, 0, 0, n);
#pragma unroll
        for (int i = 0; i < 20; i++) {
            SB; mm(0, 0); SB; rdB(1, i, 1); rdA(1, i, 1, 0); rdA(1, i, 1, 1);
            SB; mm(0, 1); SB; rdA(1, i, 1, 2); rdA(1, i, 1, 3); rdA(1, i, 1, 4);
            SB; mm(0, 2); SB; rdA(1, i, 1, 5); rdA(1, i, 1, 6); rdA(1, i, 1, 7);
            SB; mm(0, 3); SB;
            if (i == 1) load_wf(wfk, LMISC);
            if (i == 2) load_wf(wfq, LMISC + 1600);
            SB; mm(1, 0); SB;
            if (i < 19) { bar(); rdB(0, i + 1, 0); rdA(0, i + 1, 0, 0); rdA(0, i + 1, 0, 1); }
            SB; mm(1, 1); SB;
            if (i < 19) { rdA(0, i + 1, 0, 2); rdA(0, i + 1, 0, 3); rdA(0, i + 1, 0, 4); }
            SB; mm(1, 2); SB;
            if (i < 19) { rdA(0, i + 1, 0, 5); rdA(0, i + 1, 0, 6); rdA(0, i + 1, 0, 7); }
            SB; mm(1, 3); SB;
        }
        STAMP(2);
        float kr[20]; u32 phik[20];
#pragma unroll
        for (int i = 0; i < 20; i++) kr[i] = i < 16 ? acc[1][i] : acc[3][i - 12];
        const u32 sw = ((trow & 2) << 1) | ((trow & 4) >> 1) | (trow & 1);
        const u32 rowa = LIMG + h * XSLOT + trow * 128;
        features(kr, wfk, phik, [&]() {
#pragma unroll
            for (int mu = 0; mu < 5; mu++) {
                const int pc = 10 + (mu == 4 ? (h ? 0 : 4) : mu + h);
                const u32x2 v = mu < 4 ? u32x2{pk(acc[2][4 * mu], acc[2][4 * mu + 1]), pk(acc[2][4 * mu + 2], acc[2][4 * mu + 3])}
                                       : u32x2{pk(acc[3][8], acc[3][9]), pk(acc[3][10], acc[3][11])};
                ldsw<u32x2>(rowa + (((pc >> 1) ^ sw) << 4) + (pc & 1) * 8, v);
            }
            ldsw<u32x2>(rowa + ((7 ^ sw) << 4) + 8, u32x2{0x3f80u, 0u});
        });
        const u32 keep = 192 * r + 32 * w < 2048 ? 0xffffffffu : 0u;
#pragma unroll
        for (int ch = 0; ch < 5; ch++) ldsw<u32x4>(rowa + ((ch ^ sw) << 4), u32x4{phik[4 * ch], phik[4 * ch + 1], phik[4 * ch + 2], phik[4 * ch + 3]} & keep);
        STAMP(10);
    }
    tag += 1;
    bar();
    if (WOPOS == 0 && w >= 6) load_wo(wo, (sbid() >> 3) & 7, w - 6, 2, l);
    if (w < 4) {
        const int pcn = 4 * (g & 1) + p;
        const int pa = 10 + (pcn < 6 ? pcn : 5), pb = 5 * (w & 1) + (pcn < 5 ? pcn : 4);
        u32 aa[2], ab[2];
#pragma unroll
        for (int rd = 0; rd < 2; rd++) {
            const int row = 8 * (g >> 1) + 4 * rd + q;
            const u32 sw = ((row & 2) << 1) | ((row & 4) >> 1) | (row & 1);
            const u32 base = LIMG + (w >> 1) * XSLOT + row * 128;
            aa[rd] = base + (((pa >> 1) ^ sw) << 4) + (pa & 1) * 8;
            ab[rd] = base + (((pb >> 1) ^ sw) << 4) + (pb & 1) * 8;
            asm volatile("" : "+v"(aa[rd]), "+v"(ab[rd]));
        }
        s16x4 ta[24], tb[24];
#pragma unroll
        for (int ks = 0; ks < 12; ks++) {
            ta[2 * ks] = trd(aa[0] + ks * 2048); ta[2 * ks + 1] = trd(aa[1] + ks * 2048);
            tb[2 * ks] = trd(ab[0] + ks * 2048); tb[2 * ks + 1] = trd(ab[1] + ks * 2048);
        }
        SB;
        f32x16 kv = {};
#pragma unroll
        for (int ks = 0; ks < 12; ks++) kv = mfma32(cat(ta[2 * ks], ta[2 * ks + 1]), cat(tb[2 * ks], tb[2 * ks + 1]), kv);
        if ((l & 31) < 20) {
            const u32 lam = h * 20 + (l & 31), o = WS_PART + (c * 16 + r) * 4096 + w * 960;
            st128(u32x4{pk(kv[0], kv[1]), pk(kv[2], kv[3]), pk(kv[4], kv[5]), pk(kv[6], kv[7])}, rsw, lam * 16, o, 16);
            __builtin_amdgcn_raw_buffer_store_b64(u32x2{pk(kv[8], kv[9]), pk(kv[10], kv[11])}, rsw, 640 + lam * 8, o, 16);
        }
    }
    STAMP(11);
    if (w < 6) {
        float qr[20];
#pragma unroll
        for (int i = 0; i < 20; i++) qr[i] = i < 16 ? acc[0][i] : acc[3][i - 16];
        features(qr, wfq, phiq, []() {});
#pragma unroll
        for (int i = 0; i < 20; i++) asm volatile("" : "+v"(phiq[i]));
        STAMP(17);
        waitv<0>();
    }
    bar();
    post_flag(ws + WS_F1 + c * 4096, tag, tid, 11, r);
    STAMP(3);
    if (WOPOS == 1 && w >= 6) load_wo(wo, (sbid() >> 3) & 7, w - 6, 2, l);
    if (tid < 80) ldsw<u64>(LTAB + tid * TROW + 48, 0ull);
    if (w == 0) wait_flags(ws + WS_F1 + (c ^ 1) * 4096 + r * 256, tag, l, 11);
    bar();
    STAMP(5);
    if (w < 4) {
        const int t = tid < 240 ? tid : 239;
        const int un = (t * 1093) >> 16, it = t - un * 60;
        u32x4 pv[11];
#pragma unroll
        for (int i = 0; i < 11; i++) pv[i] = __builtin_amdgcn_raw_buffer_load_b128(rsw, t * 16, WS_PART + (c ^ 1) * 65536 + i * 4096, 16);
        float s[8] = {};
        u32 slo = 0x00003f80u, shi = 0x3f800000u;
        asm volatile("" : "+s"(slo), "+s"(shi));
#pragma unroll
        for (int i = 0; i < 11; i++)
#pragma unroll
            for (int e = 0; e < 4; e++) { s[2 * e] = dot2(pv[i][e], slo, s[2 * e]); s[2 * e + 1] = dot2(pv[i][e], shi, s[2 * e + 1]); }
        const int lam = it < 40 ? it : 2 * (it - 40);
        const int hh = lam >= 20, f = lam - 20 * hh;
        const u32 a0 = LTAB + (un >> 1) * THEAD + ((un & 1) * 20 + f) * TROW + hh * 8 + (it < 40 ? 0 : 32);
        ldsw<u32x2>(a0, u32x2{pk(s[0], s[1]), pk(s[2], s[3])});
        ldsw<u32x2>(a0 + (it < 40 ? 16 : TROW), u32x2{pk(s[4], s[5]), pk(s[6], s[7])});
    }
    bar();
    STAMP(6);
    if (w < 6 && 192 * r + 32 * w < 2048) {
        const u32 tb = LTAB + hk * THEAD + q * TROW;
        u32 t0 = tb + (same ? rq : 6) * 8, t1 = tb + (same && rq < 2 ? 4 + rq : 6) * 8;
        asm volatile("" : "+v"(t0), "+v"(t1));
        f32x16 o0 = {}, o1 = {};
#pragma unroll
        for (int s = 0; s < 5; s++) {
            const s16x8 B = frag(phiq[4 * s], phiq[4 * s + 1], phiq[4 * s + 2], phiq[4 * s + 3]);
            o0 = mfma32(cat(trd(t0 + 8 * s * TROW), trd(t0 + (8 * s + 4) * TROW)), B, o0);
            o1 = mfma32(cat(trd(t1 + 8 * s * TROW), trd(t1 + (8 * s + 4) * TROW)), B, o1);
        }
        const float rden = __builtin_amdgcn_rcpf(o1[4] + 1e-6f);
        u32 ob[10];
#pragma unroll
        for (int i = 0; i < 10; i++) ob[i] = i < 8 ? pk(o0[2 * i] * rden, o0[2 * i + 1] * rden) : pk(o1[2 * i - 16] * rden, o1[2 * i - 15] * rden);
        const u32 o = WS_O + ((r * 16 + (c ^ 1)) * 6 + w) * 2560;
        st128(u32x4{ob[0], ob[1], ob[2], ob[3]}, rsw, l * 16, o, 16);
        st128(u32x4{ob[4], ob[5], ob[6], ob[7]}, rsw, l * 16 + 1024, o, 16);
        __builtin_amdgcn_raw_buffer_store_b64(u32x2{ob[8], ob[9]}, rsw, l * 8 + 2048, o, 16);
    }
    STAMP(12);
    waitv<0>();
    asm volatile("" ::"v"(pfd));
    bar();
    post_flag(ws + WS_F2 + r * (24 * 256), tag, tid, 24, c);
    STAMP(7);
    return tag;
}

DI f32x2 cmul(f32x2 a, f32x2 b) { const f32x2 t = a.xx * b; const f32x2 s = {-b.y, b.x}; return __builtin_elementwise_fma(a.yy, s, t); }
DI f32x2 csq(f32x2 a) { return f32x2{__builtin_fmaf(a.x, a.x, -a.y * a.y), 2.f * a.x * a.y}; }
DI f32x2 cexp(float a, float b) {
    const float m = __builtin_amdgcn_exp2f(a * 1.4426950408889634f);
    const float c1 = 0.15915494309189535f, c2 = 6.4206382e-9f;
    const float n = __builtin_rintf(b * c1);
    const float f = __builtin_fmaf(b, c2, __builtin_fmaf(b, c1, -n));
    return f32x2{m * __builtin_amdgcn_cosf(f), m * __builtin_amdgcn_sinf(f)};
}
template <bool V> struct Flag { static constexpr bool value = V; };
constexpr int LU = 131072;
DI u32 kaddr(u32 e) { return ((e >> 5) << 8) + ((e & 31) << 1); }

DI void prologue_a(int ia, const u16* __restrict__ Are, const u16* __restrict__ Aim, const u16* __restrict__ Bre, const u16* __restrict__ Bim,
                   const u16* __restrict__ Cre, const u16* __restrict__ Cim, const u16* __restrict__ logdt, const u16* __restrict__ Dskip) {
    const int l = threadIdx.x & 63, w = __builtin_amdgcn_readfirstlane(threadIdx.x >> 6);
    const u16* src = w == 0 ? Are : w == 1 ? Aim : w == 2 ? Bre : w == 3 ? Bim : w == 4 ? Cre : w == 5 ? Cim : w == 6 ? logdt : Dskip;
    if (w < 6) { dma(w * 16384 + 8192, ia * 2048 + l * 16, src); dma(w * 16384 + 9216, ia * 2048 + 1024 + l * 16, src); }
    else dma_m(w * 16384 + 8192, ia * 16, src, 1u, 0u);
}

DI u64 path_a(const u16* __restrict__ u, const u16* __restrict__ wo, u16* __restrict__ out, char* __restrict__ ws, int ia, u32 pfd STAMP_ARG) {
    const int tid = threadIdx.x, l = tid & 63;
    const int w = __builtin_amdgcn_readfirstlane(tid >> 6);
    const u32 KB = w * 16384;
    const int g = l >> 4, q = (l >> 2) & 3, p = l & 3, h = l >> 5;
    STAMP(1);
    u64 tag;
    asm volatile("s_load_dwordx2 %0, %1, 0\n\ts_waitcnt vmcnt(0) lgkmcnt(0)" : "=&s"(tag) : "s"(ws + WS_EPOCH), "v"(pfd) : "memory");
    tag += 1;
    bar();
    if (tid == 0) __hip_atomic_store((GAS u64*)(ws + WS_ACK + ia * 8), tag, __ATOMIC_RELAXED, __HIP_MEMORY_SCOPE_AGENT);
    const float ldt = b2f(ldsr<u16>(6 * 16384 + 8192 + w * 2)), dsk = b2f(ldsr<u16>(7 * 16384 + 8192 + w * 2));
    u32 prm[6];
#pragma unroll
    for (int i = 0; i < 6; i++) prm[i] = ldsr<u32>(i * 16384 + 8192 + w * 256 + l * 4);
    bar();
    u32x4 ur[4];
    auto u_in = [&]() {
#pragma unroll
        for (int i = 0; i < 4; i++) ur[i] = *(const u32x4*)((const char*)u + (size_t)(256 * ((w + ia) & 7) + 4 * l + i) * 1280 + ia * 16);
    };
    if (UPOS == 0) u_in();
    const float dt = lo2f(pk(expf(ldt), 0.f));
    STAMP(2);
    f32x16 kd = {};
    u32 ra[2];
#pragma unroll
    for (int rd = 0; rd < 2; rd++) ra[rd] = KB + (8 * (g >> 1) + 4 * rd + q) * 64 + (((2 * (g & 1) + (p >> 1)) ^ ((2 * rd + (q >> 1)) & 3)) << 4) + (p & 1) * 8;
    const u32 wa = KB + l * 64, wsw = (l >> 1) & 3;
#pragma unroll
    for (int ps = 0; ps < 2; ps++) {
        float pf[6];
#pragma unroll
        for (int i = 0; i < 6; i++) pf[i] = ps ? hi2f(prm[i]) : lo2f(prm[i]);
        const float a = dt * pf[0], b = dt * pf[1];
        const f32x2 Wc = cmul(f32x2{pf[4], pf[5]}, f32x2{dt * pf[2], dt * pf[3]});
        const f32x2 e1 = cexp(a, b), e16 = cexp(16.f * a, 16.f * b), e256 = cexp(256.f * a, 256.f * b);
        const f32x2 e2 = csq(e1), e4 = csq(e2), e8 = csq(e4), e32 = csq(e16), e64 = csq(e32), e128 = csq(e64), e512 = csq(e256), e1024 = csq(e512);
        f32x2 X[32];
        X[0] = Wc; X[1] = cmul(Wc, e1); X[2] = cmul(Wc, e2); X[3] = cmul(X[1], e2);
#pragma unroll
        for (int i = 0; i < 4; i++) X[4 + i] = cmul(X[i], e4);
        X[8] = cmul(Wc, e256); X[9] = cmul(X[8], e2); X[10] = cmul(X[8], e4);
        X[12] = X[10]; X[13] = cmul(X[9], e4); X[14] = cmul(X[8], e8);
        X[11] = cmul(Wc, e512); X[16] = cmul(X[11], e4); X[17] = cmul(X[11], e8);
        X[15] = cmul(X[8], e512); X[20] = cmul(X[15], e4); X[21] = cmul(X[15], e8);
        X[18] = cmul(Wc, e1024); X[19] = cmul(X[18], e8);
        X[24] = cmul(X[8], e1024); X[25] = cmul(X[24], e8);
        X[22] = cmul(X[11], e1024); X[23] = cmul(X[22], e8);
        X[28] = cmul(X[15], e1024); X[29] = cmul(X[28], e8);
        X[26] = X[27] = X[30] = X[31] = f32x2{0.f, 0.f};
        f32x2 Y[8];
        Y[0] = f32x2{1.f, 0.f}; Y[1] = e8; Y[2] = e16; Y[3] = cmul(e8, e16); Y[4] = e32; Y[5] = cmul(e8, e32); Y[6] = cmul(e16, e32); Y[7] = cmul(Y[3], e32);
        const f32x2 e192 = cmul(e64, e128);
#pragma unroll
        for (int ch = 0; ch < 4; ch++) {
            const f32x2* Xc = X + 8 * ch;
            const u32 o = wa + ((ch ^ wsw) << 4);
            ldsw<u32x4>(o, u32x4{pk(Xc[0].x, Xc[1].x), pk(Xc[2].x, Xc[3].x), pk(Xc[4].x, Xc[5].x), pk(Xc[6].x, Xc[7].x)});
            ldsw<u32x4>(o + 4096, u32x4{pk(Xc[0].y, Xc[1].y), pk(Xc[2].y, Xc[3].y), pk(Xc[4].y, Xc[5].y), pk(Xc[6].y, Xc[7].y)} ^ 0x80008000u);
            f32x2 Z[8];
            const f32x2 m = ch == 1 ? e64 : ch == 2 ? e128 : e192;
#pragma unroll
            for (int i = 0; i < 8; i++) Z[i] = ch == 0 ? Y[i] : i == 0 ? m : cmul(Y[i], m);
            ldsw<u32x4>(o + 8192, u32x4{pk(Z[0].x, Z[1].x), pk(Z[2].x, Z[3].x), pk(Z[4].x, Z[5].x), pk(Z[6].x, Z[7].x)});
            ldsw<u32x4>(o + 12288, u32x4{pk(Z[0].y, Z[1].y), pk(Z[2].y, Z[3].y), pk(Z[4].y, Z[5].y), pk(Z[6].y, Z[7].y)});
        }
        s16x4 fx[16], fy[16];
#pragma unroll
        for (int ks = 0; ks < 8; ks++) {
            fx[2 * ks] = trd(ra[0] + ks * 1024); fx[2 * ks + 1] = trd(ra[1] + ks * 1024);
            fy[2 * ks] = trd(ra[0] + ks * 1024 + 8192); fy[2 * ks + 1] = trd(ra[1] + ks * 1024 + 8192);
        }
        if (ps == 0) { STAMP(4); if (UPOS == 1) u_in(); }
#pragma unroll
        for (int ks = 0; ks < 8; ks++) kd = mfma32(cat(fx[2 * ks], fx[2 * ks + 1]), cat(fy[2 * ks], fy[2 * ks + 1]), kd);
    }
    if (UPOS == 2) u_in();
    STAMP(6);
    {
        const int j = l & 31;
        ldsw<u32x2>(KB + kaddr(2044 - 8 * j - 4 * h), u32x2{pk(kd[3], kd[2]), pk(kd[1], l == 0 ? kd[0] + dsk : kd[0])});
        ldsw<u32x2>(KB + kaddr(2044 - 256 - 8 * j - 4 * h), u32x2{pk(kd[6], kd[5]), pk(kd[4], kd[4])});
        ldsw<u32x4>(KB + kaddr(2040 - 512 - 256 * h - 8 * j), u32x4{pk(kd[9], kd[9]), pk(kd[8], kd[8]), pk(kd[8], kd[7]), pk(kd[7], kd[7])});
#pragma unroll
        for (int sp = 0; sp < 2; sp++) {
            const float a = kd[10 + 2 * sp], b = kd[11 + 2 * sp];
            const u32 aa = pk(a, a), bb = pk(b, b);
            ldsw<u32x4>(KB + kaddr(2040 - 1024 - 512 * h - 256 * sp - 8 * j), u32x4{bb, (j & 1) ? bb : pk(b, a), aa, aa});
        }
        u32x4 v[4]; u32x2 n[4];
        const u32 a0 = KB + kaddr(8 * l), a1 = KB + kaddr(8 * l + 8);
#pragma unroll
        for (int it = 0; it < 4; it++) { v[it] = ldsr<u32x4>(a0 + it * 4096); n[it] = ldsr<u32x2>(a1 + it * 4096); }
#pragma unroll
        for (int it = 0; it < 4; it++) {
            const u32 s0 = __builtin_amdgcn_alignbit(v[it][1], v[it][0], 16), s1 = __builtin_amdgcn_alignbit(v[it][2], v[it][1], 16),
                      s2 = __builtin_amdgcn_alignbit(v[it][3], v[it][2], 16), s3 = __builtin_amdgcn_alignbit(n[it][0], v[it][3], 16),
                      s4 = __builtin_amdgcn_alignbit(n[it][1], n[it][0], 16);
            ldsw<u32x4>(a0 + it * 4096 + 64, u32x4{s0, s1, s2, s3});
            ldsw<u32x4>(a0 + it * 4096 + 128, u32x4{v[it][1], v[it][2], v[it][3], n[it][0]});
            ldsw<u32x4>(a0 + it * 4096 + 192, u32x4{s1, s2, s3, s4});
        }
    }
    STAMP(5);
    {
        const u32 a = LU + (256 * ((w + ia) & 7) + 4 * l) * 2;
#pragma unroll
        for (int c2 = 0; c2 < 4; c2++) {
            ldsw<u32x2>(a + 2 * c2 * 4096, u32x2{__builtin_amdgcn_perm(ur[1][c2], ur[0][c2], 0x05040100u), __builtin_amdgcn_perm(ur[3][c2], ur[2][c2], 0x05040100u)});
            ldsw<u32x2>(a + (2 * c2 + 1) * 4096, u32x2{__builtin_amdgcn_perm(ur[1][c2], ur[0][c2], 0x07060302u), __builtin_amdgcn_perm(ur[3][c2], ur[2][c2], 0x07060302u)});
        }
    }
    STAMP(13);
    bar();
    STAMP(10);
    const int rr = l & 15;
    const u32 e0 = 2047 - 16 - rr + 8 * g, sh = e0 & 3;
    auto half = [&](auto HI) {
        constexpr bool hi = decltype(HI)::value;
        constexpr int NF = hi ? 8 : 4, T0 = hi ? 4 : 0, NRD = 3 * NF;
        f32x4 y[4] = {};
        u32 ka0 = KB + kaddr(e0 - sh) + sh * 64 - (NF - 1) * 2048, ka1 = KB + kaddr(e0 - sh + 4) + sh * 64 - (NF - 1) * 2048;
        u32 ua = LU + w * 4096 + 32 * rr + 16 * g - 32;
        u32 mk[4];
#pragma unroll
        for (int i = 0; i < 4; i++) mk[i] = (2 * i > 16 + rr - 8 * g ? 0u : 0xffffu) | (2 * i + 1 > 16 + rr - 8 * g ? 0u : 0xffff0000u);
        int lim = 2 * rr + g - 2;
        u32x2 A[2][2 * NF]; u32x4 B[2][NF];
        auto rdc = [&](int s, int n) {
            const int m = n / 3, k = n % 3;
            if (k == 0) B[s][m] = ldsr<u32x4>(ua + m * 512);
            else if (k == 1) A[s][2 * (NF - 1 - m)] = ldsr<u32x2>(ka0 + m * 2048);
            else A[s][2 * (NF - 1 - m) + 1] = ldsr<u32x2>(ka1 + m * 2048);
        };
        auto group = [&](int s, bool next) {
            A[s][0] &= u32x2{mk[0], mk[1]}; A[s][1] &= u32x2{mk[2], mk[3]};
            if (lim < 0) B[s][0] = u32x4{0, 0, 0, 0};
            ka0 -= 256; ka1 -= 256; ua -= 64; lim -= 4;
            mk[0] = mk[1] = mk[2] = mk[3] = 0xffffffffu;
            asm volatile("" : "+v"(ka0), "+v"(ka1), "+v"(ua));
            int n = 0;
#pragma unroll
            for (int m = NF - 1; m >= 0; m--)
#pragma unroll
                for (int ct = NF - 1 - m; ct >= 0 && ct + m >= T0; ct--) {
                    SB;
                    y[ct + m - T0] = mfma16(frag(A[s][2 * m][0], A[s][2 * m][1], A[s][2 * m + 1][0], A[s][2 * m + 1][1]), __builtin_bit_cast(s16x8, B[s][ct]), y[ct + m - T0]);
                    SB;
                    if (next && n < NRD) rdc(s ^ 1, n);
                    n++;
                    if (!hi && next && (n == 1 || n == 6)) rdc(s ^ 1, n++);
                }
        };
        asm volatile("" : "+v"(ka0), "+v"(ka1), "+v"(ua));
#pragma unroll
        for (int n = 0; n < NRD; n++) rdc(0, n);
#pragma unroll 1
        for (int gg = 0; gg < 3; gg++) { group(0, true); group(1, true); }
        group(0, true); group(1, false);
#pragma unroll
        for (int ct = 0; ct < 4; ct++) ldsw<u32x2>(LU + w * 4096 + (T0 + ct) * 512 + rr * 32 + g * 8, u32x2{pk(y[ct][0], y[ct][1]), pk(y[ct][2], y[ct][3])});
        if (hi) STAMP(14); else STAMP(16);
        bar();
        if (hi) STAMP(15); else STAMP(17);
        u32 v[8];
#pragma unroll
        for (int c2 = 0; c2 < 8; c2++) v[c2] = ldsr<u32>(LU + c2 * 4096 + T0 * 512 + tid * 4);
        u16* op = out + (size_t)(T0 * 256 + 2 * tid) * 1280 + 8 * ia;
        *(u32x4*)op = u32x4{__builtin_amdgcn_perm(v[1], v[0], 0x05040100u), __builtin_amdgcn_perm(v[3], v[2], 0x05040100u),
                            __builtin_amdgcn_perm(v[5], v[4], 0x05040100u), __builtin_amdgcn_perm(v[7], v[6], 0x05040100u)};
        *(u32x4*)(op + 1280) = u32x4{__builtin_amdgcn_perm(v[1], v[0], 0x07060302u), __builtin_amdgcn_perm(v[3], v[2], 0x07060302u),
                                     __builtin_amdgcn_perm(v[5], v[4], 0x07060302u), __builtin_amdgcn_perm(v[7], v[6], 0x07060302u)};
    };
    half(Flag<true>{});
    STAMP(11);
    half(Flag<false>{});
    STAMP(12);
    load_wo(wo, (sbid() >> 3) & 7, w, 8, l);
    STAMP(7);
    return tag;
}

#if defined(__HIP_DEVICE_COMPILE__) && defined(NOPK)
__attribute__((target("no-packed-fp32-ops")))
#endif
__global__ void __launch_bounds__(512, 2)
fused_kernel(const u16* __restrict__ x, const u16* __restrict__ W, const u16* __restrict__ wphi, const u16* __restrict__ wo,
             u16* __restrict__ out, char* __restrict__ ws, const u16* __restrict__ u, const u16* __restrict__ Are,
             const u16* __restrict__ Aim, const u16* __restrict__ Bre, const u16* __restrict__ Bim, const u16* __restrict__ Cre,
             const u16* __restrict__ Cim, const u16* __restrict__ logdt, const u16* __restrict__ Dskip) {
    __shared__ __attribute__((aligned(16))) char smem[LDS_BYTES];
    asm volatile("" ::"s"((u32)(size_t)(LAS char*)smem));
    u32 pfd = 0;
    if (threadIdx.x < 12) {
        u64 pc; asm volatile("s_getpc_b64 %0" : "=s"(pc));
        asm volatile("global_load_dword %0, %1, %2" : "+v"(pfd) : "v"(threadIdx.x * 128), "s"(pc + min((sbid() >> 3) * 1536, CODE_PF_BYTES - 1536)));
    }
    asm volatile("" ::"s"(x), "s"(W), "s"(wphi), "s"(wo), "s"(out), "s"(ws), "s"(u), "s"(Are), "s"(Aim), "s"(Bre), "s"(Bim), "s"(Cre), "s"(Cim),
                 "s"(logdt), "s"(Dskip));
    const Role R = role_of(sbid());
    u64 tag;
    if (R.is_a) {
        prologue_a(R.ia, Are, Aim, Bre, Bim, Cre, Cim, logdt, Dskip);
        tag = path_a(u, wo, out, ws, R.ia, pfd STAMP_PASS);
    } else {
        tag = path_b(x, W, wphi, wo, out, ws, R.r, R.c, pfd STAMP_PASS);
    }
    out_proj(out, ws, tag STAMP_PASS);
}

extern "C" void kernel_launch(void* const* d_in, const int* in_sizes, int n_in, void* d_out, int out_size, void* d_ws, size_t ws_size,
                              hipStream_t stream) {
    const u16* const* in = (const u16* const*)d_in;
    fused_kernel<<<256, 512, 0, stream>>>(in[9], in[10], in[11], in[12], (u16*)d_out, (char*)d_ws, in[0], in[1], in[2], in[3], in[4], in[5],
                                          in[6], in[7], in[8]);
}
```

```cpp
#include <hip/hip_runtime.h>
#include <hip/hip_bf16.h>

typedef unsigned short u16;
typedef unsigned u32;
typedef unsigned long long u64;
typedef __attribute__((ext_vector_type(8))) short s16x8;
typedef __attribute__((ext_vector_type(4))) short s16x4;
typedef __attribute__((ext_vector_type(16))) float f32x16;
typedef __attribute__((ext_vector_type(4))) float f32x4;
typedef __attribute__((ext_vector_type(2))) float f32x2;
typedef __attribute__((ext_vector_type(4))) u32 u32x4;
typedef __attribute__((ext_vector_type(2))) u32 u32x2;
typedef __attribute__((ext_vector_type(2))) __bf16 bf16x2;
#define LAS __attribute__((address_space(3)))
#define GAS __attribute__((address_space(1)))
#define DI __device__ __forceinline__
#define SB __builtin_amdgcn_sched_barrier(0)
DI unsigned sbid() { return __builtin_amdgcn_workgroup_id_x(); }

#define PSLEEP 4
#define STW 0
#define YSLEEP 12
#define UISS 1
#define UPOS 1
#define WOPOS 0
#define YMODE 0
constexpr int LDS_BYTES = 163840;
constexpr u32 WS_EPOCH = 0, WS_ACK = 4096, WS_F1 = 65536, WS_F2 = 131072, WS_PART = 524288, WS_O = 2u << 20, WS_END = 5u << 20;
constexpr u32 WS_STAMP = 16u << 20;
constexpr u32 kSpinCap = 1u << 18;
constexpr u32 CODE_PF_BYTES = 44032;

DI u32 pk(float a, float b) { f32x2 v = {a, b}; return __builtin_bit_cast(u32, __builtin_convertvector(v, bf16x2)); }
DI float lo2f(u32 x) { return __uint_as_float(x << 16); }
DI float hi2f(u32 x) { return __uint_as_float(x & 0xffff0000u); }
DI float b2f(u16 h) { return __uint_as_float((u32)h << 16); }
DI s16x4 trd(u32 a) { return __builtin_amdgcn_ds_read_tr16_b64_v4i16((LAS s16x4*)(size_t)a); }
DI s16x8 cat(s16x4 a, s16x4 b) { return __builtin_shufflevector(a, b, 0, 1, 2, 3, 4, 5, 6, 7); }
DI s16x8 frag(u32 a, u32 b, u32 c, u32 d) { u32x4 v = {a, b, c, d}; return __builtin_bit_cast(s16x8, v); }
template <class T> DI T ldsr(u32 a) { return *(LAS T*)(size_t)a; }
template <class T> DI void ldsw(u32 a, T v) { *(LAS T*)(size_t)a = v; }
template <int N> DI void waitv() { asm volatile("s_waitcnt vmcnt(%0)" ::"i"(N) : "memory"); }
DI void bar() { asm volatile("s_waitcnt lgkmcnt(0)\n\ts_barrier" ::: "memory"); }
DI f32x16 mfma32(s16x8 a, s16x8 b, f32x16 c) { return __builtin_amdgcn_mfma_f32_32x32x16_bf16(a, b, c, 0, 0, 0); }
DI f32x4 mfma16(s16x8 a, s16x8 b, f32x4 c) { return __builtin_amdgcn_mfma_f32_16x16x32_bf16(a, b, c, 0, 0, 0); }
DI float max3(float a, float b, float c) { float r; asm("v_max3_f32 %0, %1, %2, %3" : "=v"(r) : "v"(a), "v"(b), "v"(c)); return r; }
DI float min3(float a, float b, float c) { float r; asm("v_min3_f32 %0, %1, %2, %3" : "=v"(r) : "v"(a), "v"(b), "v"(c)); return r; }
DI float dot2(u32 x, u32 sel, float c) { return __builtin_amdgcn_fdot2_f32_bf16(__builtin_bit_cast(bf16x2, sel), __builtin_bit_cast(bf16x2, x), c, false); }
template <class R> DI void st128(u32x4 v, R rs, u32 voff, u32 soff, int) { __builtin_amdgcn_raw_buffer_store_b128(v, rs, voff + soff, 0, 16); }
DI void dma(u32 m0v, u32 voff, const void* sbase) {
    asm volatile("s_mov_b32 m0, %0\n\ts_nop 0\n\tglobal_load_lds_dwordx4 %1, %2" ::"s"(m0v), "v"(voff), "s"(sbase) : "memory");
}
DI void dma_m(u32 m0v, u32 voff, const void* sbase, u32 mlo, u32 mhi) {
    asm volatile("s_mov_b32 exec_lo, %3\n\ts_mov_b32 exec_hi, %4\n\ts_mov_b32 m0, %0\n\ts_nop 0\n\tglobal_load_lds_dwordx4 %1, %2\n\ts_mov_b64 exec, -1"
                 ::"s"(m0v), "v"(voff), "s"(sbase), "i"(mlo), "i"(mhi) : "memory");
}
#define STAMP(i)
#define STAMP_ARG
#define STAMP_PASS

DI void post_flag(char* fl, u64 tag, int tid, int n, int me) {
    if (tid < n) __hip_atomic_store((GAS u64*)(fl + tid * 256 + me * 8), tag, __ATOMIC_RELAXED, __HIP_MEMORY_SCOPE_AGENT);
}
DI void wait_flags(const char* fl, u64 tag, int l, int n) {
    const GAS u64* p = (const GAS u64*)(fl + (l < n ? l : 0) * 8);
    u64 v0 = __hip_atomic_load(p, __ATOMIC_RELAXED, __HIP_MEMORY_SCOPE_AGENT);
#pragma unroll 1
    for (u32 s = 0; s < kSpinCap; s++) {
        __builtin_amdgcn_s_sleep(PSLEEP);
        const u64 v1 = __hip_atomic_load(p, __ATOMIC_RELAXED, __HIP_MEMORY_SCOPE_AGENT);
        if (__all(v0 == tag)) break;
        v0 = v1;
    }
}

template <class F> DI void features(const float (&v)[20], const s16x8 (&wf)[2][3], u32 (&phi)[20], F filler) {
    u32 b[10];
#pragma unroll
    for (int i = 0; i < 10; i++) b[i] = pk(v[2 * i], v[2 * i + 1]);
    const s16x8 B0 = frag(b[0], b[1], b[2], b[3]), B1 = frag(b[4], b[5], b[6], b[7]), B2 = frag(b[8], b[9], 0, 0);
    f32x16 p0 = {}, p1 = {};
    p0 = mfma32(wf[0][0], B0, p0); p1 = mfma32(wf[1][0], B0, p1);
    p0 = mfma32(wf[0][1], B1, p0); p1 = mfma32(wf[1][1], B1, p1);
    p0 = mfma32(wf[0][2], B2, p0); p1 = mfma32(wf[1][2], B2, p1);
    SB; filler(); SB;
    asm volatile("s_nop 11" : "+v"(p0), "+v"(p1));
    float p[20];
#pragma unroll
    for (int i = 0; i < 20; i++) p[i] = i < 16 ? p0[i] : p1[i - 16];
    float mxa = max3(p[0], p[1], p[2]), mna = min3(p[0], p[1], p[2]), mxb = max3(p[3], p[4], p[5]), mnb = min3(p[3], p[4], p[5]);
#pragma unroll
    for (int i = 6; i < 18; i += 4) {
        mxa = max3(mxa, p[i], p[i + 1]); mna = min3(mna, p[i], p[i + 1]);
        mxb = max3(mxb, p[i + 2], p[i + 3]); mnb = min3(mnb, p[i + 2], p[i + 3]);
    }
    mxa = max3(mxa, p[18], p[19]); mna = min3(mna, p[18], p[19]);
    const float mx = max3(mxa, mxb, mxb), mn = min3(mna, mnb, mnb);
    const float L2E = 1.4426950408889634f;
    const float cx = -mx * L2E, cn = mn * L2E;
    float e[40], s1, s2;
#pragma unroll
    for (int i = 0; i < 20; i++) {
        e[i] = __builtin_amdgcn_exp2f(__builtin_fmaf(p[i], L2E, cx));
        e[20 + i] = __builtin_amdgcn_exp2f(__builtin_fmaf(p[i], -L2E, cn));
        if (i == 0) { s1 = e[0]; s2 = e[20]; } else { s1 += e[i]; s2 += e[20 + i]; }
    }
    const float r1 = __builtin_amdgcn_rcpf(s1), r2 = __builtin_amdgcn_rcpf(s2);
#pragma unroll
    for (int i = 0; i < 10; i++) { phi[i] = pk(e[2 * i] * r1, e[2 * i + 1] * r1); phi[10 + i] = pk(e[20 + 2 * i] * r2, e[21 + 2 * i] * r2); }
}

constexpr int WBLK = 960, WSLOT = 8 * WBLK, XSLOT = 24576;
constexpr int LW = XSLOT, LIMG = 102400, LMISC = LIMG + 2 * XSLOT, LZERO = LMISC + 3200, LTAB = LZERO + 64, TROW = 56, THEAD = 40 * TROW;
DI int xslot(int j) { return j % 3 == 0 ? 0 : LIMG + (j % 3 - 1) * XSLOT; }
static_assert(LTAB + 80 * TROW <= LDS_BYTES, "LDS map");

template <bool V> struct Flag { static constexpr bool value = V; };

struct Role { bool is_a; int r, c, ia; u32 nb; };
DI Role role_of(u32 bid) {
    const int xcd = bid & 7, j = bid >> 3, e = xcd & 1, m = xcd >> 1, nb = e ? 20 : 24, cq = e ? (j * 52) >> 8 : (j * 43) >> 8;
    Role R;
    R.is_a = j >= nb; R.nb = nb;
    R.r = __builtin_amdgcn_readfirstlane(e ? 6 + j - 5 * cq : j - 6 * cq); R.c = __builtin_amdgcn_readfirstlane(4 * m + cq);
    R.ia = __builtin_amdgcn_readfirstlane(20 * m + (e ? 8 : 0) + j - nb);
    return R;
}

DI void load_wo(const u16* wo, int cb, int wi, int end, int nw, int l) {
    u32 off[5];
#pragma unroll
    for (int i = 0; i < 5; i++) { const u32 idx = 64 * i + l, row = (idx * 6554u) >> 16; off[i] = row * 1120 + idx * 16; }
#pragma unroll 1
    for (int gq = wi; gq < end; gq += nw) {
        const char* gp = (const char*)wo + cb * 160 + gq * (32 * 1280);
#pragma unroll
        for (int i = 0; i < 5; i++) dma(gq * 5120 + i * 1024, off[i], gp);
    }
}

DI void out_proj(u16* __restrict__ out, char* __restrict__ ws, u64 tag, int ia STAMP_ARG) {
    const int bid = sbid(), tid = threadIdx.x, l = tid & 63, w = __builtin_amdgcn_readfirstlane(tid >> 6);
    const int t = 4 * (bid & 7) + (bid >> 6), cb = (bid >> 3) & 7, rs = (t * 43) >> 7, sub = t - 3 * rs;
    bar();
    if (w >= 4) {
        if (ia >= 0) {
            u32x2 v[8];
#pragma unroll
            for (int c2 = 0; c2 < 8; c2++) v[c2] = ldsr<u32x2>(131072 + c2 * 4096 + (tid - 256) * 8);
            u16* op = out + (size_t)(4 * (tid - 256)) * 1280 + 8 * ia;
            bar();
            __builtin_amdgcn_s_sleep(YSLEEP);
#pragma unroll
            for (int i = 0; i < 4; i++) {
                const u32 sel = (i & 1) ? 0x07060302u : 0x05040100u;
                *(u32x4*)(op + i * 1280) = u32x4{__builtin_amdgcn_perm(v[1][i >> 1], v[0][i >> 1], sel), __builtin_amdgcn_perm(v[3][i >> 1], v[2][i >> 1], sel),
                                                 __builtin_amdgcn_perm(v[5][i >> 1], v[4][i >> 1], sel), __builtin_amdgcn_perm(v[7][i >> 1], v[6][i >> 1], sel)};
            }
        }
        return;
    }
    constexpr int NPRE = 6;
    const auto rsw = __builtin_amdgcn_make_buffer_rsrc((void*)ws, 0, WS_END, 0x00020000);
    const int g = l >> 4, q = (l >> 2) & 3, p = l & 3, hp = g & 1, P = g >> 1;
    u32 b1 = (20 * hp + 8 * P + q) * 160 + p * 8, b2 = (80 * P + 20 * hp + 16 + q) * 160 + p * 8;
    u32 b1h = b1 + 51200, b2h = b2 + 51200;
    asm volatile("" : "+v"(b1), "+v"(b2), "+v"(b1h), "+v"(b2h));
    s16x4 wfr[NPRE][10];
    auto rdw = [&](int ks, int n) {
        const int G = ks / 5, i = ks % 5, tt = n >> 1, rd = n & 1;
        const u32 so = (G & 1) * 25600;
        wfr[ks % NPRE][n] = i < 4 ? trd((G >> 1 ? b1h : b1) + so + (40 * i + 4 * rd) * 160 + 32 * tt) : trd((G >> 1 ? b2h : b2) + so + 40 * rd * 160 + 32 * tt);
    };
#pragma unroll
    for (int ks = 0; ks < NPRE; ks++)
#pragma unroll
        for (int n = 0; n < 10; n++) rdw(ks, n);
    if (w == 0) wait_flags(ws + WS_F2 + (rs * 24 + sub * 8 + cb) * 256, tag, l, 16);
    bar();
    STAMP(8);
    const int ti = 16 * (w & 1) + (l & 15);
    const u32 ob = WS_O + (rs * 96 + 2 * sub + (w >> 1)) * 2560;
    const u32 v1 = P * 1024 + (hp * 32 + ti) * 16, v2 = 2048 + 2 * P * 15360 + (hp * 32 + ti) * 8;
    u32x4 of[16]; u32x2 ot[8];
    auto ldo = [&](int n) {
        const int G = n / 6, i = n % 6;
        if (i < 4) of[4 * G + i] = __builtin_amdgcn_raw_buffer_load_b128(rsw, v1, ob + (4 * G + i) * 15360, 16);
        else ot[2 * G + i - 4] = __builtin_amdgcn_raw_buffer_load_b64(rsw, v2, ob + (4 * G + i - 4) * 15360, 16);
    };
#pragma unroll
    for (int n = 0; n < 10; n++) ldo(n);
    f32x4 oc[5] = {};
#pragma unroll
    for (int ks = 0; ks < 20; ks++) {
        const int G = ks / 5, i = ks % 5;
        const s16x8 B = i < 4 ? __builtin_bit_cast(s16x8, of[4 * G + i]) : frag(ot[2 * G][0], ot[2 * G][1], ot[2 * G + 1][0], ot[2 * G + 1][1]);
#pragma unroll
        for (int tt = 0; tt < 5; tt++) {
            SB;
            oc[tt] = mfma16(cat(wfr[ks % NPRE][2 * tt], wfr[ks % NPRE][2 * tt + 1]), B, oc[tt]);
            SB;
            if (ks + NPRE < 20) { rdw(ks + NPRE, 2 * tt); rdw(ks + NPRE, 2 * tt + 1); }
            if ((tt == 0 || tt == 2) && 10 + 2 * ks + (tt >> 1) < 24) ldo(10 + 2 * ks + (tt >> 1));
        }
    }
    SB;
    u16* op = out + (size_t)(64 * t + 16 * w + (l & 15)) * 1280 + 640 + 80 * cb + 4 * g;
#pragma unroll
    for (int tt = 0; tt < 5; tt++) *(u32x2*)(op + 16 * tt) = u32x2{pk(oc[tt][0], oc[tt][1]), pk(oc[tt][2], oc[tt][3])};
    STAMP(9);
    if (bid == 0 && w == 0) {
        wait_flags(ws + WS_ACK, tag, l, 64);
        wait_flags(ws + WS_ACK + 512, tag, l, 16);
        if (l == 0) __hip_atomic_store((GAS u64*)(ws + WS_EPOCH), tag, __ATOMIC_RELAXED, __HIP_MEMORY_SCOPE_AGENT);
    }
}

template <bool FIRST>
DI void loader_b(const u16* __restrict__ x, const u16* __restrict__ W, const u16* __restrict__ wo, int r, int c, int lw, u32& pfd) {
    const int l = threadIdx.x & 63;
    const int ph = (((r + 4 * c) & 15) * 10) >> 4;
    const int pl = l >> 4, idx = l & 15, q3 = (idx * 11) >> 5;
    const int q = pl < 3 ? idx >> 2 : q3 & 3, sg = pl < 3 ? pl : idx - 3 * q3, ch = pl < 3 ? (idx & 3) + ((idx >> 1) & 1) : 2;
    const u32 wl = q * 3840 + sg * 1280 + ch * 16 + (sg == 0 ? ((c & 1) ? 0 : 160) : 80);
    const u32 xo0 = (l >> 3) * 1280 + (((l & 7) ^ (l >> 4)) << 4), xo1 = ((l >> 3) + 8) * 1280 + (((l & 7) ^ ((l >> 4) | 4)) << 4);
    u32 xr[2][3];
#pragma unroll
    for (int i = 0; i < 6; i++) { const int row = 192 * r + 96 * (i / 3) + 48 * lw + 16 * (i % 3); xr[i / 3][i % 3] = (row < 2048 ? row : row - 64) * 1280; }
    const char* wb = (const char*)W + c * 80 - 80 + lw * (16 * 3840);
    auto ld_x = [&](int hx) {
        const int jx = hx >> 1, hh = hx & 1;
        int jr = jx + ph; if (jr >= 10) jr -= 10;
        const char* g = (const char*)x + jr * 128;
        const u32 d = xslot(jx) + (96 * hh + 48 * lw) * 128;
#pragma unroll
        for (int i = 0; i < 3; i++) { dma(d + i * 2048, xo0, g + xr[hh][i]); dma(d + i * 2048 + 1024, xo1, g + xr[hh][i]); }
    };
    auto ld_w = [&](int i) {
        int ir = i + 2 * ph; if (ir >= 20) ir -= 20;
        const char* g = wb + ir * (32 * 3840);
        const u32 d = LW + (i & 3) * WSLOT + lw * 4 * WBLK;
#pragma unroll
        for (int b = 0; b < 4; b++) dma_m(d + b * WBLK, wl, g + b * (4 * 3840), 0xffffffffu, 0x0fffffffu);
    };
    if (FIRST) { ld_w(0); ld_x(0); ld_x(1); return; }
    ld_w(1); ld_x(2); ld_x(3);
    waitv<16>();
    bar();
    ld_w(2); ld_w(3); ld_x(4);
#pragma unroll
    for (int i = 0; i < 19; i++) {
        if (i == 0 || i == 2) waitv<26>(); else if (i <= 15) waitv<20>(); else if (i == 16) waitv<14>(); else if (i == 17) waitv<4>(); else waitv<2>();
        bar();
        if (i + 5 < 20) ld_x(i + 5);
        if (i + 4 < 20) ld_w(i + 4);
        if (i == 17) {
            const u32 ln = __builtin_amdgcn_readfirstlane(min((sbid() >> 3) * 256 + lw * 64, 6208u)) + l;
            asm volatile("global_load_dword %0, %1, %2\n\tglobal_load_dword %0, %1, %3" : "+v"(pfd) : "v"(ln * 128), "s"(wo), "s"((const char*)wo + 128 * 128));
        }
    }
}

DI u64 path_b(const u16* __restrict__ x, const u16* __restrict__ W, const u16* __restrict__ wphi, const u16* __restrict__ wo,
               u16* __restrict__ out, char* __restrict__ ws, int r, int c, u32& pfd STAMP_ARG) {
    const int tid = threadIdx.x, l = tid & 63;
    const int w = __builtin_amdgcn_readfirstlane(tid >> 6);
    const auto rsw = __builtin_amdgcn_make_buffer_rsrc((void*)ws, 0, WS_END, 0x00020000);
    const int g = l >> 4, q = (l >> 2) & 3, p = l & 3, h = l >> 5;
    const int rq = 2 * (g & 1) + (p >> 1), hk = g >> 1;
    const bool same = hk == (p & 1);
    u64 tag;
    u32 phiq[20];
    f32x16 acc[4] = {};
    s16x8 wfq[2][3];
    if (w >= 6) {
        loader_b<false>(x, W, wo, r, c, w - 6, pfd);
        asm volatile("s_load_dwordx2 %0, %1, 0\n\ts_waitcnt lgkmcnt(0)" : "=&s"(tag) : "s"(ws + WS_EPOCH) : "memory");
    } else {
        if (w < 4) dma_m(LMISC + w * 800, l * 16, (const char*)wphi + ((c ^ (w >> 1)) * 2 + (w & 1)) * 800, 0xffffffffu, 0x3ffffu);
        if (tid < 2) ldsw<u64>(LZERO + tid * 8, 0ull);
        u32 bA = LW + (g >> 1) * 2 * WBLK + q * 64 + (p & 1) * 32 + (g & 1) * 16 + (p >> 1) * 8;
        u32 bD = LW + (g >> 1) * 2 * WBLK + 768 + (3 * q + 2 * (g & 1) + (p >> 1)) * 16 + (p & 1) * 8;
        asm volatile("" : "+v"(bA), "+v"(bD));
        const int trow = 32 * w + (l & 31);
        s16x8 wfk[2][3];
        auto load_wf = [&](s16x8 (&wf)[2][3], u32 base) {
#pragma unroll
            for (int to = 0; to < 2; to++)
#pragma unroll
                for (int s = 0; s < 3; s++) {
                    s16x4 t[2];
#pragma unroll
                    for (int rd = 0; rd < 2; rd++) {
                        const int mu = 2 * s + rd;
                        if (mu < 5) {
                            int dq = mu + hk; if (mu == 4) dq = hk ? 0 : 4;
                            const bool ok = same && (to == 0 || rq == 0);
                            t[rd] = trd(ok ? base + hk * 800 + (4 * dq + q) * 40 + (4 * to + rq) * 8 : LZERO);
                        } else t[rd] = s16x4{0, 0, 0, 0};
                    }
                    wf[to][s] = cat(t[0], t[1]);
                }
        };
        asm volatile("s_load_dwordx2 %0, %1, 0\n\ts_waitcnt vmcnt(0) lgkmcnt(0)" : "=&s"(tag) : "s"(ws + WS_EPOCH), "v"(pfd) : "memory");
        bar();
        STAMP(1);
        const u32 sw = ((trow & 2) << 1) | ((trow & 4) >> 1) | (trow & 1);
        const u32 rowa = LIMG + h * XSLOT + trow * 128;
        auto gemm = [&](auto HV) {
            constexpr bool hv = decltype(HV)::value;
            constexpr int NB = hv ? 3 : 1, NM = hv ? 6 : 3, NR = hv ? 11 : 7;
            f32x16 vc[3] = {};
            u32 aX[4], aXh[4];
            const int tx = hv ? 32 * (w - 2) + (l & 31) : trow;
#pragma unroll
            for (int k = 0; k < 4; k++) {
                aX[k] = tx * 128 + (((2 * k + h) ^ ((tx >> 1) & 7)) << 4); aXh[k] = aX[k] + LIMG;
                asm volatile("" : "+v"(aX[k]), "+v"(aXh[k]));
            }
            s16x4 fa[2][8]; s16x8 fb[2][NB];
            auto rd = [&](int s, int i, int ks, int n) {
                const int nb = hv ? (n == 0 ? 1 : n == 9 ? 0 : n == 10 ? 2 : -1) : (n == 0 ? 0 : -1);
                if (nb >= 0) {
                    const int sl = (i >> 1) % 3, k = (i & 1) * 2 + ks;
                    fb[s][nb] = sl == 0 ? ldsr<s16x8>(aX[k] + nb * 8192) : ldsr<s16x8>(aXh[k] + (sl - 1) * XSLOT + nb * 8192);
                } else {
                    const int na = n <= 4 ? n - 1 : n <= 6 ? n + 1 : n - 3;
                    const u32 o = (i & 3) * WSLOT + ks * 4 * WBLK + (na & 1) * WBLK;
                    fa[s][na] = na < 6 ? trd(bA + o + (na >> 1) * 256) : trd(bD + o);
                }
            };
            auto mm = [&](int s, int t) {
                if (t < 3) { const int tt = t == 2 ? 3 : t; acc[tt] = mfma32(cat(fa[s][2 * tt], fa[s][2 * tt + 1]), fb[s][hv ? 1 : 0], acc[tt]); }
                else { const int nb = t == 3 ? 1 : t == 4 ? 0 : 2; vc[nb] = mfma32(cat(fa[s][4], fa[s][5]), fb[s][hv ? nb : 0], vc[nb]); }
            };
            auto halfstep = [&](int s, int i, int ks, bool next) {
#pragma unroll
                for (int t = 0; t < NM; t++) {
                    SB; mm(s, t); SB;
                    if (t == 0 && ks == 0 && next) bar();
                    if (next)
#pragma unroll
                        for (int n = 3 * t; n < 3 * t + 3 && n < NR; n++) rd(s ^ 1, i, ks, n);
                }
            };
#pragma unroll
            for (int n = 0; n < NR; n++) rd(0, 0, 0, n);
#pragma unroll
            for (int i = 0; i < 20; i++) {
                halfstep(0, i, 1, true);
                if (i == 1) load_wf(wfk, LMISC);
                if (i == 2) load_wf(wfq, LMISC + 1600);
                halfstep(1, i + 1, 0, i < 19);
            }
            if (hv) {
#pragma unroll
                for (int nb = 0; nb < 3; nb++)
#pragma unroll
                    for (int mu = 0; mu < 4; mu++) {
                        const int pc = 10 + mu + h;
                        ldsw<u32x2>(rowa + (nb - 1) * 8192 + (((pc >> 1) ^ sw) << 4) + (pc & 1) * 8,
                                    u32x2{pk(vc[nb][4 * mu], vc[nb][4 * mu + 1]), pk(vc[nb][4 * mu + 2], vc[nb][4 * mu + 3])});
                    }
            }
        };
        if ((w & 3) >= 2) gemm(Flag<true>{}); else gemm(Flag<false>{});
        STAMP(2);
        float kr[20]; u32 phik[20];
#pragma unroll
        for (int i = 0; i < 20; i++) kr[i] = i < 16 ? acc[1][i] : acc[3][i - 12];
        features(kr, wfk, phik, [&]() {
            const int pc = h ? 10 : 14;
            ldsw<u32x2>(rowa + (((pc >> 1) ^ sw) << 4) + (pc & 1) * 8, u32x2{pk(acc[3][8], acc[3][9]), pk(acc[3][10], acc[3][11])});
            ldsw<u32x2>(rowa + ((7 ^ sw) << 4) + 8, u32x2{0x3f80u, 0u});
        });
        const u32 keep = 192 * r + 32 * w < 2048 ? 0xffffffffu : 0u;
#pragma unroll
        for (int ch = 0; ch < 5; ch++) ldsw<u32x4>(rowa + ((ch ^ sw) << 4), u32x4{phik[4 * ch], phik[4 * ch + 1], phik[4 * ch + 2], phik[4 * ch + 3]} & keep);
        STAMP(10);
    }
    tag += 1;
    bar();
    if (w >= 6) load_wo(wo, (sbid() >> 3) & 7, w - 6, 10, 2, l);
    if (w < 4) {
        const int pcn = 4 * (g & 1) + p;
        const int pa = 10 + (pcn < 6 ? pcn : 5), pb = 5 * (w & 1) + (pcn < 5 ? pcn : 4);
        u32 aa[2], ab[2];
#pragma unroll
        for (int rd = 0; rd < 2; rd++) {
            const int row = 8 * (g >> 1) + 4 * rd + q;
            const u32 sw = ((row & 2) << 1) | ((row & 4) >> 1) | (row & 1);
            const u32 base = LIMG + (w >> 1) * XSLOT + row * 128;
            aa[rd] = base + (((pa >> 1) ^ sw) << 4) + (pa & 1) * 8;
            ab[rd] = base + (((pb >> 1) ^ sw) << 4) + (pb & 1) * 8;
            asm volatile("" : "+v"(aa[rd]), "+v"(ab[rd]));
        }
        s16x4 ta[24], tb[24];
#pragma unroll
        for (int ks = 0; ks < 12; ks++) {
            ta[2 * ks] = trd(aa[0] + ks * 2048); ta[2 * ks + 1] = trd(aa[1] + ks * 2048);
            tb[2 * ks] = trd(ab[0] + ks * 2048); tb[2 * ks + 1] = trd(ab[1] + ks * 2048);
        }
        SB;
        f32x16 kv = {};
#pragma unroll
        for (int ks = 0; ks < 12; ks++) kv = mfma32(cat(ta[2 * ks], ta[2 * ks + 1]), cat(tb[2 * ks], tb[2 * ks + 1]), kv);
        if ((l & 31) < 20) {
            const u32 lam = h * 20 + (l & 31), o = WS_PART + (c * 16 + r) * 4096 + w * 960;
            st128(u32x4{pk(kv[0], kv[1]), pk(kv[2], kv[3]), pk(kv[4], kv[5]), pk(kv[6], kv[7])}, rsw, lam * 16, o, 16);
            __builtin_amdgcn_raw_buffer_store_b64(u32x2{pk(kv[8], kv[9]), pk(kv[10], kv[11])}, rsw, 640 + lam * 8, o, 16);
        }
    }
    STAMP(11);
    if (w < 6) {
        float qr[20];
#pragma unroll
        for (int i = 0; i < 20; i++) qr[i] = i < 16 ? acc[0][i] : acc[3][i - 16];
        features(qr, wfq, phiq, []() {});
#pragma unroll
        for (int i = 0; i < 20; i++) asm volatile("" : "+v"(phiq[i]));
        STAMP(17);
        waitv<0>();
    }
    bar();
    post_flag(ws + WS_F1 + c * 4096, tag, tid, 11, r);
    STAMP(3);
    if (w >= 6) load_wo(wo, (sbid() >> 3) & 7, w + 4, 20, 2, l);
    if (tid < 80) ldsw<u64>(LTAB + tid * TROW + 48, 0ull);
    if (w == 0) wait_flags(ws + WS_F1 + (c ^ 1) * 4096 + r * 256, tag, l, 11);
    bar();
    STAMP(5);
    if (w < 4) {
        const int t = tid < 240 ? tid : 239;
        const int un = (t * 1093) >> 16, it = t - un * 60;
        u32x4 pv[11];
#pragma unroll
        for (int i = 0; i < 11; i++) pv[i] = __builtin_amdgcn_raw_buffer_load_b128(rsw, t * 16, WS_PART + (c ^ 1) * 65536 + i * 4096, 16);
        float s[8] = {};
        u32 slo = 0x00003f80u, shi = 0x3f800000u;
        asm volatile("" : "+s"(slo), "+s"(shi));
#pragma unroll
        for (int i = 0; i < 11; i++)
#pragma unroll
            for (int e = 0; e < 4; e++) { s[2 * e] = dot2(pv[i][e], slo, s[2 * e]); s[2 * e + 1] = dot2(pv[i][e], shi, s[2 * e + 1]); }
        const int lam = it < 40 ? it : 2 * (it - 40);
        const int hh = lam >= 20, f = lam - 20 * hh;
        const u32 a0 = LTAB + (un >> 1) * THEAD + ((un & 1) * 20 + f) * TROW + hh * 8 + (it < 40 ? 0 : 32);
        ldsw<u32x2>(a0, u32x2{pk(s[0], s[1]), pk(s[2], s[3])});
        ldsw<u32x2>(a0 + (it < 40 ? 16 : TROW), u32x2{pk(s[4], s[5]), pk(s[6], s[7])});
    }
    bar();
    STAMP(6);
    if (w < 6 && 192 * r + 32 * w < 2048) {
        const u32 tb = LTAB + hk * THEAD + q * TROW;
        u32 t0 = tb + (same ? rq : 6) * 8, t1 = tb + (same && rq < 2 ? 4 + rq : 6) * 8;
        asm volatile("" : "+v"(t0), "+v"(t1));
        f32x16 o0 = {}, o1 = {};
#pragma unroll
        for (int s = 0; s < 5; s++) {
            const s16x8 B = frag(phiq[4 * s], phiq[4 * s + 1], phiq[4 * s + 2], phiq[4 * s + 3]);
            o0 = mfma32(cat(trd(t0 + 8 * s * TROW), trd(t0 + (8 * s + 4) * TROW)), B, o0);
            o1 = mfma32(cat(trd(t1 + 8 * s * TROW), trd(t1 + (8 * s + 4) * TROW)), B, o1);
        }
        const float rden = __builtin_amdgcn_rcpf(o1[4] + 1e-6f);
        u32 ob[10];
#pragma unroll
        for (int i = 0; i < 10; i++) ob[i] = i < 8 ? pk(o0[2 * i] * rden, o0[2 * i + 1] * rden) : pk(o1[2 * i - 16] * rden, o1[2 * i - 15] * rden);
        const u32 o = WS_O + ((r * 16 + (c ^ 1)) * 6 + w) * 2560;
        st128(u32x4{ob[0], ob[1], ob[2], ob[3]}, rsw, l * 16, o, 16);
        st128(u32x4{ob[4], ob[5], ob[6], ob[7]}, rsw, l * 16 + 1024, o, 16);
        __builtin_amdgcn_raw_buffer_store_b64(u32x2{ob[8], ob[9]}, rsw, l * 8 + 2048, o, 16);
    }
    STAMP(12);
    waitv<0>();
    asm volatile("" ::"v"(pfd));
    bar();
    post_flag(ws + WS_F2 + r * (24 * 256), tag, tid, 24, c);
    STAMP(7);
    return tag;
}

DI f32x2 cmul(f32x2 a, f32x2 b) {
    f32x2 d;
    asm("v_pk_mul_f32 %0, %1, %2 op_sel_hi:[0,1]\n\tv_pk_fma_f32 %0, %1, %2, %0 op_sel:[1,1,0] op_sel_hi:[1,0,1] neg_lo:[0,1,0]" : "=&v"(d) : "v"(a), "v"(b));
    return d;
}
DI f32x2 cmulc(f32x2 a, f32x2 b) {
    f32x2 d;
    asm("v_pk_mul_f32 %0, %1, %2 op_sel_hi:[0,1] neg_hi:[0,1]\n\tv_pk_fma_f32 %0, %1, %2, %0 op_sel:[1,1,0] op_sel_hi:[1,0,1]" : "=&v"(d) : "v"(a), "v"(b));
    return d;
}
DI f32x2 csq(f32x2 a) { return cmul(a, a); }
DI f32x2 cexp(float a, float b) {
    const float m = __builtin_amdgcn_exp2f(a * 1.4426950408889634f);
    const float c1 = 0.15915494309189535f, c2 = 6.4206382e-9f;
    const float n = __builtin_rintf(b * c1);
    const float f = __builtin_fmaf(b, c2, __builtin_fmaf(b, c1, -n));
    return f32x2{m * __builtin_amdgcn_cosf(f), m * __builtin_amdgcn_sinf(f)};
}
constexpr int LU = 131072;
DI u32 kaddr(u32 e) { return ((e >> 5) << 8) + ((e & 31) << 1); }

DI void prologue_a(int ia, const u16* __restrict__ Are, const u16* __restrict__ Aim, const u16* __restrict__ Bre, const u16* __restrict__ Bim,
                   const u16* __restrict__ Cre, const u16* __restrict__ Cim, const u16* __restrict__ logdt, const u16* __restrict__ Dskip) {
    const int l = threadIdx.x & 63, w = __builtin_amdgcn_readfirstlane(threadIdx.x >> 6);
    const u16* src = w == 0 ? Are : w == 1 ? Aim : w == 2 ? Bre : w == 3 ? Bim : w == 4 ? Cre : w == 5 ? Cim : w == 6 ? logdt : Dskip;
    if (w < 6) { dma(w * 16384 + 8192, ia * 2048 + l * 16, src); dma(w * 16384 + 9216, ia * 2048 + 1024 + l * 16, src); }
    else dma_m(w * 16384 + 8192, ia * 16, src, 1u, 0u);
}

DI u64 path_a(const u16* __restrict__ u, const u16* __restrict__ wo, u16* __restrict__ out, char* __restrict__ ws, int ia, u32 pfd STAMP_ARG) {
    const int tid = threadIdx.x, l = tid & 63;
    const int w = __builtin_amdgcn_readfirstlane(tid >> 6);
    const u32 KB = w * 16384;
    const int g = l >> 4, q = (l >> 2) & 3, p = l & 3, h = l >> 5;
    STAMP(1);
    u64 tag;
    asm volatile("s_load_dwordx2 %0, %1, 0\n\ts_waitcnt vmcnt(0) lgkmcnt(0)" : "=&s"(tag) : "s"(ws + WS_EPOCH), "v"(pfd) : "memory");
    tag += 1;
    bar();
    if (tid == 0) __hip_atomic_store((GAS u64*)(ws + WS_ACK + ia * 8), tag, __ATOMIC_RELAXED, __HIP_MEMORY_SCOPE_AGENT);
    const float ldt = b2f(ldsr<u16>(6 * 16384 + 8192 + w * 2)), dsk = b2f(ldsr<u16>(7 * 16384 + 8192 + w * 2));
    u32 prm[6];
#pragma unroll
    for (int i = 0; i < 6; i++) prm[i] = ldsr<u32>(i * 16384 + 8192 + w * 256 + l * 4);
    bar();
    u32x4 ur[4];
    auto u_in = [&]() {
#pragma unroll
        for (int i = 0; i < 4; i++) ur[i] = *(const u32x4*)((const char*)u + (size_t)(256 * ((w + ia) & 7) + 4 * l + i) * 1280 + ia * 16);
    };
    if (UISS == 0) u_in();
    const float dt = lo2f(pk(expf(ldt), 0.f));
    STAMP(2);
    f32x16 kd = {};
    u32 ra[2];
#pragma unroll
    for (int rd = 0; rd < 2; rd++) ra[rd] = KB + (8 * (g >> 1) + 4 * rd + q) * 64 + (((2 * (g & 1) + (p >> 1)) ^ ((2 * rd + (q >> 1)) & 3)) << 4) + (p & 1) * 8;
    const u32 wa = KB + l * 64, wsw = (l >> 1) & 3;
#pragma unroll
    for (int ps = 0; ps < 2; ps++) {
        float pf[6];
#pragma unroll
        for (int i = 0; i < 6; i++) pf[i] = ps ? hi2f(prm[i]) : lo2f(prm[i]);
        const float a = dt * pf[0], b = dt * pf[1];
        const f32x2 Wc = cmulc(f32x2{pf[4], -pf[5]}, f32x2{dt * pf[2], dt * pf[3]});
        const f32x2 e1 = cexp(a, b), e16 = cexp(16.f * a, 16.f * b), e256 = cexp(256.f * a, 256.f * b);
        const f32x2 e2 = csq(e1), e4 = csq(e2), e8 = csq(e4), e32 = csq(e16), e64 = csq(e32), e128 = csq(e64), e512 = csq(e256), e1024 = csq(e512);
        f32x2 X[32];
        X[0] = Wc; X[1] = cmulc(Wc, e1); X[2] = cmulc(Wc, e2); X[3] = cmulc(X[1], e2);
#pragma unroll
        for (int i = 0; i < 4; i++) X[4 + i] = cmulc(X[i], e4);
        X[8] = cmulc(Wc, e256); X[9] = cmulc(X[8], e2); X[10] = cmulc(X[8], e4);
        X[12] = X[10]; X[13] = cmulc(X[9], e4); X[14] = cmulc(X[8], e8);
        X[11] = cmulc(Wc, e512); X[16] = cmulc(X[11], e4); X[17] = cmulc(X[11], e8);
        X[15] = cmulc(X[8], e512); X[20] = cmulc(X[15], e4); X[21] = cmulc(X[15], e8);
        X[18] = cmulc(Wc, e1024); X[19] = cmulc(X[18], e8);
        X[24] = cmulc(X[8], e1024); X[25] = cmulc(X[24], e8);
        X[22] = cmulc(X[11], e1024); X[23] = cmulc(X[22], e8);
        X[28] = cmulc(X[15], e1024); X[29] = cmulc(X[28], e8);
        X[26] = X[27] = X[30] = X[31] = f32x2{0.f, 0.f};
        f32x2 Y[8];
        Y[0] = f32x2{1.f, 0.f}; Y[1] = e8; Y[2] = e16; Y[3] = cmul(e8, e16); Y[4] = e32; Y[5] = cmul(e8, e32); Y[6] = cmul(e16, e32); Y[7] = cmul(Y[3], e32);
        const f32x2 e192 = cmul(e64, e128);
#pragma unroll
        for (int ch = 0; ch < 4; ch++) {
            const f32x2* Xc = X + 8 * ch;
            const u32 o = wa + ((ch ^ wsw) << 4);
            ldsw<u32x4>(o, u32x4{pk(Xc[0].x, Xc[1].x), pk(Xc[2].x, Xc[3].x), pk(Xc[4].x, Xc[5].x), pk(Xc[6].x, Xc[7].x)});
            ldsw<u32x4>(o + 4096, u32x4{pk(Xc[0].y, Xc[1].y), pk(Xc[2].y, Xc[3].y), pk(Xc[4].y, Xc[5].y), pk(Xc[6].y, Xc[7].y)});
            f32x2 Z[8];
            const f32x2 m = ch == 1 ? e64 : ch == 2 ? e128 : e192;
#pragma unroll
            for (int i = 0; i < 8; i++) Z[i] = ch == 0 ? Y[i] : i == 0 ? m : cmul(Y[i], m);
            ldsw<u32x4>(o + 8192, u32x4{pk(Z[0].x, Z[1].x), pk(Z[2].x, Z[3].x), pk(Z[4].x, Z[5].x), pk(Z[6].x, Z[7].x)});
            ldsw<u32x4>(o + 12288, u32x4{pk(Z[0].y, Z[1].y), pk(Z[2].y, Z[3].y), pk(Z[4].y, Z[5].y), pk(Z[6].y, Z[7].y)});
        }
        s16x4 fx[16], fy[16];
#pragma unroll
        for (int ks = 0; ks < 8; ks++) {
            fx[2 * ks] = trd(ra[0] + ks * 1024); fx[2 * ks + 1] = trd(ra[1] + ks * 1024);
            fy[2 * ks] = trd(ra[0] + ks * 1024 + 8192); fy[2 * ks + 1] = trd(ra[1] + ks * 1024 + 8192);
        }
        if (ps == 0) { STAMP(4); if (UISS == 1) u_in(); }
#pragma unroll
        for (int ks = 0; ks < 8; ks++) kd = mfma32(cat(fx[2 * ks], fx[2 * ks + 1]), cat(fy[2 * ks], fy[2 * ks + 1]), kd);
    }
    STAMP(6);
    {
        const int j = l & 31;
        ldsw<u32x2>(KB + kaddr(2044 - 8 * j - 4 * h), u32x2{pk(kd[3], kd[2]), pk(kd[1], l == 0 ? kd[0] + dsk : kd[0])});
        ldsw<u32x2>(KB + kaddr(2044 - 256 - 8 * j - 4 * h), u32x2{pk(kd[6], kd[5]), pk(kd[4], kd[4])});
        ldsw<u32x4>(KB + kaddr(2040 - 512 - 256 * h - 8 * j), u32x4{pk(kd[9], kd[9]), pk(kd[8], kd[8]), pk(kd[8], kd[7]), pk(kd[7], kd[7])});
#pragma unroll
        for (int sp = 0; sp < 2; sp++) {
            const float a = kd[10 + 2 * sp], b = kd[11 + 2 * sp];
            const u32 aa = pk(a, a), bb = pk(b, b);
            ldsw<u32x4>(KB + kaddr(2040 - 1024 - 512 * h - 256 * sp - 8 * j), u32x4{bb, (j & 1) ? bb : pk(b, a), aa, aa});
        }
        u32x4 v[4]; u32x2 n[4];
        const u32 a0 = KB + kaddr(8 * l), a1 = KB + kaddr(8 * l + 8);
#pragma unroll
        for (int it = 0; it < 4; it++) { v[it] = ldsr<u32x4>(a0 + it * 4096); n[it] = ldsr<u32x2>(a1 + it * 4096); }
#pragma unroll
        for (int it = 0; it < 4; it++) {
            const u32 s0 = __builtin_amdgcn_alignbit(v[it][1], v[it][0], 16), s1 = __builtin_amdgcn_alignbit(v[it][2], v[it][1], 16),
                      s2 = __builtin_amdgcn_alignbit(v[it][3], v[it][2], 16), s3 = __builtin_amdgcn_alignbit(n[it][0], v[it][3], 16),
                      s4 = __builtin_amdgcn_alignbit(n[it][1], n[it][0], 16);
            ldsw<u32x4>(a0 + it * 4096 + 64, u32x4{s0, s1, s2, s3});
            ldsw<u32x4>(a0 + it * 4096 + 128, u32x4{v[it][1], v[it][2], v[it][3], n[it][0]});
            ldsw<u32x4>(a0 + it * 4096 + 192, u32x4{s1, s2, s3, s4});
        }
    }
    STAMP(5);
    {
        const u32 a = LU + (256 * ((w + ia) & 7) + 4 * l) * 2;
#pragma unroll
        for (int c2 = 0; c2 < 4; c2++) {
            ldsw<u32x2>(a + 2 * c2 * 4096, u32x2{__builtin_amdgcn_perm(ur[1][c2], ur[0][c2], 0x05040100u), __builtin_amdgcn_perm(ur[3][c2], ur[2][c2], 0x05040100u)});
            ldsw<u32x2>(a + (2 * c2 + 1) * 4096, u32x2{__builtin_amdgcn_perm(ur[1][c2], ur[0][c2], 0x07060302u), __builtin_amdgcn_perm(ur[3][c2], ur[2][c2], 0x07060302u)});
        }
    }
    STAMP(3);
    bar();
    STAMP(10);
    const int rr = l & 15;
    const u32 e0 = 2047 - 16 - rr + 8 * g, sh = e0 & 3;
    auto half = [&](auto HI) {
        constexpr bool hi = decltype(HI)::value;
        constexpr int NF = hi ? 8 : 4, T0 = hi ? 4 : 0, NRD = 3 * NF;
        f32x4 y[4] = {};
        u32 ka0 = KB + kaddr(e0 - sh) + sh * 64 - (NF - 1) * 2048, ka1 = KB + kaddr(e0 - sh + 4) + sh * 64 - (NF - 1) * 2048;
        u32 ua = LU + w * 4096 + 32 * rr + 16 * g - 32;
        u32 mk[4];
#pragma unroll
        for (int i = 0; i < 4; i++) mk[i] = (2 * i > 16 + rr - 8 * g ? 0u : 0xffffu) | (2 * i + 1 > 16 + rr - 8 * g ? 0u : 0xffff0000u);
        int lim = 2 * rr + g - 2;
        u32x2 A[2][2 * NF]; u32x4 B[2][NF];
        auto rdc = [&](int s, int n) {
            const int m = n / 3, k = n % 3;
            if (k == 0) B[s][m] = ldsr<u32x4>(ua + m * 512);
            else if (k == 1) A[s][2 * (NF - 1 - m)] = ldsr<u32x2>(ka0 + m * 2048);
            else A[s][2 * (NF - 1 - m) + 1] = ldsr<u32x2>(ka1 + m * 2048);
        };
        auto group = [&](int s, bool next) {
            A[s][0] &= u32x2{mk[0], mk[1]}; A[s][1] &= u32x2{mk[2], mk[3]};
            if (lim < 0) B[s][0] = u32x4{0, 0, 0, 0};
            ka0 -= 256; ka1 -= 256; ua -= 64; lim -= 4;
            mk[0] = mk[1] = mk[2] = mk[3] = 0xffffffffu;
            asm volatile("" : "+v"(ka0), "+v"(ka1), "+v"(ua));
            int n = 0;
#pragma unroll
            for (int m = NF - 1; m >= 0; m--)
#pragma unroll
                for (int ct = NF - 1 - m; ct >= 0 && ct + m >= T0; ct--) {
                    SB;
                    y[ct + m - T0] = mfma16(frag(A[s][2 * m][0], A[s][2 * m][1], A[s][2 * m + 1][0], A[s][2 * m + 1][1]), __builtin_bit_cast(s16x8, B[s][ct]), y[ct + m - T0]);
                    SB;
                    if (next && n < NRD) rdc(s ^ 1, n);
                    n++;
                    if (!hi && next && (n == 1 || n == 6)) rdc(s ^ 1, n++);
                }
        };
        asm volatile("" : "+v"(ka0), "+v"(ka1), "+v"(ua));
#pragma unroll
        for (int n = 0; n < NRD; n++) rdc(0, n);
#pragma unroll 1
        for (int gg = 0; gg < 3; gg++) { group(0, true); group(1, true); }
        group(0, true); group(1, false);
#pragma unroll
        for (int ct = 0; ct < 4; ct++) ldsw<u32x2>(LU + w * 4096 + (T0 + ct) * 512 + rr * 32 + g * 8, u32x2{pk(y[ct][0], y[ct][1]), pk(y[ct][2], y[ct][3])});
        if (hi) STAMP(14); else STAMP(16);
        bar();
        if (hi) STAMP(15); else STAMP(17);
        if (!hi) return;
        u32 v[8];
#pragma unroll
        for (int c2 = 0; c2 < 8; c2++) v[c2] = ldsr<u32>(LU + c2 * 4096 + T0 * 512 + tid * 4);
        u16* op = out + (size_t)(T0 * 256 + 2 * tid) * 1280 + 8 * ia;
        *(u32x4*)op = u32x4{__builtin_amdgcn_perm(v[1], v[0], 0x05040100u), __builtin_amdgcn_perm(v[3], v[2], 0x05040100u),
                            __builtin_amdgcn_perm(v[5], v[4], 0x05040100u), __builtin_amdgcn_perm(v[7], v[6], 0x05040100u)};
        *(u32x4*)(op + 1280) = u32x4{__builtin_amdgcn_perm(v[1], v[0], 0x07060302u), __builtin_amdgcn_perm(v[3], v[2], 0x07060302u),
                                     __builtin_amdgcn_perm(v[5], v[4], 0x07060302u), __builtin_amdgcn_perm(v[7], v[6], 0x07060302u)};
    };
    half(Flag<true>{});
    STAMP(11);
    half(Flag<false>{});
    STAMP(12);
    load_wo(wo, (sbid() >> 3) & 7, w, 20, 8, l);
    waitv<0>();
    STAMP(7);
    return tag;
}

__global__ void __launch_bounds__(512, 2)
fused_kernel(const u16* __restrict__ x, const u16* __restrict__ W, const u16* __restrict__ wphi, const u16* __restrict__ wo,
             u16* __restrict__ out, char* __restrict__ ws, const u16* __restrict__ u, const u16* __restrict__ Are,
             const u16* __restrict__ Aim, const u16* __restrict__ Bre, const u16* __restrict__ Bim, const u16* __restrict__ Cre,
             const u16* __restrict__ Cim, const u16* __restrict__ logdt, const u16* __restrict__ Dskip) {
    __shared__ __attribute__((aligned(16))) char smem[LDS_BYTES];
    asm volatile("" ::"s"((u32)(size_t)(LAS char*)smem));
    u32 pfd = 0;
    if (threadIdx.x < 12) {
        u64 pc; asm volatile("s_getpc_b64 %0" : "=s"(pc));
        asm volatile("global_load_dword %0, %1, %2" : "+v"(pfd) : "v"(threadIdx.x * 128), "s"(pc + min((sbid() >> 3) * 1536, CODE_PF_BYTES - 1536)));
    }
    asm volatile("" ::"s"(x), "s"(W), "s"(wphi), "s"(wo), "s"(out), "s"(ws), "s"(u), "s"(Are), "s"(Aim), "s"(Bre), "s"(Bim), "s"(Cre), "s"(Cim),
                 "s"(logdt), "s"(Dskip));
    const Role R = role_of(sbid());
    u64 tag;
    if (R.is_a) prologue_a(R.ia, Are, Aim, Bre, Bim, Cre, Cim, logdt, Dskip);
    else if (__builtin_amdgcn_readfirstlane(threadIdx.x) >= 384) loader_b<true>(x, W, wo, R.r, R.c, __builtin_amdgcn_readfirstlane(threadIdx.x >> 6) - 6, pfd);
    u32 sel = R.nb;
    asm volatile("" : "+s"(sel));
    if ((sbid() >> 3) >= sel) {
        tag = path_a(u, wo, out, ws, R.ia, pfd STAMP_PASS);
    } else {
        tag = path_b(x, W, wphi, wo, out, ws, R.r, R.c, pfd STAMP_PASS);
    }
    out_proj(out, ws, tag, R.is_a ? R.ia : -1 STAMP_PASS);
}

extern "C" void kernel_launch(void* const* d_in, const int* in_sizes, int n_in, void* d_out, int out_size, void* d_ws, size_t ws_size,
                              hipStream_t stream) {
    const u16* const* in = (const u16* const*)d_in;
    fused_kernel<<<256, 512, 0, stream>>>(in[9], in[10], in[11], in[12], (u16*)d_out, (char*)d_ws, in[0], in[1], in[2], in[3], in[4], in[5],
                                          in[6], in[7], in[8]);
}
```

```cpp
#define STAMPS
#include <hip/hip_runtime.h>
#include <hip/hip_bf16.h>

typedef unsigned short u16;
typedef unsigned u32;
typedef unsigned long long u64;
typedef __attribute__((ext_vector_type(8))) short s16x8;
typedef __attribute__((ext_vector_type(4))) short s16x4;
typedef __attribute__((ext_vector_type(16))) float f32x16;
typedef __attribute__((ext_vector_type(4))) float f32x4;
typedef __attribute__((ext_vector_type(2))) float f32x2;
typedef __attribute__((ext_vector_type(4))) u32 u32x4;
typedef __attribute__((ext_vector_type(2))) u32 u32x2;
typedef __attribute__((ext_vector_type(2))) __bf16 bf16x2;
#define LAS __attribute__((address_space(3)))
#define GAS __attribute__((address_space(1)))
#define DI __device__ __forceinline__
#define SB __builtin_amdgcn_sched_barrier(0)
DI unsigned sbid() { return __builtin_amdgcn_workgroup_id_x(); }

#define PSLEEP 4
#define STW 0
#define YSLEEP 12
#define UISS 1
#define UPOS 1
#define WOPOS 0
#define YMODE 0
constexpr int LDS_BYTES = 163840;
constexpr u32 WS_EPOCH = 0, WS_ACK = 4096, WS_F1 = 65536, WS_F2 = 131072, WS_PART = 524288, WS_O = 2u << 20, WS_END = 5u << 20;
constexpr u32 WS_STAMP = 16u << 20;
constexpr u32 kSpinCap = 1u << 18;
constexpr u32 CODE_PF_BYTES = 44032;

DI u32 pk(float a, float b) { f32x2 v = {a, b}; return __builtin_bit_cast(u32, __builtin_convertvector(v, bf16x2)); }
DI float lo2f(u32 x) { return __uint_as_float(x << 16); }
DI float hi2f(u32 x) { return __uint_as_float(x & 0xffff0000u); }
DI float b2f(u16 h) { return __uint_as_float((u32)h << 16); }
DI s16x4 trd(u32 a) { return __builtin_amdgcn_ds_read_tr16_b64_v4i16((LAS s16x4*)(size_t)a); }
DI s16x8 cat(s16x4 a, s16x4 b) { return __builtin_shufflevector(a, b, 0, 1, 2, 3, 4, 5, 6, 7); }
DI s16x8 frag(u32 a, u32 b, u32 c, u32 d) { u32x4 v = {a, b, c, d}; return __builtin_bit_cast(s16x8, v); }
template <class T> DI T ldsr(u32 a) { return *(LAS T*)(size_t)a; }
template <class T> DI void ldsw(u32 a, T v) { *(LAS T*)(size_t)a = v; }
template <int N> DI void waitv() { asm volatile("s_waitcnt vmcnt(%0)" ::"i"(N) : "memory"); }
DI void bar() { asm volatile("s_waitcnt lgkmcnt(0)\n\ts_barrier" ::: "memory"); }
DI f32x16 mfma32(s16x8 a, s16x8 b, f32x16 c) { return __builtin_amdgcn_mfma_f32_32x32x16_bf16(a, b, c, 0, 0, 0); }
DI f32x4 mfma16(s16x8 a, s16x8 b, f32x4 c) { return __builtin_amdgcn_mfma_f32_16x16x32_bf16(a, b, c, 0, 0, 0); }
DI float max3(float a, float b, float c) { float r; asm("v_max3_f32 %0, %1, %2, %3" : "=v"(r) : "v"(a), "v"(b), "v"(c)); return r; }
DI float min3(float a, float b, float c) { float r; asm("v_min3_f32 %0, %1, %2, %3" : "=v"(r) : "v"(a), "v"(b), "v"(c)); return r; }
DI float dot2(u32 x, u32 sel, float c) { return __builtin_amdgcn_fdot2_f32_bf16(__builtin_bit_cast(bf16x2, sel), __builtin_bit_cast(bf16x2, x), c, false); }
template <class R> DI void st128(u32x4 v, R rs, u32 voff, u32 soff, int) { __builtin_amdgcn_raw_buffer_store_b128(v, rs, voff + soff, 0, 16); }
DI void dma(u32 m0v, u32 voff, const void* sbase) {
    asm volatile("s_mov_b32 m0, %0\n\ts_nop 0\n\tglobal_load_lds_dwordx4 %1, %2" ::"s"(m0v), "v"(voff), "s"(sbase) : "memory");
}
DI void dma_m(u32 m0v, u32 voff, const void* sbase, u32 mlo, u32 mhi) {
    asm volatile("s_mov_b32 exec_lo, %3\n\ts_mov_b32 exec_hi, %4\n\ts_mov_b32 m0, %0\n\ts_nop 0\n\tglobal_load_lds_dwordx4 %1, %2\n\ts_mov_b64 exec, -1"
                 ::"s"(m0v), "v"(voff), "s"(sbase), "i"(mlo), "i"(mhi) : "memory");
}
#define STAMP(i) asm volatile("s_memrealtime %0\n\ts_waitcnt lgkmcnt(0)" : "=s"(stv[i])::"memory")
#define STAMP_ARG , u64 (&stv)[24]
#define STAMP_PASS , stv

DI void post_flag(char* fl, u64 tag, int tid, int n, int me) {
    if (tid < n) __hip_atomic_store((GAS u64*)(fl + tid * 256 + me * 8), tag, __ATOMIC_RELAXED, __HIP_MEMORY_SCOPE_AGENT);
}
DI void wait_flags(const char* fl, u64 tag, int l, int n) {
    const GAS u64* p = (const GAS u64*)(fl + (l < n ? l : 0) * 8);
    u64 v0 = __hip_atomic_load(p, __ATOMIC_RELAXED, __HIP_MEMORY_SCOPE_AGENT);
#pragma unroll 1
    for (u32 s = 0; s < kSpinCap; s++) {
        __builtin_amdgcn_s_sleep(PSLEEP);
        const u64 v1 = __hip_atomic_load(p, __ATOMIC_RELAXED, __HIP_MEMORY_SCOPE_AGENT);
        if (__all(v0 == tag)) break;
        v0 = v1;
    }
}

template <class F> DI void features(const float (&v)[20], const s16x8 (&wf)[2][3], u32 (&phi)[20], F filler) {
    u32 b[10];
#pragma unroll
    for (int i = 0; i < 10; i++) b[i] = pk(v[2 * i], v[2 * i + 1]);
    const s16x8 B0 = frag(b[0], b[1], b[2], b[3]), B1 = frag(b[4], b[5], b[6], b[7]), B2 = frag(b[8], b[9], 0, 0);
    f32x16 p0 = {}, p1 = {};
    p0 = mfma32(wf[0][0], B0, p0); p1 = mfma32(wf[1][0], B0, p1);
    p0 = mfma32(wf[0][1], B1, p0); p1 = mfma32(wf[1][1], B1, p1);
    p0 = mfma32(wf[0][2], B2, p0); p1 = mfma32(wf[1][2], B2, p1);
    SB; filler(); SB;
    asm volatile("s_nop 11" : "+v"(p0), "+v"(p1));
    float p[20];
#pragma unroll
    for (int i = 0; i < 20; i++) p[i] = i < 16 ? p0[i] : p1[i - 16];
    float mxa = max3(p[0], p[1], p[2]), mna = min3(p[0], p[1], p[2]), mxb = max3(p[3], p[4], p[5]), mnb = min3(p[3], p[4], p[5]);
#pragma unroll
    for (int i = 6; i < 18; i += 4) {
        mxa = max3(mxa, p[i], p[i + 1]); mna = min3(mna, p[i], p[i + 1]);
        mxb = max3(mxb, p[i + 2], p[i + 3]); mnb = min3(mnb, p[i + 2], p[i + 3]);
    }
    mxa = max3(mxa, p[18], p[19]); mna = min3(mna, p[18], p[19]);
    const float mx = max3(mxa, mxb, mxb), mn = min3(mna, mnb, mnb);
    const float L2E = 1.4426950408889634f;
    const float cx = -mx * L2E, cn = mn * L2E;
    float e[40], s1, s2;
#pragma unroll
    for (int i = 0; i < 20; i++) {
        e[i] = __builtin_amdgcn_exp2f(__builtin_fmaf(p[i], L2E, cx));
        e[20 + i] = __builtin_amdgcn_exp2f(__builtin_fmaf(p[i], -L2E, cn));
        if (i == 0) { s1 = e[0]; s2 = e[20]; } else { s1 += e[i]; s2 += e[20 + i]; }
    }
    const float r1 = __builtin_amdgcn_rcpf(s1), r2 = __builtin_amdgcn_rcpf(s2);
#pragma unroll
    for (int i = 0; i < 10; i++) { phi[i] = pk(e[2 * i] * r1, e[2 * i + 1] * r1); phi[10 + i] = pk(e[20 + 2 * i] * r2, e[21 + 2 * i] * r2); }
}

constexpr int WBLK = 960, WSLOT = 8 * WBLK, XSLOT = 24576;
constexpr int LW = XSLOT, LIMG = 102400, LMISC = LIMG + 2 * XSLOT, LZERO = LMISC + 3200, LTAB = LZERO + 64, TROW = 56, THEAD = 40 * TROW;
DI int xslot(int j) { return j % 3 == 0 ? 0 : LIMG + (j % 3 - 1) * XSLOT; }
static_assert(LTAB + 80 * TROW <= LDS_BYTES, "LDS map");

template <bool V> struct Flag { static constexpr bool value = V; };

struct Role { bool is_a; int r, c, ia; u32 nb; };
DI Role role_of(u32 bid) {
    const int xcd = bid & 7, j = bid >> 3, e = xcd & 1, m = xcd >> 1, nb = e ? 20 : 24, cq = e ? (j * 52) >> 8 : (j * 43) >> 8;
    Role R;
    R.is_a = j >= nb; R.nb = nb;
    R.r = __builtin_amdgcn_readfirstlane(e ? 6 + j - 5 * cq : j - 6 * cq); R.c = __builtin_amdgcn_readfirstlane(4 * m + cq);
    R.ia = __builtin_amdgcn_readfirstlane(20 * m + (e ? 8 : 0) + j - nb);
    return R;
}

DI void load_wo(const u16* wo, int cb, int wi, int end, int nw, int l) {
    u32 off[5];
#pragma unroll
    for (int i = 0; i < 5; i++) { const u32 idx = 64 * i + l, row = (idx * 6554u) >> 16; off[i] = row * 1120 + idx * 16; }
#pragma unroll 1
    for (int gq = wi; gq < end; gq += nw) {
        const char* gp = (const char*)wo + cb * 160 + gq * (32 * 1280);
#pragma unroll
        for (int i = 0; i < 5; i++) dma(gq * 5120 + i * 1024, off[i], gp);
    }
}

DI void out_proj(u16* __restrict__ out, char* __restrict__ ws, u64 tag, int ia STAMP_ARG) {
    const int bid = sbid(), tid = threadIdx.x, l = tid & 63, w = __builtin_amdgcn_readfirstlane(tid >> 6);
    const int t = 4 * (bid & 7) + (bid >> 6), cb = (bid >> 3) & 7, rs = (t * 43) >> 7, sub = t - 3 * rs;
    bar();
    if (w >= 4) {
        if (ia >= 0) {
            u32x2 v[8];
#pragma unroll
            for (int c2 = 0; c2 < 8; c2++) v[c2] = ldsr<u32x2>(131072 + c2 * 4096 + (tid - 256) * 8);
            u16* op = out + (size_t)(4 * (tid - 256)) * 1280 + 8 * ia;
            bar();
            __builtin_amdgcn_s_sleep(YSLEEP);
#pragma unroll
            for (int i = 0; i < 4; i++) {
                const u32 sel = (i & 1) ? 0x07060302u : 0x05040100u;
                *(u32x4*)(op + i * 1280) = u32x4{__builtin_amdgcn_perm(v[1][i >> 1], v[0][i >> 1], sel), __builtin_amdgcn_perm(v[3][i >> 1], v[2][i >> 1], sel),
                                                 __builtin_amdgcn_perm(v[5][i >> 1], v[4][i >> 1], sel), __builtin_amdgcn_perm(v[7][i >> 1], v[6][i >> 1], sel)};
            }
        }
        return;
    }
    constexpr int NPRE = 6;
    const auto rsw = __builtin_amdgcn_make_buffer_rsrc((void*)ws, 0, WS_END, 0x00020000);
    const int g = l >> 4, q = (l >> 2) & 3, p = l & 3, hp = g & 1, P = g >> 1;
    u32 b1 = (20 * hp + 8 * P + q) * 160 + p * 8, b2 = (80 * P + 20 * hp + 16 + q) * 160 + p * 8;
    u32 b1h = b1 + 51200, b2h = b2 + 51200;
    asm volatile("" : "+v"(b1), "+v"(b2), "+v"(b1h), "+v"(b2h));
    s16x4 wfr[NPRE][10];
    auto rdw = [&](int ks, int n) {
        const int G = ks / 5, i = ks % 5, tt = n >> 1, rd = n & 1;
        const u32 so = (G & 1) * 25600;
        wfr[ks % NPRE][n] = i < 4 ? trd((G >> 1 ? b1h : b1) + so + (40 * i + 4 * rd) * 160 + 32 * tt) : trd((G >> 1 ? b2h : b2) + so + 40 * rd * 160 + 32 * tt);
    };
#pragma unroll
    for (int ks = 0; ks < NPRE; ks++)
#pragma unroll
        for (int n = 0; n < 10; n++) rdw(ks, n);
    if (w == 0) wait_flags(ws + WS_F2 + (rs * 24 + sub * 8 + cb) * 256, tag, l, 16);
    bar();
    STAMP(8);
    const int ti = 16 * (w & 1) + (l & 15);
    const u32 ob = WS_O + (rs * 96 + 2 * sub + (w >> 1)) * 2560;
    const u32 v1 = P * 1024 + (hp * 32 + ti) * 16, v2 = 2048 + 2 * P * 15360 + (hp * 32 + ti) * 8;
    u32x4 of[16]; u32x2 ot[8];
    auto ldo = [&](int n) {
        const int G = n / 6, i = n % 6;
        if (i < 4) of[4 * G + i] = __builtin_amdgcn_raw_buffer_load_b128(rsw, v1, ob + (4 * G + i) * 15360, 16);
        else ot[2 * G + i - 4] = __builtin_amdgcn_raw_buffer_load_b64(rsw, v2, ob + (4 * G + i - 4) * 15360, 16);
    };
#pragma unroll
    for (int n = 0; n < 10; n++) ldo(n);
    f32x4 oc[5] = {};
#pragma unroll
    for (int ks = 0; ks < 20; ks++) {
        const int G = ks / 5, i = ks % 5;
        const s16x8 B = i < 4 ? __builtin_bit_cast(s16x8, of[4 * G + i]) : frag(ot[2 * G][0], ot[2 * G][1], ot[2 * G + 1][0], ot[2 * G + 1][1]);
#pragma unroll
        for (int tt = 0; tt < 5; tt++) {
            SB;
            oc[tt] = mfma16(cat(wfr[ks % NPRE][2 * tt], wfr[ks % NPRE][2 * tt + 1]), B, oc[tt]);
            SB;
            if (ks + NPRE < 20) { rdw(ks + NPRE, 2 * tt); rdw(ks + NPRE, 2 * tt + 1); }
            if ((tt == 0 || tt == 2) && 10 + 2 * ks + (tt >> 1) < 24) ldo(10 + 2 * ks + (tt >> 1));
        }
    }
    SB;
    u16* op = out + (size_t)(64 * t + 16 * w + (l & 15)) * 1280 + 640 + 80 * cb + 4 * g;
#pragma unroll
    for (int tt = 0; tt < 5; tt++) *(u32x2*)(op + 16 * tt) = u32x2{pk(oc[tt][0], oc[tt][1]), pk(oc[tt][2], oc[tt][3])};
    STAMP(9);
    if (bid == 0 && w == 0) {
        wait_flags(ws + WS_ACK, tag, l, 64);
        wait_flags(ws + WS_ACK + 512, tag, l, 16);
        if (l == 0) __hip_atomic_store((GAS u64*)(ws + WS_EPOCH), tag, __ATOMIC_RELAXED, __HIP_MEMORY_SCOPE_AGENT);
    }
}

template <bool FIRST>
DI void loader_b(const u16* __restrict__ x, const u16* __restrict__ W, const u16* __restrict__ wo, int r, int c, int lw, u32& pfd) {
    const int l = threadIdx.x & 63;
    const int ph = (((r + 4 * c) & 15) * 10) >> 4;
    const int pl = l >> 4, idx = l & 15, q3 = (idx * 11) >> 5;
    const int q = pl < 3 ? idx >> 2 : q3 & 3, sg = pl < 3 ? pl : idx - 3 * q3, ch = pl < 3 ? (idx & 3) + ((idx >> 1) & 1) : 2;
    const u32 wl = q * 3840 + sg * 1280 + ch * 16 + (sg == 0 ? ((c & 1) ? 0 : 160) : 80);
    const u32 xo0 = (l >> 3) * 1280 + (((l & 7) ^ (l >> 4)) << 4), xo1 = ((l >> 3) + 8) * 1280 + (((l & 7) ^ ((l >> 4) | 4)) << 4);
    u32 xr[2][3];
#pragma unroll
    for (int i = 0; i < 6; i++) { const int row = 192 * r + 96 * (i / 3) + 48 * lw + 16 * (i % 3); xr[i / 3][i % 3] = (row < 2048 ? row : row - 64) * 1280; }
    const char* wb = (const char*)W + c * 80 - 80 + lw * (16 * 3840);
    auto ld_x = [&](int hx) {
        const int jx = hx >> 1, hh = hx & 1;
        int jr = jx + ph; if (jr >= 10) jr -= 10;
        const char* g = (const char*)x + jr * 128;
        const u32 d = xslot(jx) + (96 * hh + 48 * lw) * 128;
#pragma unroll
        for (int i = 0; i < 3; i++) { dma(d + i * 2048, xo0, g + xr[hh][i]); dma(d + i * 2048 + 1024, xo1, g + xr[hh][i]); }
    };
    auto ld_w = [&](int i) {
        int ir = i + 2 * ph; if (ir >= 20) ir -= 20;
        const char* g = wb + ir * (32 * 3840);
        const u32 d = LW + (i & 3) * WSLOT + lw * 4 * WBLK;
#pragma unroll
        for (int b = 0; b < 4; b++) dma_m(d + b * WBLK, wl, g + b * (4 * 3840), 0xffffffffu, 0x0fffffffu);
    };
    if (FIRST) { ld_w(0); ld_x(0); ld_x(1); return; }
    ld_w(1); ld_x(2); ld_x(3);
    waitv<16>();
    bar();
    ld_w(2); ld_w(3); ld_x(4);
#pragma unroll
    for (int i = 0; i < 19; i++) {
        if (i == 0 || i == 2) waitv<26>(); else if (i <= 15) waitv<20>(); else if (i == 16) waitv<14>(); else if (i == 17) waitv<4>(); else waitv<2>();
        bar();
        if (i + 5 < 20) ld_x(i + 5);
        if (i + 4 < 20) ld_w(i + 4);
        if (i == 17) {
            const u32 ln = __builtin_amdgcn_readfirstlane(min((sbid() >> 3) * 256 + lw * 64, 6208u)) + l;
            asm volatile("global_load_dword %0, %1, %2\n\tglobal_load_dword %0, %1, %3" : "+v"(pfd) : "v"(ln * 128), "s"(wo), "s"((const char*)wo + 128 * 128));
        }
    }
}

DI u64 path_b(const u16* __restrict__ x, const u16* __restrict__ W, const u16* __restrict__ wphi, const u16* __restrict__ wo,
               u16* __restrict__ out, char* __restrict__ ws, int r, int c, u32& pfd STAMP_ARG) {
    const int tid = threadIdx.x, l = tid & 63;
    const int w = __builtin_amdgcn_readfirstlane(tid >> 6);
    const auto rsw = __builtin_amdgcn_make_buffer_rsrc((void*)ws, 0, WS_END, 0x00020000);
    const int g = l >> 4, q = (l >> 2) & 3, p = l & 3, h = l >> 5;
    const int rq = 2 * (g & 1) + (p >> 1), hk = g >> 1;
    const bool same = hk == (p & 1);
    u64 tag;
    u32 phiq[20];
    f32x16 acc[4] = {};
    s16x8 wfq[2][3];
    if (w >= 6) {
        loader_b<false>(x, W, wo, r, c, w - 6, pfd);
        asm volatile("s_load_dwordx2 %0, %1, 0\n\ts_waitcnt lgkmcnt(0)" : "=&s"(tag) : "s"(ws + WS_EPOCH) : "memory");
    } else {
        if (w < 4) dma_m(LMISC + w * 800, l * 16, (const char*)wphi + ((c ^ (w >> 1)) * 2 + (w & 1)) * 800, 0xffffffffu, 0x3ffffu);
        if (tid < 2) ldsw<u64>(LZERO + tid * 8, 0ull);
        u32 bA = LW + (g >> 1) * 2 * WBLK + q * 64 + (p & 1) * 32 + (g & 1) * 16 + (p >> 1) * 8;
        u32 bD = LW + (g >> 1) * 2 * WBLK + 768 + (3 * q + 2 * (g & 1) + (p >> 1)) * 16 + (p & 1) * 8;
        asm volatile("" : "+v"(bA), "+v"(bD));
        const int trow = 32 * w + (l & 31);
        s16x8 wfk[2][3];
        auto load_wf = [&](s16x8 (&wf)[2][3], u32 base) {
#pragma unroll
            for (int to = 0; to < 2; to++)
#pragma unroll
                for (int s = 0; s < 3; s++) {
                    s16x4 t[2];
#pragma unroll
                    for (int rd = 0; rd < 2; rd++) {
                        const int mu = 2 * s + rd;
                        if (mu < 5) {
                            int dq = mu + hk; if (mu == 4) dq = hk ? 0 : 4;
                            const bool ok = same && (to == 0 || rq == 0);
                            t[rd] = trd(ok ? base + hk * 800 + (4 * dq + q) * 40 + (4 * to + rq) * 8 : LZERO);
                        } else t[rd] = s16x4{0, 0, 0, 0};
                    }
                    wf[to][s] = cat(t[0], t[1]);
                }
        };
        asm volatile("s_load_dwordx2 %0, %1, 0\n\ts_waitcnt vmcnt(0) lgkmcnt(0)" : "=&s"(tag) : "s"(ws + WS_EPOCH), "v"(pfd) : "memory");
        bar();
        STAMP(1);
        u64 cy0; asm volatile("s_memtime %0\n\ts_waitcnt lgkmcnt(0)" : "=s"(cy0)::"memory");
        const u32 sw = ((trow & 2) << 1) | ((trow & 4) >> 1) | (trow & 1);
        const u32 rowa = LIMG + h * XSLOT + trow * 128;
        auto gemm = [&](auto HV) {
            constexpr bool hv = decltype(HV)::value;
            constexpr int NB = hv ? 3 : 1, NM = hv ? 6 : 3, NR = hv ? 11 : 7;
            f32x16 vc[3] = {};
            u32 aX[4], aXh[4];
            const int tx = hv ? 32 * (w - 2) + (l & 31) : trow;
#pragma unroll
            for (int k = 0; k < 4; k++) {
                aX[k] = tx * 128 + (((2 * k + h) ^ ((tx >> 1) & 7)) << 4); aXh[k] = aX[k] + LIMG;
                asm volatile("" : "+v"(aX[k]), "+v"(aXh[k]));
            }
            s16x4 fa[2][8]; s16x8 fb[2][NB];
            auto rd = [&](int s, int i, int ks, int n) {
                const int nb = hv ? (n == 0 ? 1 : n == 9 ? 0 : n == 10 ? 2 : -1) : (n == 0 ? 0 : -1);
                if (nb >= 0) {
                    const int sl = (i >> 1) % 3, k = (i & 1) * 2 + ks;
                    fb[s][nb] = sl == 0 ? ldsr<s16x8>(aX[k] + nb * 8192) : ldsr<s16x8>(aXh[k] + (sl - 1) * XSLOT + nb * 8192);
                } else {
                    const int na = n <= 4 ? n - 1 : n <= 6 ? n + 1 : n - 3;
                    const u32 o = (i & 3) * WSLOT + ks * 4 * WBLK + (na & 1) * WBLK;
                    fa[s][na] = na < 6 ? trd(bA + o + (na >> 1) * 256) : trd(bD + o);
                }
            };
            auto mm = [&](int s, int t) {
                if (t < 3) { const int tt = t == 2 ? 3 : t; acc[tt] = mfma32(cat(fa[s][2 * tt], fa[s][2 * tt + 1]), fb[s][hv ? 1 : 0], acc[tt]); }
                else { const int nb = t == 3 ? 1 : t == 4 ? 0 : 2; vc[nb] = mfma32(cat(fa[s][4], fa[s][5]), fb[s][hv ? nb : 0], vc[nb]); }
            };
            auto halfstep = [&](int s, int i, int ks, bool next) {
#pragma unroll
                for (int t = 0; t < NM; t++) {
                    SB; mm(s, t); SB;
                    if (t == 0 && ks == 0 && next) bar();
                    if (next)
#pragma unroll
                        for (int n = 3 * t; n < 3 * t + 3 && n < NR; n++) rd(s ^ 1, i, ks, n);
                }
            };
#pragma unroll
            for (int n = 0; n < NR; n++) rd(0, 0, 0, n);
#pragma unroll
            for (int i = 0; i < 20; i++) {
                if (i == 2) STAMP(13); if (i == 4) STAMP(16); if (i == 8) STAMP(18); if (i == 12) STAMP(20); if (i == 16) STAMP(22);
                halfstep(0, i, 1, true);
                if (i == 1) load_wf(wfk, LMISC);
                if (i == 2) load_wf(wfq, LMISC + 1600);
                halfstep(1, i + 1, 0, i < 19);
            }
            if (hv) {
#pragma unroll
                for (int nb = 0; nb < 3; nb++)
#pragma unroll
                    for (int mu = 0; mu < 4; mu++) {
                        const int pc = 10 + mu + h;
                        ldsw<u32x2>(rowa + (nb - 1) * 8192 + (((pc >> 1) ^ sw) << 4) + (pc & 1) * 8,
                                    u32x2{pk(vc[nb][4 * mu], vc[nb][4 * mu + 1]), pk(vc[nb][4 * mu + 2], vc[nb][4 * mu + 3])});
                    }
            }
        };
        if ((w & 3) >= 2) gemm(Flag<true>{}); else gemm(Flag<false>{});
        STAMP(2);
        { u64 cy1; asm volatile("s_memtime %0\n\ts_waitcnt lgkmcnt(0)" : "=s"(cy1)::"memory"); stv[21] = cy1 - cy0; }
        float kr[20]; u32 phik[20];
#pragma unroll
        for (int i = 0; i < 20; i++) kr[i] = i < 16 ? acc[1][i] : acc[3][i - 12];
        features(kr, wfk, phik, [&]() {
            const int pc = h ? 10 : 14;
            ldsw<u32x2>(rowa + (((pc >> 1) ^ sw) << 4) + (pc & 1) * 8, u32x2{pk(acc[3][8], acc[3][9]), pk(acc[3][10], acc[3][11])});
            ldsw<u32x2>(rowa + ((7 ^ sw) << 4) + 8, u32x2{0x3f80u, 0u});
        });
        const u32 keep = 192 * r + 32 * w < 2048 ? 0xffffffffu : 0u;
#pragma unroll
        for (int ch = 0; ch < 5; ch++) ldsw<u32x4>(rowa + ((ch ^ sw) << 4), u32x4{phik[4 * ch], phik[4 * ch + 1], phik[4 * ch + 2], phik[4 * ch + 3]} & keep);
        STAMP(10);
    }
    tag += 1;
    bar();
    if (w >= 6) load_wo(wo, (sbid() >> 3) & 7, w - 6, 10, 2, l);
    if (w < 4) {
        const int pcn = 4 * (g & 1) + p;
        const int pa = 10 + (pcn < 6 ? pcn : 5), pb = 5 * (w & 1) + (pcn < 5 ? pcn : 4);
        u32 aa[2], ab[2];
#pragma unroll
        for (int rd = 0; rd < 2; rd++) {
            const int row = 8 * (g >> 1) + 4 * rd + q;
            const u32 sw = ((row & 2) << 1) | ((row & 4) >> 1) | (row & 1);
            const u32 base = LIMG + (w >> 1) * XSLOT + row * 128;
            aa[rd] = base + (((pa >> 1) ^ sw) << 4) + (pa & 1) * 8;
            ab[rd] = base + (((pb >> 1) ^ sw) << 4) + (pb & 1) * 8;
            asm volatile("" : "+v"(aa[rd]), "+v"(ab[rd]));
        }
        s16x4 ta[24], tb[24];
#pragma unroll
        for (int ks = 0; ks < 12; ks++) {
            ta[2 * ks] = trd(aa[0] + ks * 2048); ta[2 * ks + 1] = trd(aa[1] + ks * 2048);
            tb[2 * ks] = trd(ab[0] + ks * 2048); tb[2 * ks + 1] = trd(ab[1] + ks * 2048);
        }
        SB;
        f32x16 kv = {};
#pragma unroll
        for (int ks = 0; ks < 12; ks++) kv = mfma32(cat(ta[2 * ks], ta[2 * ks + 1]), cat(tb[2 * ks], tb[2 * ks + 1]), kv);
        if ((l & 31) < 20) {
            const u32 lam = h * 20 + (l & 31), o = WS_PART + (c * 16 + r) * 4096 + w * 960;
            st128(u32x4{pk(kv[0], kv[1]), pk(kv[2], kv[3]), pk(kv[4], kv[5]), pk(kv[6], kv[7])}, rsw, lam * 16, o, 16);
            __builtin_amdgcn_raw_buffer_store_b64(u32x2{pk(kv[8], kv[9]), pk(kv[10], kv[11])}, rsw, 640 + lam * 8, o, 16);
        }
    }
    STAMP(11);
    if (w < 6) {
        float qr[20];
#pragma unroll
        for (int i = 0; i < 20; i++) qr[i] = i < 16 ? acc[0][i] : acc[3][i - 16];
        features(qr, wfq, phiq, []() {});
#pragma unroll
        for (int i = 0; i < 20; i++) asm volatile("" : "+v"(phiq[i]));
        STAMP(17);
        waitv<0>();
    }
    bar();
    post_flag(ws + WS_F1 + c * 4096, tag, tid, 11, r);
    STAMP(3);
    if (w >= 6) load_wo(wo, (sbid() >> 3) & 7, w + 4, 20, 2, l);
    if (tid < 80) ldsw<u64>(LTAB + tid * TROW + 48, 0ull);
    if (w == 0) wait_flags(ws + WS_F1 + (c ^ 1) * 4096 + r * 256, tag, l, 11);
    bar();
    STAMP(5);
    if (w < 4) {
        const int t = tid < 240 ? tid : 239;
        const int un = (t * 1093) >> 16, it = t - un * 60;
        u32x4 pv[11];
#pragma unroll
        for (int i = 0; i < 11; i++) pv[i] = __builtin_amdgcn_raw_buffer_load_b128(rsw, t * 16, WS_PART + (c ^ 1) * 65536 + i * 4096, 16);
        float s[8] = {};
        u32 slo = 0x00003f80u, shi = 0x3f800000u;
        asm volatile("" : "+s"(slo), "+s"(shi));
#pragma unroll
        for (int i = 0; i < 11; i++)
#pragma unroll
            for (int e = 0; e < 4; e++) { s[2 * e] = dot2(pv[i][e], slo, s[2 * e]); s[2 * e + 1] = dot2(pv[i][e], shi, s[2 * e + 1]); }
        const int lam = it < 40 ? it : 2 * (it - 40);
        const int hh = lam >= 20, f = lam - 20 * hh;
        const u32 a0 = LTAB + (un >> 1) * THEAD + ((un & 1) * 20 + f) * TROW + hh * 8 + (it < 40 ? 0 : 32);
        ldsw<u32x2>(a0, u32x2{pk(s[0], s[1]), pk(s[2], s[3])});
        ldsw<u32x2>(a0 + (it < 40 ? 16 : TROW), u32x2{pk(s[4], s[5]), pk(s[6], s[7])});
    }
    bar();
    STAMP(6);
    if (w < 6 && 192 * r + 32 * w < 2048) {
        const u32 tb = LTAB + hk * THEAD + q * TROW;
        u32 t0 = tb + (same ? rq : 6) * 8, t1 = tb + (same && rq < 2 ? 4 + rq : 6) * 8;
        asm volatile("" : "+v"(t0), "+v"(t1));
        f32x16 o0 = {}, o1 = {};
#pragma unroll
        for (int s = 0; s < 5; s++) {
            const s16x8 B = frag(phiq[4 * s], phiq[4 * s + 1], phiq[4 * s + 2], phiq[4 * s + 3]);
            o0 = mfma32(cat(trd(t0 + 8 * s * TROW), trd(t0 + (8 * s + 4) * TROW)), B, o0);
            o1 = mfma32(cat(trd(t1 + 8 * s * TROW), trd(t1 + (8 * s + 4) * TROW)), B, o1);
        }
        const float rden = __builtin_amdgcn_rcpf(o1[4] + 1e-6f);
        u32 ob[10];
#pragma unroll
        for (int i = 0; i < 10; i++) ob[i] = i < 8 ? pk(o0[2 * i] * rden, o0[2 * i + 1] * rden) : pk(o1[2 * i - 16] * rden, o1[2 * i - 15] * rden);
        const u32 o = WS_O + ((r * 16 + (c ^ 1)) * 6 + w) * 2560;
        st128(u32x4{ob[0], ob[1], ob[2], ob[3]}, rsw, l * 16, o, 16);
        st128(u32x4{ob[4], ob[5], ob[6], ob[7]}, rsw, l * 16 + 1024, o, 16);
        __builtin_amdgcn_raw_buffer_store_b64(u32x2{ob[8], ob[9]}, rsw, l * 8 + 2048, o, 16);
    }
    STAMP(12);
    waitv<0>();
    asm volatile("" ::"v"(pfd));
    bar();
    post_flag(ws + WS_F2 + r * (24 * 256), tag, tid, 24, c);
    STAMP(7);
    return tag;
}

DI f32x2 cmul(f32x2 a, f32x2 b) {
    f32x2 d;
    asm("v_pk_mul_f32 %0, %1, %2 op_sel_hi:[0,1]\n\tv_pk_fma_f32 %0, %1, %2, %0 op_sel:[1,1,0] op_sel_hi:[1,0,1] neg_lo:[0,1,0]" : "=&v"(d) : "v"(a), "v"(b));
    return d;
}
DI f32x2 cmulc(f32x2 a, f32x2 b) {
    f32x2 d;
    asm("v_pk_mul_f32 %0, %1, %2 op_sel_hi:[0,1] neg_hi:[0,1]\n\tv_pk_fma_f32 %0, %1, %2, %0 op_sel:[1,1,0] op_sel_hi:[1,0,1]" : "=&v"(d) : "v"(a), "v"(b));
    return d;
}
DI f32x2 csq(f32x2 a) { return cmul(a, a); }
DI f32x2 cexp(float a, float b) {
    const float m = __builtin_amdgcn_exp2f(a * 1.4426950408889634f);
    const float c1 = 0.15915494309189535f, c2 = 6.4206382e-9f;
    const float n = __builtin_rintf(b * c1);
    const float f = __builtin_fmaf(b, c2, __builtin_fmaf(b, c1, -n));
    return f32x2{m * __builtin_amdgcn_cosf(f), m * __builtin_amdgcn_sinf(f)};
}
constexpr int LU = 131072;
DI u32 kaddr(u32 e) { return ((e >> 5) << 8) + ((e & 31) << 1); }

DI void prologue_a(int ia, const u16* __restrict__ Are, const u16* __restrict__ Aim, const u16* __restrict__ Bre, const u16* __restrict__ Bim,
                   const u16* __restrict__ Cre, const u16* __restrict__ Cim, const u16* __restrict__ logdt, const u16* __restrict__ Dskip) {
    const int l = threadIdx.x & 63, w = __builtin_amdgcn_readfirstlane(threadIdx.x >> 6);
    const u16* src = w == 0 ? Are : w == 1 ? Aim : w == 2 ? Bre : w == 3 ? Bim : w == 4 ? Cre : w == 5 ? Cim : w == 6 ? logdt : Dskip;
    if (w < 6) { dma(w * 16384 + 8192, ia * 2048 + l * 16, src); dma(w * 16384 + 9216, ia * 2048 + 1024 + l * 16, src); }
    else dma_m(w * 16384 + 8192, ia * 16, src, 1u, 0u);
}

DI u64 path_a(const u16* __restrict__ u, const u16* __restrict__ wo, u16* __restrict__ out, char* __restrict__ ws, int ia, u32 pfd STAMP_ARG) {
    const int tid = threadIdx.x, l = tid & 63;
    const int w = __builtin_amdgcn_readfirstlane(tid >> 6);
    const u32 KB = w * 16384;
    const int g = l >> 4, q = (l >> 2) & 3, p = l & 3, h = l >> 5;
    STAMP(1);
    u64 tag;
    asm volatile("s_load_dwordx2 %0, %1, 0\n\ts_waitcnt vmcnt(0) lgkmcnt(0)" : "=&s"(tag) : "s"(ws + WS_EPOCH), "v"(pfd) : "memory");
    tag += 1;
    bar();
    if (tid == 0) __hip_atomic_store((GAS u64*)(ws + WS_ACK + ia * 8), tag, __ATOMIC_RELAXED, __HIP_MEMORY_SCOPE_AGENT);
    const float ldt = b2f(ldsr<u16>(6 * 16384 + 8192 + w * 2)), dsk = b2f(ldsr<u16>(7 * 16384 + 8192 + w * 2));
    u32 prm[6];
#pragma unroll
    for (int i = 0; i < 6; i++) prm[i] = ldsr<u32>(i * 16384 + 8192 + w * 256 + l * 4);
    bar();
    u32x4 ur[4];
    auto u_in = [&]() {
#pragma unroll
        for (int i = 0; i < 4; i++) ur[i] = *(const u32x4*)((const char*)u + (size_t)(256 * ((w + ia) & 7) + 4 * l + i) * 1280 + ia * 16);
    };
    if (UISS == 0) u_in();
    const float dt = lo2f(pk(expf(ldt), 0.f));
    STAMP(2);
    f32x16 kd = {};
    u32 ra[2];
#pragma unroll
    for (int rd = 0; rd < 2; rd++) ra[rd] = KB + (8 * (g >> 1) + 4 * rd + q) * 64 + (((2 * (g & 1) + (p >> 1)) ^ ((2 * rd + (q >> 1)) & 3)) << 4) + (p & 1) * 8;
    const u32 wa = KB + l * 64, wsw = (l >> 1) & 3;
#pragma unroll
    for (int ps = 0; ps < 2; ps++) {
        float pf[6];
#pragma unroll
        for (int i = 0; i < 6; i++) pf[i] = ps ? hi2f(prm[i]) : lo2f(prm[i]);
        const float a = dt * pf[0], b = dt * pf[1];
        const f32x2 Wc = cmulc(f32x2{pf[4], -pf[5]}, f32x2{dt * pf[2], dt * pf[3]});
        const f32x2 e1 = cexp(a, b), e16 = cexp(16.f * a, 16.f * b), e256 = cexp(256.f * a, 256.f * b);
        const f32x2 e2 = csq(e1), e4 = csq(e2), e8 = csq(e4), e32 = csq(e16), e64 = csq(e32), e128 = csq(e64), e512 = csq(e256), e1024 = csq(e512);
        f32x2 X[32];
        X[0] = Wc; X[1] = cmulc(Wc, e1); X[2] = cmulc(Wc, e2); X[3] = cmulc(X[1], e2);
#pragma unroll
        for (int i = 0; i < 4; i++) X[4 + i] = cmulc(X[i], e4);
        X[8] = cmulc(Wc, e256); X[9] = cmulc(X[8], e2); X[10] = cmulc(X[8], e4);
        X[12] = X[10]; X[13] = cmulc(X[9], e4); X[14] = cmulc(X[8], e8);
        X[11] = cmulc(Wc, e512); X[16] = cmulc(X[11], e4); X[17] = cmulc(X[11], e8);
        X[15] = cmulc(X[8], e512); X[20] = cmulc(X[15], e4); X[21] = cmulc(X[15], e8);
        X[18] = cmulc(Wc, e1024); X[19] = cmulc(X[18], e8);
        X[24] = cmulc(X[8], e1024); X[25] = cmulc(X[24], e8);
        X[22] = cmulc(X[11], e1024); X[23] = cmulc(X[22], e8);
        X[28] = cmulc(X[15], e1024); X[29] = cmulc(X[28], e8);
        X[26] = X[27] = X[30] = X[31] = f32x2{0.f, 0.f};
        f32x2 Y[8];
        Y[0] = f32x2{1.f, 0.f}; Y[1] = e8; Y[2] = e16; Y[3] = cmul(e8, e16); Y[4] = e32; Y[5] = cmul(e8, e32); Y[6] = cmul(e16, e32); Y[7] = cmul(Y[3], e32);
        const f32x2 e192 = cmul(e64, e128);
#pragma unroll
        for (int ch = 0; ch < 4; ch++) {
            const f32x2* Xc = X + 8 * ch;
            const u32 o = wa + ((ch ^ wsw) << 4);
            ldsw<u32x4>(o, u32x4{pk(Xc[0].x, Xc[1].x), pk(Xc[2].x, Xc[3].x), pk(Xc[4].x, Xc[5].x), pk(Xc[6].x, Xc[7].x)});
            ldsw<u32x4>(o + 4096, u32x4{pk(Xc[0].y, Xc[1].y), pk(Xc[2].y, Xc[3].y), pk(Xc[4].y, Xc[5].y), pk(Xc[6].y, Xc[7].y)});
            f32x2 Z[8];
            const f32x2 m = ch == 1 ? e64 : ch == 2 ? e128 : e192;
#pragma unroll
            for (int i = 0; i < 8; i++) Z[i] = ch == 0 ? Y[i] : i == 0 ? m : cmul(Y[i], m);
            ldsw<u32x4>(o + 8192, u32x4{pk(Z[0].x, Z[1].x), pk(Z[2].x, Z[3].x), pk(Z[4].x, Z[5].x), pk(Z[6].x, Z[7].x)});
            ldsw<u32x4>(o + 12288, u32x4{pk(Z[0].y, Z[1].y), pk(Z[2].y, Z[3].y), pk(Z[4].y, Z[5].y), pk(Z[6].y, Z[7].y)});
        }
        s16x4 fx[16], fy[16];
#pragma unroll
        for (int ks = 0; ks < 8; ks++) {
            fx[2 * ks] = trd(ra[0] + ks * 1024); fx[2 * ks + 1] = trd(ra[1] + ks * 1024);
            fy[2 * ks] = trd(ra[0] + ks * 1024 + 8192); fy[2 * ks + 1] = trd(ra[1] + ks * 1024 + 8192);
        }
        if (ps == 0) { STAMP(4); if (UISS == 1) u_in(); }
#pragma unroll
        for (int ks = 0; ks < 8; ks++) kd = mfma32(cat(fx[2 * ks], fx[2 * ks + 1]), cat(fy[2 * ks], fy[2 * ks + 1]), kd);
    }
    STAMP(6);
    {
        const int j = l & 31;
        ldsw<u32x2>(KB + kaddr(2044 - 8 * j - 4 * h), u32x2{pk(kd[3], kd[2]), pk(kd[1], l == 0 ? kd[0] + dsk : kd[0])});
        ldsw<u32x2>(KB + kaddr(2044 - 256 - 8 * j - 4 * h), u32x2{pk(kd[6], kd[5]), pk(kd[4], kd[4])});
        ldsw<u32x4>(KB + kaddr(2040 - 512 - 256 * h - 8 * j), u32x4{pk(kd[9], kd[9]), pk(kd[8], kd[8]), pk(kd[8], kd[7]), pk(kd[7], kd[7])});
#pragma unroll
        for (int sp = 0; sp < 2; sp++) {
            const float a = kd[10 + 2 * sp], b = kd[11 + 2 * sp];
            const u32 aa = pk(a, a), bb = pk(b, b);
            ldsw<u32x4>(KB + kaddr(2040 - 1024 - 512 * h - 256 * sp - 8 * j), u32x4{bb, (j & 1) ? bb : pk(b, a), aa, aa});
        }
        u32x4 v[4]; u32x2 n[4];
        const u32 a0 = KB + kaddr(8 * l), a1 = KB + kaddr(8 * l + 8);
#pragma unroll
        for (int it = 0; it < 4; it++) { v[it] = ldsr<u32x4>(a0 + it * 4096); n[it] = ldsr<u32x2>(a1 + it * 4096); }
#pragma unroll
        for (int it = 0; it < 4; it++) {
            const u32 s0 = __builtin_amdgcn_alignbit(v[it][1], v[it][0], 16), s1 = __builtin_amdgcn_alignbit(v[it][2], v[it][1], 16),
                      s2 = __builtin_amdgcn_alignbit(v[it][3], v[it][2], 16), s3 = __builtin_amdgcn_alignbit(n[it][0], v[it][3], 16),
                      s4 = __builtin_amdgcn_alignbit(n[it][1], n[it][0], 16);
            ldsw<u32x4>(a0 + it * 4096 + 64, u32x4{s0, s1, s2, s3});
            ldsw<u32x4>(a0 + it * 4096 + 128, u32x4{v[it][1], v[it][2], v[it][3], n[it][0]});
            ldsw<u32x4>(a0 + it * 4096 + 192, u32x4{s1, s2, s3, s4});
        }
    }
    STAMP(5);
    {
        const u32 a = LU + (256 * ((w + ia) & 7) + 4 * l) * 2;
#pragma unroll
        for (int c2 = 0; c2 < 4; c2++) {
            ldsw<u32x2>(a + 2 * c2 * 4096, u32x2{__builtin_amdgcn_perm(ur[1][c2], ur[0][c2], 0x05040100u), __builtin_amdgcn_perm(ur[3][c2], ur[2][c2], 0x05040100u)});
            ldsw<u32x2>(a + (2 * c2 + 1) * 4096, u32x2{__builtin_amdgcn_perm(ur[1][c2], ur[0][c2], 0x07060302u), __builtin_amdgcn_perm(ur[3][c2], ur[2][c2], 0x07060302u)});
        }
    }
    STAMP(3);
    bar();
    STAMP(10);
    const int rr = l & 15;
    const u32 e0 = 2047 - 16 - rr + 8 * g, sh = e0 & 3;
    auto half = [&](auto HI) {
        constexpr bool hi = decltype(HI)::value;
        constexpr int NF = hi ? 8 : 4, T0 = hi ? 4 : 0, NRD = 3 * NF;
        f32x4 y[4] = {};
        u32 ka0 = KB + kaddr(e0 - sh) + sh * 64 - (NF - 1) * 2048, ka1 = KB + kaddr(e0 - sh + 4) + sh * 64 - (NF - 1) * 2048;
        u32 ua = LU + w * 4096 + 32 * rr + 16 * g - 32;
        u32 mk[4];
#pragma unroll
        for (int i = 0; i < 4; i++) mk[i] = (2 * i > 16 + rr - 8 * g ? 0u : 0xffffu) | (2 * i + 1 > 16 + rr - 8 * g ? 0u : 0xffff0000u);
        int lim = 2 * rr + g - 2;
        u32x2 A[2][2 * NF]; u32x4 B[2][NF];
        auto rdc = [&](int s, int n) {
            const int m = n / 3, k = n % 3;
            if (k == 0) B[s][m] = ldsr<u32x4>(ua + m * 512);
            else if (k == 1) A[s][2 * (NF - 1 - m)] = ldsr<u32x2>(ka0 + m * 2048);
            else A[s][2 * (NF - 1 - m) + 1] = ldsr<u32x2>(ka1 + m * 2048);
        };
        auto group = [&](int s, bool next) {
            A[s][0] &= u32x2{mk[0], mk[1]}; A[s][1] &= u32x2{mk[2], mk[3]};
            if (lim < 0) B[s][0] = u32x4{0, 0, 0, 0};
            ka0 -= 256; ka1 -= 256; ua -= 64; lim -= 4;
            mk[0] = mk[1] = mk[2] = mk[3] = 0xffffffffu;
            asm volatile("" : "+v"(ka0), "+v"(ka1), "+v"(ua));
            int n = 0;
#pragma unroll
            for (int m = NF - 1; m >= 0; m--)
#pragma unroll
                for (int ct = NF - 1 - m; ct >= 0 && ct + m >= T0; ct--) {
                    SB;
                    y[ct + m - T0] = mfma16(frag(A[s][2 * m][0], A[s][2 * m][1], A[s][2 * m + 1][0], A[s][2 * m + 1][1]), __builtin_bit_cast(s16x8, B[s][ct]), y[ct + m - T0]);
                    SB;
                    if (next && n < NRD) rdc(s ^ 1, n);
                    n++;
                    if (!hi && next && (n == 1 || n == 6)) rdc(s ^ 1, n++);
                }
        };
        asm volatile("" : "+v"(ka0), "+v"(ka1), "+v"(ua));
#pragma unroll
        for (int n = 0; n < NRD; n++) rdc(0, n);
#pragma unroll 1
        for (int gg = 0; gg < 3; gg++) { group(0, true); group(1, true); }
        group(0, true); group(1, false);
#pragma unroll
        for (int ct = 0; ct < 4; ct++) ldsw<u32x2>(LU + w * 4096 + (T0 + ct) * 512 + rr * 32 + g * 8, u32x2{pk(y[ct][0], y[ct][1]), pk(y[ct][2], y[ct][3])});
        if (hi) STAMP(14); else STAMP(16);
        bar();
        if (hi) STAMP(15); else STAMP(17);
        if (!hi) return;
        u32 v[8];
#pragma unroll
        for (int c2 = 0; c2 < 8; c2++) v[c2] = ldsr<u32>(LU + c2 * 4096 + T0 * 512 + tid * 4);
        u16* op = out + (size_t)(T0 * 256 + 2 * tid) * 1280 + 8 * ia;
        *(u32x4*)op = u32x4{__builtin_amdgcn_perm(v[1], v[0], 0x05040100u), __builtin_amdgcn_perm(v[3], v[2], 0x05040100u),
                            __builtin_amdgcn_perm(v[5], v[4], 0x05040100u), __builtin_amdgcn_perm(v[7], v[6], 0x05040100u)};
        *(u32x4*)(op + 1280) = u32x4{__builtin_amdgcn_perm(v[1], v[0], 0x07060302u), __builtin_amdgcn_perm(v[3], v[2], 0x07060302u),
                                     __builtin_amdgcn_perm(v[5], v[4], 0x07060302u), __builtin_amdgcn_perm(v[7], v[6], 0x07060302u)};
    };
    half(Flag<true>{});
    STAMP(11);
    half(Flag<false>{});
    STAMP(12);
    load_wo(wo, (sbid() >> 3) & 7, w, 20, 8, l);
    waitv<0>();
    STAMP(7);
    return tag;
}

__global__ void __launch_bounds__(512, 2)
fused_kernel(const u16* __restrict__ x, const u16* __restrict__ W, const u16* __restrict__ wphi, const u16* __restrict__ wo,
             u16* __restrict__ out, char* __restrict__ ws, const u16* __restrict__ u, const u16* __restrict__ Are,
             const u16* __restrict__ Aim, const u16* __restrict__ Bre, const u16* __restrict__ Bim, const u16* __restrict__ Cre,
             const u16* __restrict__ Cim, const u16* __restrict__ logdt, const u16* __restrict__ Dskip) {
    __shared__ __attribute__((aligned(16))) char smem[LDS_BYTES];
    asm volatile("" ::"s"((u32)(size_t)(LAS char*)smem));
    u64 stv[24] = {};
    asm volatile("s_memrealtime %0" : "=s"(stv[0]));
    u32 pfd = 0;
    if (threadIdx.x < 12) {
        u64 pc; asm volatile("s_getpc_b64 %0" : "=s"(pc));
        asm volatile("global_load_dword %0, %1, %2" : "+v"(pfd) : "v"(threadIdx.x * 128), "s"(pc + min((sbid() >> 3) * 1536, CODE_PF_BYTES - 1536)));
    }
    asm volatile("" ::"s"(x), "s"(W), "s"(wphi), "s"(wo), "s"(out), "s"(ws), "s"(u), "s"(Are), "s"(Aim), "s"(Bre), "s"(Bim), "s"(Cre), "s"(Cim),
                 "s"(logdt), "s"(Dskip));
    const Role R = role_of(sbid());
    u64 tag;
    if (R.is_a) prologue_a(R.ia, Are, Aim, Bre, Bim, Cre, Cim, logdt, Dskip);
    else if (__builtin_amdgcn_readfirstlane(threadIdx.x) >= 384) loader_b<true>(x, W, wo, R.r, R.c, __builtin_amdgcn_readfirstlane(threadIdx.x >> 6) - 6, pfd);
    u32 sel = R.nb;
    asm volatile("" : "+s"(sel));
    if ((sbid() >> 3) >= sel) {
        tag = path_a(u, wo, out, ws, R.ia, pfd STAMP_PASS);
    } else {
        tag = path_b(x, W, wphi, wo, out, ws, R.r, R.c, pfd STAMP_PASS);
    }
    out_proj(out, ws, tag, R.is_a ? R.ia : -1 STAMP_PASS);
    if (sbid() == 0 && threadIdx.x < 64) { u64 now; const u64 until = stv[9] + 0 * (stv[0] - stv[0]);
        do { asm volatile("s_memrealtime %0\n\ts_waitcnt lgkmcnt(0)" : "=s"(now)::"memory"); } while (now < until && now - stv[0] < 100000); }
}

extern "C" void kernel_launch(void* const* d_in, const int* in_sizes, int n_in, void* d_out, int out_size, void* d_ws, size_t ws_size,
                              hipStream_t stream) {
    const u16* const* in = (const u16* const*)d_in;
    fused_kernel<<<256, 512, 0, stream>>>(in[9], in[10], in[11], in[12], (u16*)d_out, (char*)d_ws, in[0], in[1], in[2], in[3], in[4], in[5],
                                          in[6], in[7], in[8]);
}
```

```cpp
#include <hip/hip_runtime.h>
#include <hip/hip_bf16.h>

typedef unsigned short u16;
typedef unsigned u32;
typedef unsigned long long u64;
typedef __attribute__((ext_vector_type(8))) short s16x8;
typedef __attribute__((ext_vector_type(4))) short s16x4;
typedef __attribute__((ext_vector_type(16))) float f32x16;
typedef __attribute__((ext_vector_type(4))) float f32x4;
typedef __attribute__((ext_vector_type(2))) float f32x2;
typedef __attribute__((ext_vector_type(4))) u32 u32x4;
typedef __attribute__((ext_vector_type(2))) u32 u32x2;
typedef __attribute__((ext_vector_type(2))) __bf16 bf16x2;
#define LAS __attribute__((address_space(3)))
#define GAS __attribute__((address_space(1)))
#define DI __device__ __forceinline__
#define SB __builtin_amdgcn_sched_barrier(0)
DI unsigned sbid() { return __builtin_amdgcn_workgroup_id_x(); }

#define PSLEEP 4
#define STW 0
#define YSLEEP 12
#define AUXA 0
#define AUXB 0
#define UISS 1
#define UPOS 1
#define WOPOS 0
#define YMODE 0
constexpr int LDS_BYTES = 163840;
constexpr u32 WS_EPOCH = 0, WS_ACK = 4096, WS_F1 = 65536, WS_F2 = 131072, WS_PART = 524288, WS_O = 2u << 20, WS_END = 5u << 20;
constexpr u32 WS_STAMP = 16u << 20;
constexpr u32 kSpinCap = 1u << 18;
constexpr u32 CODE_PF_BYTES = 44032;

DI u32 pk(float a, float b) { f32x2 v = {a, b}; return __builtin_bit_cast(u32, __builtin_convertvector(v, bf16x2)); }
DI float lo2f(u32 x) { return __uint_as_float(x << 16); }
DI float hi2f(u32 x) { return __uint_as_float(x & 0xffff0000u); }
DI float b2f(u16 h) { return __uint_as_float((u32)h << 16); }
DI s16x4 trd(u32 a) { return __builtin_amdgcn_ds_read_tr16_b64_v4i16((LAS s16x4*)(size_t)a); }
DI s16x8 cat(s16x4 a, s16x4 b) { return __builtin_shufflevector(a, b, 0, 1, 2, 3, 4, 5, 6, 7); }
DI s16x8 frag(u32 a, u32 b, u32 c, u32 d) { u32x4 v = {a, b, c, d}; return __builtin_bit_cast(s16x8, v); }
template <class T> DI T ldsr(u32 a) { return *(LAS T*)(size_t)a; }
template <class T> DI void ldsw(u32 a, T v) { *(LAS T*)(size_t)a = v; }
template <int N> DI void waitv() { asm volatile("s_waitcnt vmcnt(%0)" ::"i"(N) : "memory"); }
DI void bar() { asm volatile("s_waitcnt lgkmcnt(0)\n\ts_barrier" ::: "memory"); }
DI f32x16 mfma32(s16x8 a, s16x8 b, f32x16 c) { return __builtin_amdgcn_mfma_f32_32x32x16_bf16(a, b, c, 0, 0, 0); }
DI f32x4 mfma16(s16x8 a, s16x8 b, f32x4 c) { return __builtin_amdgcn_mfma_f32_16x16x32_bf16(a, b, c, 0, 0, 0); }
DI float max3(float a, float b, float c) { float r; asm("v_max3_f32 %0, %1, %2, %3" : "=v"(r) : "v"(a), "v"(b), "v"(c)); return r; }
DI float min3(float a, float b, float c) { float r; asm("v_min3_f32 %0, %1, %2, %3" : "=v"(r) : "v"(a), "v"(b), "v"(c)); return r; }
DI float dot2(u32 x, u32 sel, float c) { return __builtin_amdgcn_fdot2_f32_bf16(__builtin_bit_cast(bf16x2, sel), __builtin_bit_cast(bf16x2, x), c, false); }
template <class R> DI void st128(u32x4 v, R rs, u32 voff, u32 soff, int) { __builtin_amdgcn_raw_buffer_store_b128(v, rs, voff + soff, 0, 16); }
DI void out16(u16* out, u32 off, u32x4 v) {
    __builtin_amdgcn_raw_buffer_store_b128(v, __builtin_amdgcn_make_buffer_rsrc((void*)out, 0, 2048 * 2560, 0x00020000), off * 2, 0, AUXA);
}
DI void out8(u16* out, u32 off, u32x2 v) {
    __builtin_amdgcn_raw_buffer_store_b64(v, __builtin_amdgcn_make_buffer_rsrc((void*)out, 0, 2048 * 2560, 0x00020000), off * 2, 0, AUXB);
}
DI void dma(u32 m0v, u32 voff, const void* sbase) {
    asm volatile("s_mov_b32 m0, %0\n\ts_nop 0\n\tglobal_load_lds_dwordx4 %1, %2" ::"s"(m0v), "v"(voff), "s"(sbase) : "memory");
}
DI void dma_m(u32 m0v, u32 voff, const void* sbase, u32 mlo, u32 mhi) {
    asm volatile("s_mov_b32 exec_lo, %3\n\ts_mov_b32 exec_hi, %4\n\ts_mov_b32 m0, %0\n\ts_nop 0\n\tglobal_load_lds_dwordx4 %1, %2\n\ts_mov_b64 exec, -1"
                 ::"s"(m0v), "v"(voff), "s"(sbase), "i"(mlo), "i"(mhi) : "memory");
}
#define STAMP(i)
#define STAMP_ARG
#define STAMP_PASS

DI void post_flag(char* fl, u64 tag, int tid, int n, int me) {
    if (tid < n) __hip_atomic_store((GAS u64*)(fl + tid * 256 + me * 8), tag, __ATOMIC_RELAXED, __HIP_MEMORY_SCOPE_AGENT);
}
DI void wait_flags(const char* fl, u64 tag, int l, int n) {
    const GAS u64* p = (const GAS u64*)(fl + (l < n ? l : 0) * 8);
    u64 v0 = __hip_atomic_load(p, __ATOMIC_RELAXED, __HIP_MEMORY_SCOPE_AGENT);
#pragma unroll 1
    for (u32 s = 0; s < kSpinCap; s++) {
        __builtin_amdgcn_s_sleep(PSLEEP);
        const u64 v1 = __hip_atomic_load(p, __ATOMIC_RELAXED, __HIP_MEMORY_SCOPE_AGENT);
        if (__all(v0 == tag)) break;
        v0 = v1;
    }
}

template <class F> DI void features(const float (&v)[20], const s16x8 (&wf)[2][3], u32 (&phi)[20], F filler) {
    u32 b[10];
#pragma unroll
    for (int i = 0; i < 10; i++) b[i] = pk(v[2 * i], v[2 * i + 1]);
    const s16x8 B0 = frag(b[0], b[1], b[2], b[3]), B1 = frag(b[4], b[5], b[6], b[7]), B2 = frag(b[8], b[9], 0, 0);
    f32x16 p0 = {}, p1 = {};
    p0 = mfma32(wf[0][0], B0, p0); p1 = mfma32(wf[1][0], B0, p1);
    p0 = mfma32(wf[0][1], B1, p0); p1 = mfma32(wf[1][1], B1, p1);
    p0 = mfma32(wf[0][2], B2, p0); p1 = mfma32(wf[1][2], B2, p1);
    SB; filler(); SB;
    asm volatile("s_nop 11" : "+v"(p0), "+v"(p1));
    float p[20];
#pragma unroll
    for (int i = 0; i < 20; i++) p[i] = i < 16 ? p0[i] : p1[i - 16];
    float mxa = max3(p[0], p[1], p[2]), mna = min3(p[0], p[1], p[2]), mxb = max3(p[3], p[4], p[5]), mnb = min3(p[3], p[4], p[5]);
#pragma unroll
    for (int i = 6; i < 18; i += 4) {
        mxa = max3(mxa, p[i], p[i + 1]); mna = min3(mna, p[i], p[i + 1]);
        mxb = max3(mxb, p[i + 2], p[i + 3]); mnb = min3(mnb, p[i + 2], p[i + 3]);
    }
    mxa = max3(mxa, p[18], p[19]); mna = min3(mna, p[18], p[19]);
    const float mx = max3(mxa, mxb, mxb), mn = min3(mna, mnb, mnb);
    const float L2E = 1.4426950408889634f;
    const float cx = -mx * L2E, cn = mn * L2E;
    float e[40], s1, s2;
#pragma unroll
    for (int i = 0; i < 20; i++) {
        e[i] = __builtin_amdgcn_exp2f(__builtin_fmaf(p[i], L2E, cx));
        e[20 + i] = __builtin_amdgcn_exp2f(__builtin_fmaf(p[i], -L2E, cn));
        if (i == 0) { s1 = e[0]; s2 = e[20]; } else { s1 += e[i]; s2 += e[20 + i]; }
    }
    const float r1 = __builtin_amdgcn_rcpf(s1), r2 = __builtin_amdgcn_rcpf(s2);
#pragma unroll
    for (int i = 0; i < 10; i++) { phi[i] = pk(e[2 * i] * r1, e[2 * i + 1] * r1); phi[10 + i] = pk(e[20 + 2 * i] * r2, e[21 + 2 * i] * r2); }
}

constexpr int WBLK = 960, WSLOT = 8 * WBLK, XSLOT = 24576;
constexpr int LW = XSLOT, LIMG = 102400, LMISC = LIMG + 2 * XSLOT, LZERO = LMISC + 3200, LTAB = LZERO + 64, TROW = 56, THEAD = 40 * TROW;
DI int xslot(int j) { return j % 3 == 0 ? 0 : LIMG + (j % 3 - 1) * XSLOT; }
static_assert(LTAB + 80 * TROW <= LDS_BYTES, "LDS map");

template <bool V> struct Flag { static constexpr bool value = V; };

struct Role { bool is_a; int r, c, ia; u32 nb; };
DI Role role_of(u32 bid) {
    const int xcd = bid & 7, j = bid >> 3, e = xcd & 1, m = xcd >> 1, nb = e ? 20 : 24, cq = e ? (j * 52) >> 8 : (j * 43) >> 8;
    Role R;
    R.is_a = j >= nb; R.nb = nb;
    R.r = __builtin_amdgcn_readfirstlane(e ? 6 + j - 5 * cq : j - 6 * cq); R.c = __builtin_amdgcn_readfirstlane(4 * m + cq);
    R.ia = __builtin_amdgcn_readfirstlane(20 * m + (e ? 8 : 0) + j - nb);
    return R;
}

DI void load_wo(const u16* wo, int cb, int wi, int end, int nw, int l) {
    u32 off[5];
#pragma unroll
    for (int i = 0; i < 5; i++) { const u32 idx = 64 * i + l, row = (idx * 6554u) >> 16; off[i] = row * 1120 + idx * 16; }
#pragma unroll 1
    for (int gq = wi; gq < end; gq += nw) {
        const char* gp = (const char*)wo + cb * 160 + gq * (32 * 1280);
#pragma unroll
        for (int i = 0; i < 5; i++) dma(gq * 5120 + i * 1024, off[i], gp);
    }
}

DI void out_proj(u16* __restrict__ out, char* __restrict__ ws, u64 tag, int ia STAMP_ARG) {
    const int bid = sbid(), tid = threadIdx.x, l = tid & 63, w = __builtin_amdgcn_readfirstlane(tid >> 6);
    const int t = 4 * (bid & 7) + (bid >> 6), cb = (bid >> 3) & 7, rs = (t * 43) >> 7, sub = t - 3 * rs;
    bar();
    if (w >= 4) {
        if (ia >= 0) {
            u32x2 v[8];
#pragma unroll
            for (int c2 = 0; c2 < 8; c2++) v[c2] = ldsr<u32x2>(131072 + c2 * 4096 + (tid - 256) * 8);
            const u32 op = 4 * (tid - 256) * 1280 + 8 * ia;
            bar();
            __builtin_amdgcn_s_sleep(YSLEEP);
#pragma unroll
            for (int i = 0; i < 4; i++) {
                const u32 sel = (i & 1) ? 0x07060302u : 0x05040100u;
                out16(out, op + i * 1280, u32x4{__builtin_amdgcn_perm(v[1][i >> 1], v[0][i >> 1], sel), __builtin_amdgcn_perm(v[3][i >> 1], v[2][i >> 1], sel),
                                                 __builtin_amdgcn_perm(v[5][i >> 1], v[4][i >> 1], sel), __builtin_amdgcn_perm(v[7][i >> 1], v[6][i >> 1], sel)});
            }
        }
        return;
    }
    constexpr int NPRE = 6;
    const auto rsw = __builtin_amdgcn_make_buffer_rsrc((void*)ws, 0, WS_END, 0x00020000);
    const int g = l >> 4, q = (l >> 2) & 3, p = l & 3, hp = g & 1, P = g >> 1;
    u32 b1 = (20 * hp + 8 * P + q) * 160 + p * 8, b2 = (80 * P + 20 * hp + 16 + q) * 160 + p * 8;
    u32 b1h = b1 + 51200, b2h = b2 + 51200;
    asm volatile("" : "+v"(b1), "+v"(b2), "+v"(b1h), "+v"(b2h));
    s16x4 wfr[NPRE][10];
    auto rdw = [&](int ks, int n) {
        const int G = ks / 5, i = ks % 5, tt = n >> 1, rd = n & 1;
        const u32 so = (G & 1) * 25600;
        wfr[ks % NPRE][n] = i < 4 ? trd((G >> 1 ? b1h : b1) + so + (40 * i + 4 * rd) * 160 + 32 * tt) : trd((G >> 1 ? b2h : b2) + so + 40 * rd * 160 + 32 * tt);
    };
#pragma unroll
    for (int ks = 0; ks < NPRE; ks++)
#pragma unroll
        for (int n = 0; n < 10; n++) rdw(ks, n);
    if (w == 0) wait_flags(ws + WS_F2 + (rs * 24 + sub * 8 + cb) * 256, tag, l, 16);
    bar();
    STAMP(8);
    const int ti = 16 * (w & 1) + (l & 15);
    const u32 ob = WS_O + (rs * 96 + 2 * sub + (w >> 1)) * 2560;
    const u32 v1 = P * 1024 + (hp * 32 + ti) * 16, v2 = 2048 + 2 * P * 15360 + (hp * 32 + ti) * 8;
    u32x4 of[16]; u32x2 ot[8];
    auto ldo = [&](int n) {
        const int G = n / 6, i = n % 6;
        if (i < 4) of[4 * G + i] = __builtin_amdgcn_raw_buffer_load_b128(rsw, v1, ob + (4 * G + i) * 15360, 16);
        else ot[2 * G + i - 4] = __builtin_amdgcn_raw_buffer_load_b64(rsw, v2, ob + (4 * G + i - 4) * 15360, 16);
    };
#pragma unroll
    for (int n = 0; n < 10; n++) ldo(n);
    f32x4 oc[5] = {};
#pragma unroll
    for (int ks = 0; ks < 20; ks++) {
        const int G = ks / 5, i = ks % 5;
        const s16x8 B = i < 4 ? __builtin_bit_cast(s16x8, of[4 * G + i]) : frag(ot[2 * G][0], ot[2 * G][1], ot[2 * G + 1][0], ot[2 * G + 1][1]);
#pragma unroll
        for (int tt = 0; tt < 5; tt++) {
            SB;
            oc[tt] = mfma16(cat(wfr[ks % NPRE][2 * tt], wfr[ks % NPRE][2 * tt + 1]), B, oc[tt]);
            SB;
            if (ks + NPRE < 20) { rdw(ks + NPRE, 2 * tt); rdw(ks + NPRE, 2 * tt + 1); }
            if ((tt == 0 || tt == 2) && 10 + 2 * ks + (tt >> 1) < 24) ldo(10 + 2 * ks + (tt >> 1));
        }
    }
    SB;
    const u32 op = (64 * t + 16 * w + (l & 15)) * 1280 + 640 + 80 * cb + 4 * g;
#pragma unroll
    for (int tt = 0; tt < 5; tt++) out8(out, op + 16 * tt, u32x2{pk(oc[tt][0], oc[tt][1]), pk(oc[tt][2], oc[tt][3])});
    STAMP(9);
    if (bid == 0 && w == 0) {
        wait_flags(ws + WS_ACK, tag, l, 64);
        wait_flags(ws + WS_ACK + 512, tag, l, 16);
        if (l == 0) __hip_atomic_store((GAS u64*)(ws + WS_EPOCH), tag, __ATOMIC_RELAXED, __HIP_MEMORY_SCOPE_AGENT);
    }
}

template <bool FIRST>
DI void loader_b(const u16* __restrict__ x, const u16* __restrict__ W, const u16* __restrict__ wo, int r, int c, int lw, u32& pfd) {
    const int l = threadIdx.x & 63;
    const int ph = (((r + 4 * c) & 15) * 10) >> 4;
    const int pl = l >> 4, idx = l & 15, q3 = (idx * 11) >> 5;
    const int q = pl < 3 ? idx >> 2 : q3 & 3, sg = pl < 3 ? pl : idx - 3 * q3, ch = pl < 3 ? (idx & 3) + ((idx >> 1) & 1) : 2;
    const u32 wl = q * 3840 + sg * 1280 + ch * 16 + (sg == 0 ? ((c & 1) ? 0 : 160) : 80);
    const u32 xo0 = (l >> 3) * 1280 + (((l & 7) ^ (l >> 4)) << 4), xo1 = ((l >> 3) + 8) * 1280 + (((l & 7) ^ ((l >> 4) | 4)) << 4);
    u32 xr[2][3];
#pragma unroll
    for (int i = 0; i < 6; i++) { const int row = 192 * r + 96 * (i / 3) + 48 * lw + 16 * (i % 3); xr[i / 3][i % 3] = (row < 2048 ? row : row - 64) * 1280; }
    const char* wb = (const char*)W + c * 80 - 80 + lw * (16 * 3840);
    auto ld_x = [&](int hx) {
        const int jx = hx >> 1, hh = hx & 1;
        int jr = jx + ph; if (jr >= 10) jr -= 10;
        const char* g = (const char*)x + jr * 128;
        const u32 d = xslot(jx) + (96 * hh + 48 * lw) * 128;
#pragma unroll
        for (int i = 0; i < 3; i++) { dma(d + i * 2048, xo0, g + xr[hh][i]); dma(d + i * 2048 + 1024, xo1, g + xr[hh][i]); }
    };
    auto ld_w = [&](int i) {
        int ir = i + 2 * ph; if (ir >= 20) ir -= 20;
        const char* g = wb + ir * (32 * 3840);
        const u32 d = LW + (i & 3) * WSLOT + lw * 4 * WBLK;
#pragma unroll
        for (int b = 0; b < 4; b++) dma_m(d + b * WBLK, wl, g + b * (4 * 3840), 0xffffffffu, 0x0fffffffu);
    };
    if (FIRST) { ld_w(0); ld_x(0); ld_x(1); return; }
    ld_w(1); ld_x(2); ld_x(3);
    waitv<16>();
    bar();
    ld_w(2); ld_w(3); ld_x(4);
#pragma unroll
    for (int i = 0; i < 19; i++) {
        if (i == 0 || i == 2) waitv<26>(); else if (i <= 15) waitv<20>(); else if (i == 16) waitv<14>(); else if (i == 17) waitv<4>(); else waitv<2>();
        bar();
        if (i + 5 < 20) ld_x(i + 5);
        if (i + 4 < 20) ld_w(i + 4);
        if (i == 17) {
            const u32 ln = __builtin_amdgcn_readfirstlane(min((sbid() >> 3) * 256 + lw * 64, 6208u)) + l;
            asm volatile("global_load_dword %0, %1, %2\n\tglobal_load_dword %0, %1, %3" : "+v"(pfd) : "v"(ln * 128), "s"(wo), "s"((const char*)wo + 128 * 128));
        }
    }
}

DI u64 path_b(const u16* __restrict__ x, const u16* __restrict__ W, const u16* __restrict__ wphi, const u16* __restrict__ wo,
               u16* __restrict__ out, char* __restrict__ ws, int r, int c, u32& pfd STAMP_ARG) {
    const int tid = threadIdx.x, l = tid & 63;
    const int w = __builtin_amdgcn_readfirstlane(tid >> 6);
    const auto rsw = __builtin_amdgcn_make_buffer_rsrc((void*)ws, 0, WS_END, 0x00020000);
    const int g = l >> 4, q = (l >> 2) & 3, p = l & 3, h = l >> 5;
    const int rq = 2 * (g & 1) + (p >> 1), hk = g >> 1;
    const bool same = hk == (p & 1);
    u64 tag;
    u32 phiq[20];
    f32x16 acc[4] = {};
    s16x8 wfq[2][3];
    if (w >= 6) {
        loader_b<false>(x, W, wo, r, c, w - 6, pfd);
        asm volatile("s_load_dwordx2 %0, %1, 0\n\ts_waitcnt lgkmcnt(0)" : "=&s"(tag) : "s"(ws + WS_EPOCH) : "memory");
    } else {
        if (w < 4) dma_m(LMISC + w * 800, l * 16, (const char*)wphi + ((c ^ (w >> 1)) * 2 + (w & 1)) * 800, 0xffffffffu, 0x3ffffu);
        if (tid < 2) ldsw<u64>(LZERO + tid * 8, 0ull);
        u32 bA = LW + (g >> 1) * 2 * WBLK + q * 64 + (p & 1) * 32 + (g & 1) * 16 + (p >> 1) * 8;
        u32 bD = LW + (g >> 1) * 2 * WBLK + 768 + (3 * q + 2 * (g & 1) + (p >> 1)) * 16 + (p & 1) * 8;
        asm volatile("" : "+v"(bA), "+v"(bD));
        const int trow = 32 * w + (l & 31);
        s16x8 wfk[2][3];
        auto load_wf = [&](s16x8 (&wf)[2][3], u32 base) {
#pragma unroll
            for (int to = 0; to < 2; to++)
#pragma unroll
                for (int s = 0; s < 3; s++) {
                    s16x4 t[2];
#pragma unroll
                    for (int rd = 0; rd < 2; rd++) {
                        const int mu = 2 * s + rd;
                        if (mu < 5) {
                            int dq = mu + hk; if (mu == 4) dq = hk ? 0 : 4;
                            const bool ok = same && (to == 0 || rq == 0);
                            t[rd] = trd(ok ? base + hk * 800 + (4 * dq + q) * 40 + (4 * to + rq) * 8 : LZERO);
                        } else t[rd] = s16x4{0, 0, 0, 0};
                    }
                    wf[to][s] = cat(t[0], t[1]);
                }
        };
        asm volatile("s_load_dwordx2 %0, %1, 0\n\ts_waitcnt vmcnt(0) lgkmcnt(0)" : "=&s"(tag) : "s"(ws + WS_EPOCH), "v"(pfd) : "memory");
        bar();
        STAMP(1);
        const u32 sw = ((trow & 2) << 1) | ((trow & 4) >> 1) | (trow & 1);
        const u32 rowa = LIMG + h * XSLOT + trow * 128;
        auto gemm = [&](auto HV) {
            constexpr bool hv = decltype(HV)::value;
            constexpr int NB = hv ? 3 : 1, NM = hv ? 6 : 3, NR = hv ? 11 : 7;
            f32x16 vc[3] = {};
            u32 aX[4], aXh[4];
            const int tx = hv ? 32 * (w - 2) + (l & 31) : trow;
#pragma unroll
            for (int k = 0; k < 4; k++) {
                aX[k] = tx * 128 + (((2 * k + h) ^ ((tx >> 1) & 7)) << 4); aXh[k] = aX[k] + LIMG;
                asm volatile("" : "+v"(aX[k]), "+v"(aXh[k]));
            }
            s16x4 fa[2][8]; s16x8 fb[2][NB];
            auto rd = [&](int s, int i, int ks, int n) {
                const int nb = hv ? (n == 0 ? 1 : n == 9 ? 0 : n == 10 ? 2 : -1) : (n == 0 ? 0 : -1);
                if (nb >= 0) {
                    const int sl = (i >> 1) % 3, k = (i & 1) * 2 + ks;
                    fb[s][nb] = sl == 0 ? ldsr<s16x8>(aX[k] + nb * 8192) : ldsr<s16x8>(aXh[k] + (sl - 1) * XSLOT + nb * 8192);
                } else {
                    const int na = n <= 4 ? n - 1 : n <= 6 ? n + 1 : n - 3;
                    const u32 o = (i & 3) * WSLOT + ks * 4 * WBLK + (na & 1) * WBLK;
                    fa[s][na] = na < 6 ? trd(bA + o + (na >> 1) * 256) : trd(bD + o);
                }
            };
            auto mm = [&](int s, int t) {
                if (t < 3) { const int tt = t == 2 ? 3 : t; acc[tt] = mfma32(cat(fa[s][2 * tt], fa[s][2 * tt + 1]), fb[s][hv ? 1 : 0], acc[tt]); }
                else { const int nb = t == 3 ? 1 : t == 4 ? 0 : 2; vc[nb] = mfma32(cat(fa[s][4], fa[s][5]), fb[s][hv ? nb : 0], vc[nb]); }
            };
            auto halfstep = [&](int s, int i, int ks, bool next) {
#pragma unroll
                for (int t = 0; t < NM; t++) {
                    SB; mm(s, t); SB;
                    if (t == 0 && ks == 0 && next) bar();
                    if (next)
#pragma unroll
                        for (int n = 3 * t; n < 3 * t + 3 && n < NR; n++) rd(s ^ 1, i, ks, n);
                }
            };
#pragma unroll
            for (int n = 0; n < NR; n++) rd(0, 0, 0, n);
#pragma unroll
            for (int i = 0; i < 20; i++) {
                halfstep(0, i, 1, true);
                if (i == 1) load_wf(wfk, LMISC);
                if (i == 2) load_wf(wfq, LMISC + 1600);
                halfstep(1, i + 1, 0, i < 19);
            }
            if (hv) {
#pragma unroll
                for (int nb = 0; nb < 3; nb++)
#pragma unroll
                    for (int mu = 0; mu < 4; mu++) {
                        const int pc = 10 + mu + h;
                        ldsw<u32x2>(rowa + (nb - 1) * 8192 + (((pc >> 1) ^ sw) << 4) + (pc & 1) * 8,
                                    u32x2{pk(vc[nb][4 * mu], vc[nb][4 * mu + 1]), pk(vc[nb][4 * mu + 2], vc[nb][4 * mu + 3])});
                    }
            }
        };
        if ((w & 3) >= 2) gemm(Flag<true>{}); else gemm(Flag<false>{});
        STAMP(2);
        float kr[20]; u32 phik[20];
#pragma unroll
        for (int i = 0; i < 20; i++) kr[i] = i < 16 ? acc[1][i] : acc[3][i - 12];
        features(kr, wfk, phik, [&]() {
            const int pc = h ? 10 : 14;
            ldsw<u32x2>(rowa + (((pc >> 1) ^ sw) << 4) + (pc & 1) * 8, u32x2{pk(acc[3][8], acc[3][9]), pk(acc[3][10], acc[3][11])});
            ldsw<u32x2>(rowa + ((7 ^ sw) << 4) + 8, u32x2{0x3f80u, 0u});
        });
        const u32 keep = 192 * r + 32 * w < 2048 ? 0xffffffffu : 0u;
#pragma unroll
        for (int ch = 0; ch < 5; ch++) ldsw<u32x4>(rowa + ((ch ^ sw) << 4), u32x4{phik[4 * ch], phik[4 * ch + 1], phik[4 * ch + 2], phik[4 * ch + 3]} & keep);
        STAMP(10);
    }
    tag += 1;
    bar();
    if (w >= 6) load_wo(wo, (sbid() >> 3) & 7, w - 6, 10, 2, l);
    if (w < 4) {
        const int pcn = 4 * (g & 1) + p;
        const int pa = 10 + (pcn < 6 ? pcn : 5), pb = 5 * (w & 1) + (pcn < 5 ? pcn : 4);
        u32 aa[2], ab[2];
#pragma unroll
        for (int rd = 0; rd < 2; rd++) {
            const int row = 8 * (g >> 1) + 4 * rd + q;
            const u32 sw = ((row & 2) << 1) | ((row & 4) >> 1) | (row & 1);
            const u32 base = LIMG + (w >> 1) * XSLOT + row * 128;
            aa[rd] = base + (((pa >> 1) ^ sw) << 4) + (pa & 1) * 8;
            ab[rd] = base + (((pb >> 1) ^ sw) << 4) + (pb & 1) * 8;
            asm volatile("" : "+v"(aa[rd]), "+v"(ab[rd]));
        }
        s16x4 ta[24], tb[24];
#pragma unroll
        for (int ks = 0; ks < 12; ks++) {
            ta[2 * ks] = trd(aa[0] + ks * 2048); ta[2 * ks + 1] = trd(aa[1] + ks * 2048);
            tb[2 * ks] = trd(ab[0] + ks * 2048); tb[2 * ks + 1] = trd(ab[1] + ks * 2048);
        }
        SB;
        f32x16 kv = {};
#pragma unroll
        for (int ks = 0; ks < 12; ks++) kv = mfma32(cat(ta[2 * ks], ta[2 * ks + 1]), cat(tb[2 * ks], tb[2 * ks + 1]), kv);
        if ((l & 31) < 20) {
            const u32 lam = h * 20 + (l & 31), o = WS_PART + (c * 16 + r) * 4096 + w * 960;
            st128(u32x4{pk(kv[0], kv[1]), pk(kv[2], kv[3]), pk(kv[4], kv[5]), pk(kv[6], kv[7])}, rsw, lam * 16, o, 16);
            __builtin_amdgcn_raw_buffer_store_b64(u32x2{pk(kv[8], kv[9]), pk(kv[10], kv[11])}, rsw, 640 + lam * 8, o, 16);
        }
    }
    STAMP(11);
    if (w < 6) {
        float qr[20];
#pragma unroll
        for (int i = 0; i < 20; i++) qr[i] = i < 16 ? acc[0][i] : acc[3][i - 16];
        features(qr, wfq, phiq, []() {});
#pragma unroll
        for (int i = 0; i < 20; i++) asm volatile("" : "+v"(phiq[i]));
        STAMP(17);
        waitv<0>();
    }
    bar();
    post_flag(ws + WS_F1 + c * 4096, tag, tid, 11, r);
    STAMP(3);
    if (w >= 6) load_wo(wo, (sbid() >> 3) & 7, w + 4, 20, 2, l);
    if (tid < 80) ldsw<u64>(LTAB + tid * TROW + 48, 0ull);
    if (w == 0) wait_flags(ws + WS_F1 + (c ^ 1) * 4096 + r * 256, tag, l, 11);
    bar();
    STAMP(5);
    if (w < 4) {
        const int t = tid < 240 ? tid : 239;
        const int un = (t * 1093) >> 16, it = t - un * 60;
        u32x4 pv[11];
#pragma unroll
        for (int i = 0; i < 11; i++) pv[i] = __builtin_amdgcn_raw_buffer_load_b128(rsw, t * 16, WS_PART + (c ^ 1) * 65536 + i * 4096, 16);
        float s[8] = {};
        u32 slo = 0x00003f80u, shi = 0x3f800000u;
        asm volatile("" : "+s"(slo), "+s"(shi));
#pragma unroll
        for (int i = 0; i < 11; i++)
#pragma unroll
            for (int e = 0; e < 4; e++) { s[2 * e] = dot2(pv[i][e], slo, s[2 * e]); s[2 * e + 1] = dot2(pv[i][e], shi, s[2 * e + 1]); }
        const int lam = it < 40 ? it : 2 * (it - 40);
        const int hh = lam >= 20, f = lam - 20 * hh;
        const u32 a0 = LTAB + (un >> 1) * THEAD + ((un & 1) * 20 + f) * TROW + hh * 8 + (it < 40 ? 0 : 32);
        ldsw<u32x2>(a0, u32x2{pk(s[0], s[1]), pk(s[2], s[3])});
        ldsw<u32x2>(a0 + (it < 40 ? 16 : TROW), u32x2{pk(s[4], s[5]), pk(s[6], s[7])});
    }
    bar();
    STAMP(6);
    if (w < 6 && 192 * r + 32 * w < 2048) {
        const u32 tb = LTAB + hk * THEAD + q * TROW;
        u32 t0 = tb + (same ? rq : 6) * 8, t1 = tb + (same && rq < 2 ? 4 + rq : 6) * 8;
        asm volatile("" : "+v"(t0), "+v"(t1));
        f32x16 o0 = {}, o1 = {};
#pragma unroll
        for (int s = 0; s < 5; s++) {
            const s16x8 B = frag(phiq[4 * s], phiq[4 * s + 1], phiq[4 * s + 2], phiq[4 * s + 3]);
            o0 = mfma32(cat(trd(t0 + 8 * s * TROW), trd(t0 + (8 * s + 4) * TROW)), B, o0);
            o1 = mfma32(cat(trd(t1 + 8 * s * TROW), trd(t1 + (8 * s + 4) * TROW)), B, o1);
        }
        const float rden = __builtin_amdgcn_rcpf(o1[4] + 1e-6f);
        u32 ob[10];
#pragma unroll
        for (int i = 0; i < 10; i++) ob[i] = i < 8 ? pk(o0[2 * i] * rden, o0[2 * i + 1] * rden) : pk(o1[2 * i - 16] * rden, o1[2 * i - 15] * rden);
        const u32 o = WS_O + ((r * 16 + (c ^ 1)) * 6 + w) * 2560;
        st128(u32x4{ob[0], ob[1], ob[2], ob[3]}, rsw, l * 16, o, 16);
        st128(u32x4{ob[4], ob[5], ob[6], ob[7]}, rsw, l * 16 + 1024, o, 16);
        __builtin_amdgcn_raw_buffer_store_b64(u32x2{ob[8], ob[9]}, rsw, l * 8 + 2048, o, 16);
    }
    STAMP(12);
    waitv<0>();
    asm volatile("" ::"v"(pfd));
    bar();
    post_flag(ws + WS_F2 + r * (24 * 256), tag, tid, 24, c);
    STAMP(7);
    return tag;
}

DI f32x2 cmul(f32x2 a, f32x2 b) {
    f32x2 d;
    asm("v_pk_mul_f32 %0, %1, %2 op_sel_hi:[0,1]\n\tv_pk_fma_f32 %0, %1, %2, %0 op_sel:[1,1,0] op_sel_hi:[1,0,1] neg_lo:[0,1,0]" : "=&v"(d) : "v"(a), "v"(b));
    return d;
}
DI f32x2 cmulc(f32x2 a, f32x2 b) {
    f32x2 d;
    asm("v_pk_mul_f32 %0, %1, %2 op_sel_hi:[0,1] neg_hi:[0,1]\n\tv_pk_fma_f32 %0, %1, %2, %0 op_sel:[1,1,0] op_sel_hi:[1,0,1]" : "=&v"(d) : "v"(a), "v"(b));
    return d;
}
DI f32x2 csq(f32x2 a) { return cmul(a, a); }
DI f32x2 cexp(float a, float b) {
    const float m = __builtin_amdgcn_exp2f(a * 1.4426950408889634f);
    const float c1 = 0.15915494309189535f, c2 = 6.4206382e-9f;
    const float n = __builtin_rintf(b * c1);
    const float f = __builtin_fmaf(b, c2, __builtin_fmaf(b, c1, -n));
    return f32x2{m * __builtin_amdgcn_cosf(f), m * __builtin_amdgcn_sinf(f)};
}
constexpr int LU = 131072;
DI u32 kaddr(u32 e) { return ((e >> 5) << 8) + ((e & 31) << 1); }

DI void prologue_a(int ia, const u16* __restrict__ Are, const u16* __restrict__ Aim, const u16* __restrict__ Bre, const u16* __restrict__ Bim,
                   const u16* __restrict__ Cre, const u16* __restrict__ Cim, const u16* __restrict__ logdt, const u16* __restrict__ Dskip) {
    const int l = threadIdx.x & 63, w = __builtin_amdgcn_readfirstlane(threadIdx.x >> 6);
    const u16* src = w == 0 ? Are : w == 1 ? Aim : w == 2 ? Bre : w == 3 ? Bim : w == 4 ? Cre : w == 5 ? Cim : w == 6 ? logdt : Dskip;
    if (w < 6) { dma(w * 16384 + 8192, ia * 2048 + l * 16, src); dma(w * 16384 + 9216, ia * 2048 + 1024 + l * 16, src); }
    else dma_m(w * 16384 + 8192, ia * 16, src, 1u, 0u);
}

DI u64 path_a(const u16* __restrict__ u, const u16* __restrict__ wo, u16* __restrict__ out, char* __restrict__ ws, int ia, u32 pfd STAMP_ARG) {
    const int tid = threadIdx.x, l = tid & 63;
    const int w = __builtin_amdgcn_readfirstlane(tid >> 6);
    const u32 KB = w * 16384;
    const int g = l >> 4, q = (l >> 2) & 3, p = l & 3, h = l >> 5;
    STAMP(1);
    u64 tag;
    asm volatile("s_load_dwordx2 %0, %1, 0\n\ts_waitcnt vmcnt(0) lgkmcnt(0)" : "=&s"(tag) : "s"(ws + WS_EPOCH), "v"(pfd) : "memory");
    tag += 1;
    bar();
    if (tid == 0) __hip_atomic_store((GAS u64*)(ws + WS_ACK + ia * 8), tag, __ATOMIC_RELAXED, __HIP_MEMORY_SCOPE_AGENT);
    const float ldt = b2f(ldsr<u16>(6 * 16384 + 8192 + w * 2)), dsk = b2f(ldsr<u16>(7 * 16384 + 8192 + w * 2));
    u32 prm[6];
#pragma unroll
    for (int i = 0; i < 6; i++) prm[i] = ldsr<u32>(i * 16384 + 8192 + w * 256 + l * 4);
    bar();
    u32x4 ur[4];
    auto u_in = [&]() {
#pragma unroll
        for (int i = 0; i < 4; i++) ur[i] = *(const u32x4*)((const char*)u + (size_t)(256 * ((w + ia) & 7) + 4 * l + i) * 1280 + ia * 16);
    };
    if (UISS == 0) u_in();
    const float dt = lo2f(pk(expf(ldt), 0.f));
    STAMP(2);
    f32x16 kd = {};
    u32 ra[2];
#pragma unroll
    for (int rd = 0; rd < 2; rd++) ra[rd] = KB + (8 * (g >> 1) + 4 * rd + q) * 64 + (((2 * (g & 1) + (p >> 1)) ^ ((2 * rd + (q >> 1)) & 3)) << 4) + (p & 1) * 8;
    const u32 wa = KB + l * 64, wsw = (l >> 1) & 3;
#pragma unroll
    for (int ps = 0; ps < 2; ps++) {
        float pf[6];
#pragma unroll
        for (int i = 0; i < 6; i++) pf[i] = ps ? hi2f(prm[i]) : lo2f(prm[i]);
        const float a = dt * pf[0], b = dt * pf[1];
        const f32x2 Wc = cmulc(f32x2{pf[4], -pf[5]}, f32x2{dt * pf[2], dt * pf[3]});
        const f32x2 e1 = cexp(a, b), e16 = cexp(16.f * a, 16.f * b), e256 = cexp(256.f * a, 256.f * b);
        const f32x2 e2 = csq(e1), e4 = csq(e2), e8 = csq(e4), e32 = csq(e16), e64 = csq(e32), e128 = csq(e64), e512 = csq(e256), e1024 = csq(e512);
        f32x2 X[32];
        X[0] = Wc; X[1] = cmulc(Wc, e1); X[2] = cmulc(Wc, e2); X[3] = cmulc(X[1], e2);
#pragma unroll
        for (int i = 0; i < 4; i++) X[4 + i] = cmulc(X[i], e4);
        X[8] = cmulc(Wc, e256); X[9] = cmulc(X[8], e2); X[10] = cmulc(X[8], e4);
        X[12] = X[10]; X[13] = cmulc(X[9], e4); X[14] = cmulc(X[8], e8);
        X[11] = cmulc(Wc, e512); X[16] = cmulc(X[11], e4); X[17] = cmulc(X[11], e8);
        X[15] = cmulc(X[8], e512); X[20] = cmulc(X[15], e4); X[21] = cmulc(X[15], e8);
        X[18] = cmulc(Wc, e1024); X[19] = cmulc(X[18], e8);
        X[24] = cmulc(X[8], e1024); X[25] = cmulc(X[24], e8);
        X[22] = cmulc(X[11], e1024); X[23] = cmulc(X[22], e8);
        X[28] = cmulc(X[15], e1024); X[29] = cmulc(X[28], e8);
        X[26] = X[27] = X[30] = X[31] = f32x2{0.f, 0.f};
        f32x2 Y[8];
        Y[0] = f32x2{1.f, 0.f}; Y[1] = e8; Y[2] = e16; Y[3] = cmul(e8, e16); Y[4] = e32; Y[5] = cmul(e8, e32); Y[6] = cmul(e16, e32); Y[7] = cmul(Y[3], e32);
        const f32x2 e192 = cmul(e64, e128);
#pragma unroll
        for (int ch = 0; ch < 4; ch++) {
            const f32x2* Xc = X + 8 * ch;
            const u32 o = wa + ((ch ^ wsw) << 4);
            ldsw<u32x4>(o, u32x4{pk(Xc[0].x, Xc[1].x), pk(Xc[2].x, Xc[3].x), pk(Xc[4].x, Xc[5].x), pk(Xc[6].x, Xc[7].x)});
            ldsw<u32x4>(o + 4096, u32x4{pk(Xc[0].y, Xc[1].y), pk(Xc[2].y, Xc[3].y), pk(Xc[4].y, Xc[5].y), pk(Xc[6].y, Xc[7].y)});
            f32x2 Z[8];
            const f32x2 m = ch == 1 ? e64 : ch == 2 ? e128 : e192;
#pragma unroll
            for (int i = 0; i < 8; i++) Z[i] = ch == 0 ? Y[i] : i == 0 ? m : cmul(Y[i], m);
            ldsw<u32x4>(o + 8192, u32x4{pk(Z[0].x, Z[1].x), pk(Z[2].x, Z[3].x), pk(Z[4].x, Z[5].x), pk(Z[6].x, Z[7].x)});
            ldsw<u32x4>(o + 12288, u32x4{pk(Z[0].y, Z[1].y), pk(Z[2].y, Z[3].y), pk(Z[4].y, Z[5].y), pk(Z[6].y, Z[7].y)});
        }
        s16x4 fx[16], fy[16];
#pragma unroll
        for (int ks = 0; ks < 8; ks++) {
            fx[2 * ks] = trd(ra[0] + ks * 1024); fx[2 * ks + 1] = trd(ra[1] + ks * 1024);
            fy[2 * ks] = trd(ra[0] + ks * 1024 + 8192); fy[2 * ks + 1] = trd(ra[1] + ks * 1024 + 8192);
        }
        if (ps == 0) { STAMP(4); if (UISS == 1) u_in(); }
#pragma unroll
        for (int ks = 0; ks < 8; ks++) kd = mfma32(cat(fx[2 * ks], fx[2 * ks + 1]), cat(fy[2 * ks], fy[2 * ks + 1]), kd);
    }
    STAMP(6);
    {
        const int j = l & 31;
        ldsw<u32x2>(KB + kaddr(2044 - 8 * j - 4 * h), u32x2{pk(kd[3], kd[2]), pk(kd[1], l == 0 ? kd[0] + dsk : kd[0])});
        ldsw<u32x2>(KB + kaddr(2044 - 256 - 8 * j - 4 * h), u32x2{pk(kd[6], kd[5]), pk(kd[4], kd[4])});
        ldsw<u32x4>(KB + kaddr(2040 - 512 - 256 * h - 8 * j), u32x4{pk(kd[9], kd[9]), pk(kd[8], kd[8]), pk(kd[8], kd[7]), pk(kd[7], kd[7])});
#pragma unroll
        for (int sp = 0; sp < 2; sp++) {
            const float a = kd[10 + 2 * sp], b = kd[11 + 2 * sp];
            const u32 aa = pk(a, a), bb = pk(b, b);
            ldsw<u32x4>(KB + kaddr(2040 - 1024 - 512 * h - 256 * sp - 8 * j), u32x4{bb, (j & 1) ? bb : pk(b, a), aa, aa});
        }
        u32x4 v[4]; u32x2 n[4];
        const u32 a0 = KB + kaddr(8 * l), a1 = KB + kaddr(8 * l + 8);
#pragma unroll
        for (int it = 0; it < 4; it++) { v[it] = ldsr<u32x4>(a0 + it * 4096); n[it] = ldsr<u32x2>(a1 + it * 4096); }
#pragma unroll
        for (int it = 0; it < 4; it++) {
            const u32 s0 = __builtin_amdgcn_alignbit(v[it][1], v[it][0], 16), s1 = __builtin_amdgcn_alignbit(v[it][2], v[it][1], 16),
                      s2 = __builtin_amdgcn_alignbit(v[it][3], v[it][2], 16), s3 = __builtin_amdgcn_alignbit(n[it][0], v[it][3], 16),
                      s4 = __builtin_amdgcn_alignbit(n[it][1], n[it][0], 16);
            ldsw<u32x4>(a0 + it * 4096 + 64, u32x4{s0, s1, s2, s3});
            ldsw<u32x4>(a0 + it * 4096 + 128, u32x4{v[it][1], v[it][2], v[it][3], n[it][0]});
            ldsw<u32x4>(a0 + it * 4096 + 192, u32x4{s1, s2, s3, s4});
        }
    }
    STAMP(5);
    {
        const u32 a = LU + (256 * ((w + ia) & 7) + 4 * l) * 2;
#pragma unroll
        for (int c2 = 0; c2 < 4; c2++) {
            ldsw<u32x2>(a + 2 * c2 * 4096, u32x2{__builtin_amdgcn_perm(ur[1][c2], ur[0][c2], 0x05040100u), __builtin_amdgcn_perm(ur[3][c2], ur[2][c2], 0x05040100u)});
            ldsw<u32x2>(a + (2 * c2 + 1) * 4096, u32x2{__builtin_amdgcn_perm(ur[1][c2], ur[0][c2], 0x07060302u), __builtin_amdgcn_perm(ur[3][c2], ur[2][c2], 0x07060302u)});
        }
    }
    STAMP(3);
    bar();
    STAMP(10);
    const int rr = l & 15;
    const u32 e0 = 2047 - 16 - rr + 8 * g, sh = e0 & 3;
    auto half = [&](auto HI) {
        constexpr bool hi = decltype(HI)::value;
        constexpr int NF = hi ? 8 : 4, T0 = hi ? 4 : 0, NRD = 3 * NF;
        f32x4 y[4] = {};
        u32 ka0 = KB + kaddr(e0 - sh) + sh * 64 - (NF - 1) * 2048, ka1 = KB + kaddr(e0 - sh + 4) + sh * 64 - (NF - 1) * 2048;
        u32 ua = LU + w * 4096 + 32 * rr + 16 * g - 32;
        u32 mk[4];
#pragma unroll
        for (int i = 0; i < 4; i++) mk[i] = (2 * i > 16 + rr - 8 * g ? 0u : 0xffffu) | (2 * i + 1 > 16 + rr - 8 * g ? 0u : 0xffff0000u);
        int lim = 2 * rr + g - 2;
        u32x2 A[2][2 * NF]; u32x4 B[2][NF];
        auto rdc = [&](int s, int n) {
            const int m = n / 3, k = n % 3;
            if (k == 0) B[s][m] = ldsr<u32x4>(ua + m * 512);
            else if (k == 1) A[s][2 * (NF - 1 - m)] = ldsr<u32x2>(ka0 + m * 2048);
            else A[s][2 * (NF - 1 - m) + 1] = ldsr<u32x2>(ka1 + m * 2048);
        };
        auto group = [&](int s, bool next) {
            A[s][0] &= u32x2{mk[0], mk[1]}; A[s][1] &= u32x2{mk[2], mk[3]};
            if (lim < 0) B[s][0] = u32x4{0, 0, 0, 0};
            ka0 -= 256; ka1 -= 256; ua -= 64; lim -= 4;
            mk[0] = mk[1] = mk[2] = mk[3] = 0xffffffffu;
            asm volatile("" : "+v"(ka0), "+v"(ka1), "+v"(ua));
            int n = 0;
#pragma unroll
            for (int m = NF - 1; m >= 0; m--)
#pragma unroll
                for (int ct = NF - 1 - m; ct >= 0 && ct + m >= T0; ct--) {
                    SB;
                    y[ct + m - T0] = mfma16(frag(A[s][2 * m][0], A[s][2 * m][1], A[s][2 * m + 1][0], A[s][2 * m + 1][1]), __builtin_bit_cast(s16x8, B[s][ct]), y[ct + m - T0]);
                    SB;
                    if (next && n < NRD) rdc(s ^ 1, n);
                    n++;
                    if (!hi && next && (n == 1 || n == 6)) rdc(s ^ 1, n++);
                }
        };
        asm volatile("" : "+v"(ka0), "+v"(ka1), "+v"(ua));
#pragma unroll
        for (int n = 0; n < NRD; n++) rdc(0, n);
#pragma unroll 1
        for (int gg = 0; gg < 3; gg++) { group(0, true); group(1, true); }
        group(0, true); group(1, false);
#pragma unroll
        for (int ct = 0; ct < 4; ct++) ldsw<u32x2>(LU + w * 4096 + (T0 + ct) * 512 + rr * 32 + g * 8, u32x2{pk(y[ct][0], y[ct][1]), pk(y[ct][2], y[ct][3])});
        if (hi) STAMP(14); else STAMP(16);
        bar();
        if (hi) STAMP(15); else STAMP(17);
        if (!hi) return;
        u32 v[8];
#pragma unroll
        for (int c2 = 0; c2 < 8; c2++) v[c2] = ldsr<u32>(LU + c2 * 4096 + T0 * 512 + tid * 4);
        const u32 op = (T0 * 256 + 2 * tid) * 1280 + 8 * ia;
        out16(out, op, u32x4{__builtin_amdgcn_perm(v[1], v[0], 0x05040100u), __builtin_amdgcn_perm(v[3], v[2], 0x05040100u),
                            __builtin_amdgcn_perm(v[5], v[4], 0x05040100u), __builtin_amdgcn_perm(v[7], v[6], 0x05040100u)});
        out16(out, op + 1280, u32x4{__builtin_amdgcn_perm(v[1], v[0], 0x07060302u), __builtin_amdgcn_perm(v[3], v[2], 0x07060302u),
                                     __builtin_amdgcn_perm(v[5], v[4], 0x07060302u), __builtin_amdgcn_perm(v[7], v[6], 0x07060302u)});
    };
    half(Flag<true>{});
    STAMP(11);
    half(Flag<false>{});
    STAMP(12);
    load_wo(wo, (sbid() >> 3) & 7, w, 20, 8, l);
    waitv<0>();
    STAMP(7);
    return tag;
}

__global__ void __launch_bounds__(512, 2)
fused_kernel(const u16* __restrict__ x, const u16* __restrict__ W, const u16* __restrict__ wphi, const u16* __restrict__ wo,
             u16* __restrict__ out, char* __restrict__ ws, const u16* __restrict__ u, const u16* __restrict__ Are,
             const u16* __restrict__ Aim, const u16* __restrict__ Bre, const u16* __restrict__ Bim, const u16* __restrict__ Cre,
             const u16* __restrict__ Cim, const u16* __restrict__ logdt, const u16* __restrict__ Dskip) {
    __shared__ __attribute__((aligned(16))) char smem[LDS_BYTES];
    asm volatile("" ::"s"((u32)(size_t)(LAS char*)smem));
    u32 pfd = 0;
    if (threadIdx.x < 12) {
        u64 pc; asm volatile("s_getpc_b64 %0" : "=s"(pc));
        asm volatile("global_load_dword %0, %1, %2" : "+v"(pfd) : "v"(threadIdx.x * 128), "s"(pc + min((sbid() >> 3) * 1536, CODE_PF_BYTES - 1536)));
    }
    asm volatile("" ::"s"(x), "s"(W), "s"(wphi), "s"(wo), "s"(out), "s"(ws), "s"(u), "s"(Are), "s"(Aim), "s"(Bre), "s"(Bim), "s"(Cre), "s"(Cim),
                 "s"(logdt), "s"(Dskip));
    const Role R = role_of(sbid());
    u64 tag;
    if (R.is_a) prologue_a(R.ia, Are, Aim, Bre, Bim, Cre, Cim, logdt, Dskip);
    else if (__builtin_amdgcn_readfirstlane(threadIdx.x) >= 384) loader_b<true>(x, W, wo, R.r, R.c, __builtin_amdgcn_readfirstlane(threadIdx.x >> 6) - 6, pfd);
    u32 sel = R.nb;
    asm volatile("" : "+s"(sel));
    if ((sbid() >> 3) >= sel) {
        tag = path_a(u, wo, out, ws, R.ia, pfd STAMP_PASS);
    } else {
        tag = path_b(x, W, wphi, wo, out, ws, R.r, R.c, pfd STAMP_PASS);
    }
    out_proj(out, ws, tag, R.is_a ? R.ia : -1 STAMP_PASS);
}

extern "C" void kernel_launch(void* const* d_in, const int* in_sizes, int n_in, void* d_out, int out_size, void* d_ws, size_t ws_size,
                              hipStream_t stream) {
    const u16* const* in = (const u16* const*)d_in;
    fused_kernel<<<256, 512, 0, stream>>>(in[9], in[10], in[11], in[12], (u16*)d_out, (char*)d_ws, in[0], in[1], in[2], in[3], in[4], in[5],
                                          in[6], in[7], in[8]);
}
```

```cpp
#include <hip/hip_runtime.h>
#include <hip/hip_bf16.h>

typedef unsigned short u16;
typedef unsigned u32;
typedef unsigned long long u64;
typedef __attribute__((ext_vector_type(8))) short s16x8;
typedef __attribute__((ext_vector_type(4))) short s16x4;
typedef __attribute__((ext_vector_type(16))) float f32x16;
typedef __attribute__((ext_vector_type(4))) float f32x4;
typedef __attribute__((ext_vector_type(2))) float f32x2;
typedef __attribute__((ext_vector_type(4))) u32 u32x4;
typedef __attribute__((ext_vector_type(2))) u32 u32x2;
typedef __attribute__((ext_vector_type(2))) __bf16 bf16x2;
#define LAS __attribute__((address_space(3)))
#define GAS __attribute__((address_space(1)))
#define DI __device__ __forceinline__
#define SB __builtin_amdgcn_sched_barrier(0)
DI unsigned sbid() { return __builtin_amdgcn_workgroup_id_x(); }

#define PSLEEP 4
#define STW 0
#define YSLEEP 12
#define AUXA 0
#define AUXB 0
#define RMAP 0
#define UISS 1
#define UPOS 1
#define WOPOS 0
#define YMODE 0
constexpr int LDS_BYTES = 163840;
constexpr u32 WS_EPOCH = 0, WS_ACK = 4096, WS_F1 = 65536, WS_F2 = 131072, WS_PART = 524288, WS_O = 2u << 20, WS_END = 5u << 20;
constexpr u32 WS_STAMP = 16u << 20;
constexpr u32 kSpinCap = 1u << 18;
constexpr u32 CODE_PF_BYTES = 44032;

DI u32 pk(float a, float b) { f32x2 v = {a, b}; return __builtin_bit_cast(u32, __builtin_convertvector(v, bf16x2)); }
DI float lo2f(u32 x) { return __uint_as_float(x << 16); }
DI float hi2f(u32 x) { return __uint_as_float(x & 0xffff0000u); }
DI float b2f(u16 h) { return __uint_as_float((u32)h << 16); }
DI s16x4 trd(u32 a) { return __builtin_amdgcn_ds_read_tr16_b64_v4i16((LAS s16x4*)(size_t)a); }
DI s16x8 cat(s16x4 a, s16x4 b) { return __builtin_shufflevector(a, b, 0, 1, 2, 3, 4, 5, 6, 7); }
DI s16x8 frag(u32 a, u32 b, u32 c, u32 d) { u32x4 v = {a, b, c, d}; return __builtin_bit_cast(s16x8, v); }
template <class T> DI T ldsr(u32 a) { return *(LAS T*)(size_t)a; }
template <class T> DI void ldsw(u32 a, T v) { *(LAS T*)(size_t)a = v; }
template <int N> DI void waitv() { asm volatile("s_waitcnt vmcnt(%0)" ::"i"(N) : "memory"); }
DI void bar() { asm volatile("s_waitcnt lgkmcnt(0)\n\ts_barrier" ::: "memory"); }
DI f32x16 mfma32(s16x8 a, s16x8 b, f32x16 c) { return __builtin_amdgcn_mfma_f32_32x32x16_bf16(a, b, c, 0, 0, 0); }
DI f32x4 mfma16(s16x8 a, s16x8 b, f32x4 c) { return __builtin_amdgcn_mfma_f32_16x16x32_bf16(a, b, c, 0, 0, 0); }
DI float max3(float a, float b, float c) { float r; asm("v_max3_f32 %0, %1, %2, %3" : "=v"(r) : "v"(a), "v"(b), "v"(c)); return r; }
DI float min3(float a, float b, float c) { float r; asm("v_min3_f32 %0, %1, %2, %3" : "=v"(r) : "v"(a), "v"(b), "v"(c)); return r; }
DI float dot2(u32 x, u32 sel, float c) { return __builtin_amdgcn_fdot2_f32_bf16(__builtin_bit_cast(bf16x2, sel), __builtin_bit_cast(bf16x2, x), c, false); }
template <class R> DI void st128(u32x4 v, R rs, u32 voff, u32 soff, int) { __builtin_amdgcn_raw_buffer_store_b128(v, rs, voff + soff, 0, 16); }
DI void out16(u16* out, u32 off, u32x4 v) {
    __builtin_amdgcn_raw_buffer_store_b128(v, __builtin_amdgcn_make_buffer_rsrc((void*)out, 0, 2048 * 2560, 0x00020000), off * 2, 0, AUXA);
}
DI void out8(u16* out, u32 off, u32x2 v) {
    __builtin_amdgcn_raw_buffer_store_b64(v, __builtin_amdgcn_make_buffer_rsrc((void*)out, 0, 2048 * 2560, 0x00020000), off * 2, 0, AUXB);
}
DI void dma(u32 m0v, u32 voff, const void* sbase) {
    asm volatile("s_mov_b32 m0, %0\n\ts_nop 0\n\tglobal_load_lds_dwordx4 %1, %2" ::"s"(m0v), "v"(voff), "s"(sbase) : "memory");
}
DI void dma_m(u32 m0v, u32 voff, const void* sbase, u32 mlo, u32 mhi) {
    asm volatile("s_mov_b32 exec_lo, %3\n\ts_mov_b32 exec_hi, %4\n\ts_mov_b32 m0, %0\n\ts_nop 0\n\tglobal_load_lds_dwordx4 %1, %2\n\ts_mov_b64 exec, -1"
                 ::"s"(m0v), "v"(voff), "s"(sbase), "i"(mlo), "i"(mhi) : "memory");
}
#define STAMP(i)
#define STAMP_ARG
#define STAMP_PASS

DI void post_flag(char* fl, u64 tag, int tid, int n, int me) {
    if (tid < n) __hip_atomic_store((GAS u64*)(fl + tid * 256 + me * 8), tag, __ATOMIC_RELAXED, __HIP_MEMORY_SCOPE_AGENT);
}
DI void wait_flags(const char* fl, u64 tag, int l, int n) {
    const GAS u64* p = (const GAS u64*)(fl + (l < n ? l : 0) * 8);
    u64 v0 = __hip_atomic_load(p, __ATOMIC_RELAXED, __HIP_MEMORY_SCOPE_AGENT);
#pragma unroll 1
    for (u32 s = 0; s < kSpinCap; s++) {
        __builtin_amdgcn_s_sleep(PSLEEP);
        const u64 v1 = __hip_atomic_load(p, __ATOMIC_RELAXED, __HIP_MEMORY_SCOPE_AGENT);
        if (__all(v0 == tag)) break;
        v0 = v1;
    }
}

template <class F> DI void features(const float (&v)[20], const s16x8 (&wf)[2][3], u32 (&phi)[20], F filler) {
    u32 b[10];
#pragma unroll
    for (int i = 0; i < 10; i++) b[i] = pk(v[2 * i], v[2 * i + 1]);
    const s16x8 B0 = frag(b[0], b[1], b[2], b[3]), B1 = frag(b[4], b[5], b[6], b[7]), B2 = frag(b[8], b[9], 0, 0);
    f32x16 p0 = {}, p1 = {};
    p0 = mfma32(wf[0][0], B0, p0); p1 = mfma32(wf[1][0], B0, p1);
    p0 = mfma32(wf[0][1], B1, p0); p1 = mfma32(wf[1][1], B1, p1);
    p0 = mfma32(wf[0][2], B2, p0); p1 = mfma32(wf[1][2], B2, p1);
    SB; filler(); SB;
    asm volatile("s_nop 11" : "+v"(p0), "+v"(p1));
    float p[20];
#pragma unroll
    for (int i = 0; i < 20; i++) p[i] = i < 16 ? p0[i] : p1[i - 16];
    float mxa = max3(p[0], p[1], p[2]), mna = min3(p[0], p[1], p[2]), mxb = max3(p[3], p[4], p[5]), mnb = min3(p[3], p[4], p[5]);
#pragma unroll
    for (int i = 6; i < 18; i += 4) {
        mxa = max3(mxa, p[i], p[i + 1]); mna = min3(mna, p[i], p[i + 1]);
        mxb = max3(mxb, p[i + 2], p[i + 3]); mnb = min3(mnb, p[i + 2], p[i + 3]);
    }
    mxa = max3(mxa, p[18], p[19]); mna = min3(mna, p[18], p[19]);
    const float mx = max3(mxa, mxb, mxb), mn = min3(mna, mnb, mnb);
    const float L2E = 1.4426950408889634f;
    const float cx = -mx * L2E, cn = mn * L2E;
    float e[40], s1, s2;
#pragma unroll
    for (int i = 0; i < 20; i++) {
        e[i] = __builtin_amdgcn_exp2f(__builtin_fmaf(p[i], L2E, cx));
        e[20 + i] = __builtin_amdgcn_exp2f(__builtin_fmaf(p[i], -L2E, cn));
        if (i == 0) { s1 = e[0]; s2 = e[20]; } else { s1 += e[i]; s2 += e[20 + i]; }
    }
    const float r1 = __builtin_amdgcn_rcpf(s1), r2 = __builtin_amdgcn_rcpf(s2);
#pragma unroll
    for (int i = 0; i < 10; i++) { phi[i] = pk(e[2 * i] * r1, e[2 * i + 1] * r1); phi[10 + i] = pk(e[20 + 2 * i] * r2, e[21 + 2 * i] * r2); }
}

constexpr int WBLK = 960, WSLOT = 8 * WBLK, XSLOT = 24576;
constexpr int LW = XSLOT, LIMG = 102400, LMISC = LIMG + 2 * XSLOT, LZERO = LMISC + 3200, LTAB = LZERO + 64, TROW = 56, THEAD = 40 * TROW;
DI int xslot(int j) { return j % 3 == 0 ? 0 : LIMG + (j % 3 - 1) * XSLOT; }
static_assert(LTAB + 80 * TROW <= LDS_BYTES, "LDS map");

template <bool V> struct Flag { static constexpr bool value = V; };

struct Role { bool is_a; int r, c, ia; u32 nb; };
DI Role role_of(u32 bid) {
    const int xcd = bid & 7, j = bid >> 3;
    Role R;
    const int e = xcd & 1, m = xcd >> 1, nb = e ? 20 : 24, cq = e ? (j * 52) >> 8 : (j * 43) >> 8;
    R.r = __builtin_amdgcn_readfirstlane(e ? 6 + j - 5 * cq : j - 6 * cq); R.c = __builtin_amdgcn_readfirstlane(4 * m + cq);
    R.ia = __builtin_amdgcn_readfirstlane((RMAP == 1 ? (e ? 32 + 12 * m : 8 * m) : 20 * m + (e ? 8 : 0)) + j - nb);
    R.is_a = j >= nb; R.nb = nb;
    return R;
}

DI void load_wo(const u16* wo, int cb, int wi, int end, int nw, int l) {
    u32 off[5];
#pragma unroll
    for (int i = 0; i < 5; i++) { const u32 idx = 64 * i + l, row = (idx * 6554u) >> 16; off[i] = row * 1120 + idx * 16; }
#pragma unroll 1
    for (int gq = wi; gq < end; gq += nw) {
        const char* gp = (const char*)wo + cb * 160 + gq * (32 * 1280);
#pragma unroll
        for (int i = 0; i < 5; i++) dma(gq * 5120 + i * 1024, off[i], gp);
    }
}

DI void out_proj(u16* __restrict__ out, char* __restrict__ ws, u64 tag, int ia STAMP_ARG) {
    const int bid = sbid(), tid = threadIdx.x, l = tid & 63, w = __builtin_amdgcn_readfirstlane(tid >> 6);
    const int t = 4 * (bid & 7) + (bid >> 6), cb = (bid >> 3) & 7, rs = (t * 43) >> 7, sub = t - 3 * rs;
    bar();
    if (w >= 4) {
        if (ia >= 0) {
            u32x2 v[8];
#pragma unroll
            for (int c2 = 0; c2 < 8; c2++) v[c2] = ldsr<u32x2>(131072 + c2 * 4096 + (tid - 256) * 8);
            const u32 op = 4 * (tid - 256) * 1280 + 8 * ia;
            bar();
            __builtin_amdgcn_s_sleep(YSLEEP);
#pragma unroll
            for (int i = 0; i < 4; i++) {
                const u32 sel = (i & 1) ? 0x07060302u : 0x05040100u;
                out16(out, op + i * 1280, u32x4{__builtin_amdgcn_perm(v[1][i >> 1], v[0][i >> 1], sel), __builtin_amdgcn_perm(v[3][i >> 1], v[2][i >> 1], sel),
                                                 __builtin_amdgcn_perm(v[5][i >> 1], v[4][i >> 1], sel), __builtin_amdgcn_perm(v[7][i >> 1], v[6][i >> 1], sel)});
            }
        }
        return;
    }
    constexpr int NPRE = 6;
    const auto rsw = __builtin_amdgcn_make_buffer_rsrc((void*)ws, 0, WS_END, 0x00020000);
    const int g = l >> 4, q = (l >> 2) & 3, p = l & 3, hp = g & 1, P = g >> 1;
    u32 b1 = (20 * hp + 8 * P + q) * 160 + p * 8, b2 = (80 * P + 20 * hp + 16 + q) * 160 + p * 8;
    u32 b1h = b1 + 51200, b2h = b2 + 51200;
    asm volatile("" : "+v"(b1), "+v"(b2), "+v"(b1h), "+v"(b2h));
    s16x4 wfr[NPRE][10];
    auto rdw = [&](int ks, int n) {
        const int G = ks / 5, i = ks % 5, tt = n >> 1, rd = n & 1;
        const u32 so = (G & 1) * 25600;
        wfr[ks % NPRE][n] = i < 4 ? trd((G >> 1 ? b1h : b1) + so + (40 * i + 4 * rd) * 160 + 32 * tt) : trd((G >> 1 ? b2h : b2) + so + 40 * rd * 160 + 32 * tt);
    };
#pragma unroll
    for (int ks = 0; ks < NPRE; ks++)
#pragma unroll
        for (int n = 0; n < 10; n++) rdw(ks, n);
    if (w == 0) wait_flags(ws + WS_F2 + (rs * 24 + sub * 8 + cb) * 256, tag, l, 16);
    bar();
    STAMP(8);
    const int ti = 16 * (w & 1) + (l & 15);
    const u32 ob = WS_O + (rs * 96 + 2 * sub + (w >> 1)) * 2560;
    const u32 v1 = P * 1024 + (hp * 32 + ti) * 16, v2 = 2048 + 2 * P * 15360 + (hp * 32 + ti) * 8;
    u32x4 of[16]; u32x2 ot[8];
    auto ldo = [&](int n) {
        const int G = n / 6, i = n % 6;
        if (i < 4) of[4 * G + i] = __builtin_amdgcn_raw_buffer_load_b128(rsw, v1, ob + (4 * G + i) * 15360, 16);
        else ot[2 * G + i - 4] = __builtin_amdgcn_raw_buffer_load_b64(rsw, v2, ob + (4 * G + i - 4) * 15360, 16);
    };
#pragma unroll
    for (int n = 0; n < 10; n++) ldo(n);
    f32x4 oc[5] = {};
#pragma unroll
    for (int ks = 0; ks < 20; ks++) {
        const int G = ks / 5, i = ks % 5;
        const s16x8 B = i < 4 ? __builtin_bit_cast(s16x8, of[4 * G + i]) : frag(ot[2 * G][0], ot[2 * G][1], ot[2 * G + 1][0], ot[2 * G + 1][1]);
#pragma unroll
        for (int tt = 0; tt < 5; tt++) {
            SB;
            oc[tt] = mfma16(cat(wfr[ks % NPRE][2 * tt], wfr[ks % NPRE][2 * tt + 1]), B, oc[tt]);
            SB;
            if (ks + NPRE < 20) { rdw(ks + NPRE, 2 * tt); rdw(ks + NPRE, 2 * tt + 1); }
            if ((tt == 0 || tt == 2) && 10 + 2 * ks + (tt >> 1) < 24) ldo(10 + 2 * ks + (tt >> 1));
        }
    }
    SB;
    const u32 op = (64 * t + 16 * w + (l & 15)) * 1280 + 640 + 80 * cb + 4 * g;
#pragma unroll
    for (int tt = 0; tt < 5; tt++) out8(out, op + 16 * tt, u32x2{pk(oc[tt][0], oc[tt][1]), pk(oc[tt][2], oc[tt][3])});
    STAMP(9);
    if (bid == 0 && w == 0) {
        wait_flags(ws + WS_ACK, tag, l, 64);
        wait_flags(ws + WS_ACK + 512, tag, l, 16);
        if (l == 0) __hip_atomic_store((GAS u64*)(ws + WS_EPOCH), tag, __ATOMIC_RELAXED, __HIP_MEMORY_SCOPE_AGENT);
    }
}

template <bool FIRST>
DI void loader_b(const u16* __restrict__ x, const u16* __restrict__ W, const u16* __restrict__ wo, int r, int c, int lw, u32& pfd) {
    const int l = threadIdx.x & 63;
    const int ph = (((r + 4 * c) & 15) * 10) >> 4;
    const int pl = l >> 4, idx = l & 15, q3 = (idx * 11) >> 5;
    const int q = pl < 3 ? idx >> 2 : q3 & 3, sg = pl < 3 ? pl : idx - 3 * q3, ch = pl < 3 ? (idx & 3) + ((idx >> 1) & 1) : 2;
    const u32 wl = q * 3840 + sg * 1280 + ch * 16 + (sg == 0 ? ((c & 1) ? 0 : 160) : 80);
    const u32 xo0 = (l >> 3) * 1280 + (((l & 7) ^ (l >> 4)) << 4), xo1 = ((l >> 3) + 8) * 1280 + (((l & 7) ^ ((l >> 4) | 4)) << 4);
    u32 xr[2][3];
#pragma unroll
    for (int i = 0; i < 6; i++) { const int row = 192 * r + 96 * (i / 3) + 48 * lw + 16 * (i % 3); xr[i / 3][i % 3] = (row < 2048 ? row : row - 64) * 1280; }
    const char* wb = (const char*)W + c * 80 - 80 + lw * (16 * 3840);
    auto ld_x = [&](int hx) {
        const int jx = hx >> 1, hh = hx & 1;
        int jr = jx + ph; if (jr >= 10) jr -= 10;
        const char* g = (const char*)x + jr * 128;
        const u32 d = xslot(jx) + (96 * hh + 48 * lw) * 128;
#pragma unroll
        for (int i = 0; i < 3; i++) { dma(d + i * 2048, xo0, g + xr[hh][i]); dma(d + i * 2048 + 1024, xo1, g + xr[hh][i]); }
    };
    auto ld_w = [&](int i) {
        int ir = i + 2 * ph; if (ir >= 20) ir -= 20;
        const char* g = wb + ir * (32 * 3840);
        const u32 d = LW + (i & 3) * WSLOT + lw * 4 * WBLK;
#pragma unroll
        for (int b = 0; b < 4; b++) dma_m(d + b * WBLK, wl, g + b * (4 * 3840), 0xffffffffu, 0x0fffffffu);
    };
    if (FIRST) { ld_w(0); ld_x(0); ld_x(1); return; }
    ld_w(1); ld_x(2); ld_x(3);
    waitv<16>();
    bar();
    ld_w(2); ld_w(3); ld_x(4);
#pragma unroll
    for (int i = 0; i < 19; i++) {
        if (i == 0 || i == 2) waitv<26>(); else if (i <= 15) waitv<20>(); else if (i == 16) waitv<14>(); else if (i == 17) waitv<4>(); else waitv<2>();
        bar();
        if (i + 5 < 20) ld_x(i + 5);
        if (i + 4 < 20) ld_w(i + 4);
        if (i == 17) {
            const u32 ln = __builtin_amdgcn_readfirstlane(min((sbid() >> 3) * 256 + lw * 64, 6208u)) + l;
            asm volatile("global_load_dword %0, %1, %2\n\tglobal_load_dword %0, %1, %3" : "+v"(pfd) : "v"(ln * 128), "s"(wo), "s"((const char*)wo + 128 * 128));
        }
    }
}

DI u64 path_b(const u16* __restrict__ x, const u16* __restrict__ W, const u16* __restrict__ wphi, const u16* __restrict__ wo,
               u16* __restrict__ out, char* __restrict__ ws, int r, int c, u32& pfd STAMP_ARG) {
    const int tid = threadIdx.x, l = tid & 63;
    const int w = __builtin_amdgcn_readfirstlane(tid >> 6);
    const auto rsw = __builtin_amdgcn_make_buffer_rsrc((void*)ws, 0, WS_END, 0x00020000);
    const int g = l >> 4, q = (l >> 2) & 3, p = l & 3, h = l >> 5;
    const int rq = 2 * (g & 1) + (p >> 1), hk = g >> 1;
    const bool same = hk == (p & 1);
    u64 tag;
    u32 phiq[20];
    f32x16 acc[4] = {};
    s16x8 wfq[2][3];
    if (w >= 6) {
        loader_b<false>(x, W, wo, r, c, w - 6, pfd);
        asm volatile("s_load_dwordx2 %0, %1, 0\n\ts_waitcnt lgkmcnt(0)" : "=&s"(tag) : "s"(ws + WS_EPOCH) : "memory");
    } else {
        if (w < 4) dma_m(LMISC + w * 800, l * 16, (const char*)wphi + ((c ^ (w >> 1)) * 2 + (w & 1)) * 800, 0xffffffffu, 0x3ffffu);
        if (tid < 2) ldsw<u64>(LZERO + tid * 8, 0ull);
        u32 bA = LW + (g >> 1) * 2 * WBLK + q * 64 + (p & 1) * 32 + (g & 1) * 16 + (p >> 1) * 8;
        u32 bD = LW + (g >> 1) * 2 * WBLK + 768 + (3 * q + 2 * (g & 1) + (p >> 1)) * 16 + (p & 1) * 8;
        asm volatile("" : "+v"(bA), "+v"(bD));
        const int trow = 32 * w + (l & 31);
        s16x8 wfk[2][3];
        auto load_wf = [&](s16x8 (&wf)[2][3], u32 base) {
#pragma unroll
            for (int to = 0; to < 2; to++)
#pragma unroll
                for (int s = 0; s < 3; s++) {
                    s16x4 t[2];
#pragma unroll
                    for (int rd = 0; rd < 2; rd++) {
                        const int mu = 2 * s + rd;
                        if (mu < 5) {
                            int dq = mu + hk; if (mu == 4) dq = hk ? 0 : 4;
                            const bool ok = same && (to == 0 || rq == 0);
                            t[rd] = trd(ok ? base + hk * 800 + (4 * dq + q) * 40 + (4 * to + rq) * 8 : LZERO);
                        } else t[rd] = s16x4{0, 0, 0, 0};
                    }
                    wf[to][s] = cat(t[0], t[1]);
                }
        };
        asm volatile("s_load_dwordx2 %0, %1, 0\n\ts_waitcnt vmcnt(0) lgkmcnt(0)" : "=&s"(tag) : "s"(ws + WS_EPOCH), "v"(pfd) : "memory");
        bar();
        STAMP(1);
        const u32 sw = ((trow & 2) << 1) | ((trow & 4) >> 1) | (trow & 1);
        const u32 rowa = LIMG + h * XSLOT + trow * 128;
        auto gemm = [&](auto HV) {
            constexpr bool hv = decltype(HV)::value;
            constexpr int NB = hv ? 3 : 1, NM = hv ? 6 : 3, NR = hv ? 11 : 7;
            f32x16 vc[3] = {};
            u32 aX[4], aXh[4];
            const int tx = hv ? 32 * (w - 2) + (l & 31) : trow;
#pragma unroll
            for (int k = 0; k < 4; k++) {
                aX[k] = tx * 128 + (((2 * k + h) ^ ((tx >> 1) & 7)) << 4); aXh[k] = aX[k] + LIMG;
                asm volatile("" : "+v"(aX[k]), "+v"(aXh[k]));
            }
            s16x4 fa[2][8]; s16x8 fb[2][NB];
            auto rd = [&](int s, int i, int ks, int n) {
                const int nb = hv ? (n == 0 ? 1 : n == 9 ? 0 : n == 10 ? 2 : -1) : (n == 0 ? 0 : -1);
                if (nb >= 0) {
                    const int sl = (i >> 1) % 3, k = (i & 1) * 2 + ks;
                    fb[s][nb] = sl == 0 ? ldsr<s16x8>(aX[k] + nb * 8192) : ldsr<s16x8>(aXh[k] + (sl - 1) * XSLOT + nb * 8192);
                } else {
                    const int na = n <= 4 ? n - 1 : n <= 6 ? n + 1 : n - 3;
                    const u32 o = (i & 3) * WSLOT + ks * 4 * WBLK + (na & 1) * WBLK;
                    fa[s][na] = na < 6 ? trd(bA + o + (na >> 1) * 256) : trd(bD + o);
                }
            };
            auto mm = [&](int s, int t) {
                if (t < 3) { const int tt = t == 2 ? 3 : t; acc[tt] = mfma32(cat(fa[s][2 * tt], fa[s][2 * tt + 1]), fb[s][hv ? 1 : 0], acc[tt]); }
                else { const int nb = t == 3 ? 1 : t == 4 ? 0 : 2; vc[nb] = mfma32(cat(fa[s][4], fa[s][5]), fb[s][hv ? nb : 0], vc[nb]); }
            };
            auto halfstep = [&](int s, int i, int ks, bool next) {
#pragma unroll
                for (int t = 0; t < NM; t++) {
                    SB; mm(s, t); SB;
                    if (t == 0 && ks == 0 && next) bar();
                    if (next)
#pragma unroll
                        for (int n = 3 * t; n < 3 * t + 3 && n < NR; n++) rd(s ^ 1, i, ks, n);
                }
            };
#pragma unroll
            for (int n = 0; n < NR; n++) rd(0, 0, 0, n);
#pragma unroll
            for (int i = 0; i < 20; i++) {
                halfstep(0, i, 1, true);
                if (i == 1) load_wf(wfk, LMISC);
                if (i == 2) load_wf(wfq, LMISC + 1600);
                halfstep(1, i + 1, 0, i < 19);
            }
            if (hv) {
#pragma unroll
                for (int nb = 0; nb < 3; nb++)
#pragma unroll
                    for (int mu = 0; mu < 4; mu++) {
                        const int pc = 10 + mu + h;
                        ldsw<u32x2>(rowa + (nb - 1) * 8192 + (((pc >> 1) ^ sw) << 4) + (pc & 1) * 8,
                                    u32x2{pk(vc[nb][4 * mu], vc[nb][4 * mu + 1]), pk(vc[nb][4 * mu + 2], vc[nb][4 * mu + 3])});
                    }
            }
        };
        if ((w & 3) >= 2) gemm(Flag<true>{}); else gemm(Flag<false>{});
        STAMP(2);
        float kr[20]; u32 phik[20];
#pragma unroll
        for (int i = 0; i < 20; i++) kr[i] = i < 16 ? acc[1][i] : acc[3][i - 12];
        features(kr, wfk, phik, [&]() {
            const int pc = h ? 10 : 14;
            ldsw<u32x2>(rowa + (((pc >> 1) ^ sw) << 4) + (pc & 1) * 8, u32x2{pk(acc[3][8], acc[3][9]), pk(acc[3][10], acc[3][11])});
            ldsw<u32x2>(rowa + ((7 ^ sw) << 4) + 8, u32x2{0x3f80u, 0u});
        });
        const u32 keep = 192 * r + 32 * w < 2048 ? 0xffffffffu : 0u;
#pragma unroll
        for (int ch = 0; ch < 5; ch++) ldsw<u32x4>(rowa + ((ch ^ sw) << 4), u32x4{phik[4 * ch], phik[4 * ch + 1], phik[4 * ch + 2], phik[4 * ch + 3]} & keep);
        STAMP(10);
    }
    tag += 1;
    bar();
    if (w >= 6) load_wo(wo, (sbid() >> 3) & 7, w - 6, 10, 2, l);
    if (w < 4) {
        const int pcn = 4 * (g & 1) + p;
        const int pa = 10 + (pcn < 6 ? pcn : 5), pb = 5 * (w & 1) + (pcn < 5 ? pcn : 4);
        u32 aa[2], ab[2];
#pragma unroll
        for (int rd = 0; rd < 2; rd++) {
            const int row = 8 * (g >> 1) + 4 * rd + q;
            const u32 sw = ((row & 2) << 1) | ((row & 4) >> 1) | (row & 1);
            const u32 base = LIMG + (w >> 1) * XSLOT + row * 128;
            aa[rd] = base + (((pa >> 1) ^ sw) << 4) + (pa & 1) * 8;
            ab[rd] = base + (((pb >> 1) ^ sw) << 4) + (pb & 1) * 8;
            asm volatile("" : "+v"(aa[rd]), "+v"(ab[rd]));
        }
        s16x4 ta[24], tb[24];
#pragma unroll
        for (int ks = 0; ks < 12; ks++) {
            ta[2 * ks] = trd(aa[0] + ks * 2048); ta[2 * ks + 1] = trd(aa[1] + ks * 2048);
            tb[2 * ks] = trd(ab[0] + ks * 2048); tb[2 * ks + 1] = trd(ab[1] + ks * 2048);
        }
        SB;
        f32x16 kv = {};
#pragma unroll
        for (int ks = 0; ks < 12; ks++) kv = mfma32(cat(ta[2 * ks], ta[2 * ks + 1]), cat(tb[2 * ks], tb[2 * ks + 1]), kv);
        if ((l & 31) < 20) {
            const u32 lam = h * 20 + (l & 31), o = WS_PART + (c * 16 + r) * 4096 + w * 960;
            st128(u32x4{pk(kv[0], kv[1]), pk(kv[2], kv[3]), pk(kv[4], kv[5]), pk(kv[6], kv[7])}, rsw, lam * 16, o, 16);
            __builtin_amdgcn_raw_buffer_store_b64(u32x2{pk(kv[8], kv[9]), pk(kv[10], kv[11])}, rsw, 640 + lam * 8, o, 16);
        }
    }
    STAMP(11);
    if (w < 6) {
        float qr[20];
#pragma unroll
        for (int i = 0; i < 20; i++) qr[i] = i < 16 ? acc[0][i] : acc[3][i - 16];
        features(qr, wfq, phiq, []() {});
#pragma unroll
        for (int i = 0; i < 20; i++) asm volatile("" : "+v"(phiq[i]));
        STAMP(17);
        waitv<0>();
    }
    bar();
    post_flag(ws + WS_F1 + c * 4096, tag, tid, 11, r);
    STAMP(3);
    if (w >= 6) load_wo(wo, (sbid() >> 3) & 7, w + 4, 20, 2, l);
    if (tid < 80) ldsw<u64>(LTAB + tid * TROW + 48, 0ull);
    if (w == 0) wait_flags(ws + WS_F1 + (c ^ 1) * 4096 + r * 256, tag, l, 11);
    bar();
    STAMP(5);
    if (w < 4) {
        const int t = tid < 240 ? tid : 239;
        const int un = (t * 1093) >> 16, it = t - un * 60;
        u32x4 pv[11];
#pragma unroll
        for (int i = 0; i < 11; i++) pv[i] = __builtin_amdgcn_raw_buffer_load_b128(rsw, t * 16, WS_PART + (c ^ 1) * 65536 + i * 4096, 16);
        float s[8] = {};
        u32 slo = 0x00003f80u, shi = 0x3f800000u;
        asm volatile("" : "+s"(slo), "+s"(shi));
#pragma unroll
        for (int i = 0; i < 11; i++)
#pragma unroll
            for (int e = 0; e < 4; e++) { s[2 * e] = dot2(pv[i][e], slo, s[2 * e]); s[2 * e + 1] = dot2(pv[i][e], shi, s[2 * e + 1]); }
        const int lam = it < 40 ? it : 2 * (it - 40);
        const int hh = lam >= 20, f = lam - 20 * hh;
        const u32 a0 = LTAB + (un >> 1) * THEAD + ((un & 1) * 20 + f) * TROW + hh * 8 + (it < 40 ? 0 : 32);
        ldsw<u32x2>(a0, u32x2{pk(s[0], s[1]), pk(s[2], s[3])});
        ldsw<u32x2>(a0 + (it < 40 ? 16 : TROW), u32x2{pk(s[4], s[5]), pk(s[6], s[7])});
    }
    bar();
    STAMP(6);
    if (w < 6 && 192 * r + 32 * w < 2048) {
        const u32 tb = LTAB + hk * THEAD + q * TROW;
        u32 t0 = tb + (same ? rq : 6) * 8, t1 = tb + (same && rq < 2 ? 4 + rq : 6) * 8;
        asm volatile("" : "+v"(t0), "+v"(t1));
        f32x16 o0 = {}, o1 = {};
#pragma unroll
        for (int s = 0; s < 5; s++) {
            const s16x8 B = frag(phiq[4 * s], phiq[4 * s + 1], phiq[4 * s + 2], phiq[4 * s + 3]);
            o0 = mfma32(cat(trd(t0 + 8 * s * TROW), trd(t0 + (8 * s + 4) * TROW)), B, o0);
            o1 = mfma32(cat(trd(t1 + 8 * s * TROW), trd(t1 + (8 * s + 4) * TROW)), B, o1);
        }
        const float rden = __builtin_amdgcn_rcpf(o1[4] + 1e-6f);
        u32 ob[10];
#pragma unroll
        for (int i = 0; i < 10; i++) ob[i] = i < 8 ? pk(o0[2 * i] * rden, o0[2 * i + 1] * rden) : pk(o1[2 * i - 16] * rden, o1[2 * i - 15] * rden);
        const u32 o = WS_O + ((r * 16 + (c ^ 1)) * 6 + w) * 2560;
        st128(u32x4{ob[0], ob[1], ob[2], ob[3]}, rsw, l * 16, o, 16);
        st128(u32x4{ob[4], ob[5], ob[6], ob[7]}, rsw, l * 16 + 1024, o, 16);
        __builtin_amdgcn_raw_buffer_store_b64(u32x2{ob[8], ob[9]}, rsw, l * 8 + 2048, o, 16);
    }
    STAMP(12);
    waitv<0>();
    asm volatile("" ::"v"(pfd));
    bar();
    post_flag(ws + WS_F2 + r * (24 * 256), tag, tid, 24, c);
    STAMP(7);
    return tag;
}

DI f32x2 cmul(f32x2 a, f32x2 b) {
    f32x2 d;
    asm("v_pk_mul_f32 %0, %1, %2 op_sel_hi:[0,1]\n\tv_pk_fma_f32 %0, %1, %2, %0 op_sel:[1,1,0] op_sel_hi:[1,0,1] neg_lo:[0,1,0]" : "=&v"(d) : "v"(a), "v"(b));
    return d;
}
DI f32x2 cmulc(f32x2 a, f32x2 b) {
    f32x2 d;
    asm("v_pk_mul_f32 %0, %1, %2 op_sel_hi:[0,1] neg_hi:[0,1]\n\tv_pk_fma_f32 %0, %1, %2, %0 op_sel:[1,1,0] op_sel_hi:[1,0,1]" : "=&v"(d) : "v"(a), "v"(b));
    return d;
}
DI f32x2 csq(f32x2 a) { return cmul(a, a); }
DI f32x2 cexp(float a, float b) {
    const float m = __builtin_amdgcn_exp2f(a * 1.4426950408889634f);
    const float c1 = 0.15915494309189535f, c2 = 6.4206382e-9f;
    const float n = __builtin_rintf(b * c1);
    const float f = __builtin_fmaf(b, c2, __builtin_fmaf(b, c1, -n));
    return f32x2{m * __builtin_amdgcn_cosf(f), m * __builtin_amdgcn_sinf(f)};
}
constexpr int LU = 131072;
DI u32 kaddr(u32 e) { return ((e >> 5) << 8) + ((e & 31) << 1); }

DI void prologue_a(int ia, const u16* __restrict__ Are, const u16* __restrict__ Aim, const u16* __restrict__ Bre, const u16* __restrict__ Bim,
                   const u16* __restrict__ Cre, const u16* __restrict__ Cim, const u16* __restrict__ logdt, const u16* __restrict__ Dskip) {
    const int l = threadIdx.x & 63, w = __builtin_amdgcn_readfirstlane(threadIdx.x >> 6);
    const u16* src = w == 0 ? Are : w == 1 ? Aim : w == 2 ? Bre : w == 3 ? Bim : w == 4 ? Cre : w == 5 ? Cim : w == 6 ? logdt : Dskip;
    if (w < 6) { dma(w * 16384 + 8192, ia * 2048 + l * 16, src); dma(w * 16384 + 9216, ia * 2048 + 1024 + l * 16, src); }
    else dma_m(w * 16384 + 8192, ia * 16, src, 1u, 0u);
}

DI u64 path_a(const u16* __restrict__ u, const u16* __restrict__ wo, u16* __restrict__ out, char* __restrict__ ws, int ia, u32 pfd STAMP_ARG) {
    const int tid = threadIdx.x, l = tid & 63;
    const int w = __builtin_amdgcn_readfirstlane(tid >> 6);
    const u32 KB = w * 16384;
    const int g = l >> 4, q = (l >> 2) & 3, p = l & 3, h = l >> 5;
    STAMP(1);
    u64 tag;
    asm volatile("s_load_dwordx2 %0, %1, 0\n\ts_waitcnt vmcnt(0) lgkmcnt(0)" : "=&s"(tag) : "s"(ws + WS_EPOCH), "v"(pfd) : "memory");
    tag += 1;
    bar();
    if (tid == 0) __hip_atomic_store((GAS u64*)(ws + WS_ACK + ia * 8), tag, __ATOMIC_RELAXED, __HIP_MEMORY_SCOPE_AGENT);
    const float ldt = b2f(ldsr<u16>(6 * 16384 + 8192 + w * 2)), dsk = b2f(ldsr<u16>(7 * 16384 + 8192 + w * 2));
    u32 prm[6];
#pragma unroll
    for (int i = 0; i < 6; i++) prm[i] = ldsr<u32>(i * 16384 + 8192 + w * 256 + l * 4);
    bar();
    u32x4 ur[4];
    auto u_in = [&]() {
#pragma unroll
        for (int i = 0; i < 4; i++) ur[i] = *(const u32x4*)((const char*)u + (size_t)(256 * ((w + ia) & 7) + 4 * l + i) * 1280 + ia * 16);
    };
    if (UISS == 0) u_in();
    const float dt = lo2f(pk(expf(ldt), 0.f));
    STAMP(2);
    f32x16 kd = {};
    u32 ra[2];
#pragma unroll
    for (int rd = 0; rd < 2; rd++) ra[rd] = KB + (8 * (g >> 1) + 4 * rd + q) * 64 + (((2 * (g & 1) + (p >> 1)) ^ ((2 * rd + (q >> 1)) & 3)) << 4) + (p & 1) * 8;
    const u32 wa = KB + l * 64, wsw = (l >> 1) & 3;
#pragma unroll
    for (int ps = 0; ps < 2; ps++) {
        float pf[6];
#pragma unroll
        for (int i = 0; i < 6; i++) pf[i] = ps ? hi2f(prm[i]) : lo2f(prm[i]);
        const float a = dt * pf[0], b = dt * pf[1];
        const f32x2 Wc = cmulc(f32x2{pf[4], -pf[5]}, f32x2{dt * pf[2], dt * pf[3]});
        const f32x2 e1 = cexp(a, b), e16 = cexp(16.f * a, 16.f * b), e256 = cexp(256.f * a, 256.f * b);
        const f32x2 e2 = csq(e1), e4 = csq(e2), e8 = csq(e4), e32 = csq(e16), e64 = csq(e32), e128 = csq(e64), e512 = csq(e256), e1024 = csq(e512);
        f32x2 X[32];
        X[0] = Wc; X[1] = cmulc(Wc, e1); X[2] = cmulc(Wc, e2); X[3] = cmulc(X[1], e2);
#pragma unroll
        for (int i = 0; i < 4; i++) X[4 + i] = cmulc(X[i], e4);
        X[8] = cmulc(Wc, e256); X[9] = cmulc(X[8], e2); X[10] = cmulc(X[8], e4);
        X[12] = X[10]; X[13] = cmulc(X[9], e4); X[14] = cmulc(X[8], e8);
        X[11] = cmulc(Wc, e512); X[16] = cmulc(X[11], e4); X[17] = cmulc(X[11], e8);
        X[15] = cmulc(X[8], e512); X[20] = cmulc(X[15], e4); X[21] = cmulc(X[15], e8);
        X[18] = cmulc(Wc, e1024); X[19] = cmulc(X[18], e8);
        X[24] = cmulc(X[8], e1024); X[25] = cmulc(X[24], e8);
        X[22] = cmulc(X[11], e1024); X[23] = cmulc(X[22], e8);
        X[28] = cmulc(X[15], e1024); X[29] = cmulc(X[28], e8);
        X[26] = X[27] = X[30] = X[31] = f32x2{0.f, 0.f};
        f32x2 Y[8];
        Y[0] = f32x2{1.f, 0.f}; Y[1] = e8; Y[2] = e16; Y[3] = cmul(e8, e16); Y[4] = e32; Y[5] = cmul(e8, e32); Y[6] = cmul(e16, e32); Y[7] = cmul(Y[3], e32);
        const f32x2 e192 = cmul(e64, e128);
#pragma unroll
        for (int ch = 0; ch < 4; ch++) {
            const f32x2* Xc = X + 8 * ch;
            const u32 o = wa + ((ch ^ wsw) << 4);
            ldsw<u32x4>(o, u32x4{pk(Xc[0].x, Xc[1].x), pk(Xc[2].x, Xc[3].x), pk(Xc[4].x, Xc[5].x), pk(Xc[6].x, Xc[7].x)});
            ldsw<u32x4>(o + 4096, u32x4{pk(Xc[0].y, Xc[1].y), pk(Xc[2].y, Xc[3].y), pk(Xc[4].y, Xc[5].y), pk(Xc[6].y, Xc[7].y)});
            f32x2 Z[8];
            const f32x2 m = ch == 1 ? e64 : ch == 2 ? e128 : e192;
#pragma unroll
            for (int i = 0; i < 8; i++) Z[i] = ch == 0 ? Y[i] : i == 0 ? m : cmul(Y[i], m);
            ldsw<u32x4>(o + 8192, u32x4{pk(Z[0].x, Z[1].x), pk(Z[2].x, Z[3].x), pk(Z[4].x, Z[5].x), pk(Z[6].x, Z[7].x)});
            ldsw<u32x4>(o + 12288, u32x4{pk(Z[0].y, Z[1].y), pk(Z[2].y, Z[3].y), pk(Z[4].y, Z[5].y), pk(Z[6].y, Z[7].y)});
        }
        s16x4 fx[16], fy[16];
#pragma unroll
        for (int ks = 0; ks < 8; ks++) {
            fx[2 * ks] = trd(ra[0] + ks * 1024); fx[2 * ks + 1] = trd(ra[1] + ks * 1024);
            fy[2 * ks] = trd(ra[0] + ks * 1024 + 8192); fy[2 * ks + 1] = trd(ra[1] + ks * 1024 + 8192);
        }
        if (ps == 0) { STAMP(4); if (UISS == 1) u_in(); }
#pragma unroll
        for (int ks = 0; ks < 8; ks++) kd = mfma32(cat(fx[2 * ks], fx[2 * ks + 1]), cat(fy[2 * ks], fy[2 * ks + 1]), kd);
    }
    STAMP(6);
    {
        const int j = l & 31;
        ldsw<u32x2>(KB + kaddr(2044 - 8 * j - 4 * h), u32x2{pk(kd[3], kd[2]), pk(kd[1], l == 0 ? kd[0] + dsk : kd[0])});
        ldsw<u32x2>(KB + kaddr(2044 - 256 - 8 * j - 4 * h), u32x2{pk(kd[6], kd[5]), pk(kd[4], kd[4])});
        ldsw<u32x4>(KB + kaddr(2040 - 512 - 256 * h - 8 * j), u32x4{pk(kd[9], kd[9]), pk(kd[8], kd[8]), pk(kd[8], kd[7]), pk(kd[7], kd[7])});
#pragma unroll
        for (int sp = 0; sp < 2; sp++) {
            const float a = kd[10 + 2 * sp], b = kd[11 + 2 * sp];
            const u32 aa = pk(a, a), bb = pk(b, b);
            ldsw<u32x4>(KB + kaddr(2040 - 1024 - 512 * h - 256 * sp - 8 * j), u32x4{bb, (j & 1) ? bb : pk(b, a), aa, aa});
        }
        u32x4 v[4]; u32x2 n[4];
        const u32 a0 = KB + kaddr(8 * l), a1 = KB + kaddr(8 * l + 8);
#pragma unroll
        for (int it = 0; it < 4; it++) { v[it] = ldsr<u32x4>(a0 + it * 4096); n[it] = ldsr<u32x2>(a1 + it * 4096); }
#pragma unroll
        for (int it = 0; it < 4; it++) {
            const u32 s0 = __builtin_amdgcn_alignbit(v[it][1], v[it][0], 16), s1 = __builtin_amdgcn_alignbit(v[it][2], v[it][1], 16),
                      s2 = __builtin_amdgcn_alignbit(v[it][3], v[it][2], 16), s3 = __builtin_amdgcn_alignbit(n[it][0], v[it][3], 16),
                      s4 = __builtin_amdgcn_alignbit(n[it][1], n[it][0], 16);
            ldsw<u32x4>(a0 + it * 4096 + 64, u32x4{s0, s1, s2, s3});
            ldsw<u32x4>(a0 + it * 4096 + 128, u32x4{v[it][1], v[it][2], v[it][3], n[it][0]});
            ldsw<u32x4>(a0 + it * 4096 + 192, u32x4{s1, s2, s3, s4});
        }
    }
    STAMP(5);
    {
        const u32 a = LU + (256 * ((w + ia) & 7) + 4 * l) * 2;
#pragma unroll
        for (int c2 = 0; c2 < 4; c2++) {
            ldsw<u32x2>(a + 2 * c2 * 4096, u32x2{__builtin_amdgcn_perm(ur[1][c2], ur[0][c2], 0x05040100u), __builtin_amdgcn_perm(ur[3][c2], ur[2][c2], 0x05040100u)});
            ldsw<u32x2>(a + (2 * c2 + 1) * 4096, u32x2{__builtin_amdgcn_perm(ur[1][c2], ur[0][c2], 0x07060302u), __builtin_amdgcn_perm(ur[3][c2], ur[2][c2], 0x07060302u)});
        }
    }
    STAMP(3);
    bar();
    STAMP(10);
    const int rr = l & 15;
    const u32 e0 = 2047 - 16 - rr + 8 * g, sh = e0 & 3;
    auto half = [&](auto HI) {
        constexpr bool hi = decltype(HI)::value;
        constexpr int NF = hi ? 8 : 4, T0 = hi ? 4 : 0, NRD = 3 * NF;
        f32x4 y[4] = {};
        u32 ka0 = KB + kaddr(e0 - sh) + sh * 64 - (NF - 1) * 2048, ka1 = KB + kaddr(e0 - sh + 4) + sh * 64 - (NF - 1) * 2048;
        u32 ua = LU + w * 4096 + 32 * rr + 16 * g - 32;
        u32 mk[4];
#pragma unroll
        for (int i = 0; i < 4; i++) mk[i] = (2 * i > 16 + rr - 8 * g ? 0u : 0xffffu) | (2 * i + 1 > 16 + rr - 8 * g ? 0u : 0xffff0000u);
        int lim = 2 * rr + g - 2;
        u32x2 A[2][2 * NF]; u32x4 B[2][NF];
        auto rdc = [&](int s, int n) {
            const int m = n / 3, k = n % 3;
            if (k == 0) B[s][m] = ldsr<u32x4>(ua + m * 512);
            else if (k == 1) A[s][2 * (NF - 1 - m)] = ldsr<u32x2>(ka0 + m * 2048);
            else A[s][2 * (NF - 1 - m) + 1] = ldsr<u32x2>(ka1 + m * 2048);
        };
        auto group = [&](int s, bool next) {
            A[s][0] &= u32x2{mk[0], mk[1]}; A[s][1] &= u32x2{mk[2], mk[3]};
            if (lim < 0) B[s][0] = u32x4{0, 0, 0, 0};
            ka0 -= 256; ka1 -= 256; ua -= 64; lim -= 4;
            mk[0] = mk[1] = mk[2] = mk[3] = 0xffffffffu;
            asm volatile("" : "+v"(ka0), "+v"(ka1), "+v"(ua));
            int n = 0;
#pragma unroll
            for (int m = NF - 1; m >= 0; m--)
#pragma unroll
                for (int ct = NF - 1 - m; ct >= 0 && ct + m >= T0; ct--) {
                    SB;
                    y[ct + m - T0] = mfma16(frag(A[s][2 * m][0], A[s][2 * m][1], A[s][2 * m + 1][0], A[s][2 * m + 1][1]), __builtin_bit_cast(s16x8, B[s][ct]), y[ct + m - T0]);
                    SB;
                    if (next && n < NRD) rdc(s ^ 1, n);
                    n++;
                    if (!hi && next && (n == 1 || n == 6)) rdc(s ^ 1, n++);
                }
        };
        asm volatile("" : "+v"(ka0), "+v"(ka1), "+v"(ua));
#pragma unroll
        for (int n = 0; n < NRD; n++) rdc(0, n);
#pragma unroll 1
        for (int gg = 0; gg < 3; gg++) { group(0, true); group(1, true); }
        group(0, true); group(1, false);
#pragma unroll
        for (int ct = 0; ct < 4; ct++) ldsw<u32x2>(LU + w * 4096 + (T0 + ct) * 512 + rr * 32 + g * 8, u32x2{pk(y[ct][0], y[ct][1]), pk(y[ct][2], y[ct][3])});
        if (hi) STAMP(14); else STAMP(16);
        bar();
        if (hi) STAMP(15); else STAMP(17);
        if (!hi) return;
        u32 v[8];
#pragma unroll
        for (int c2 = 0; c2 < 8; c2++) v[c2] = ldsr<u32>(LU + c2 * 4096 + T0 * 512 + tid * 4);
        const u32 op = (T0 * 256 + 2 * tid) * 1280 + 8 * ia;
        out16(out, op, u32x4{__builtin_amdgcn_perm(v[1], v[0], 0x05040100u), __builtin_amdgcn_perm(v[3], v[2], 0x05040100u),
                            __builtin_amdgcn_perm(v[5], v[4], 0x05040100u), __builtin_amdgcn_perm(v[7], v[6], 0x05040100u)});
        out16(out, op + 1280, u32x4{__builtin_amdgcn_perm(v[1], v[0], 0x07060302u), __builtin_amdgcn_perm(v[3], v[2], 0x07060302u),
                                     __builtin_amdgcn_perm(v[5], v[4], 0x07060302u), __builtin_amdgcn_perm(v[7], v[6], 0x07060302u)});
    };
    half(Flag<true>{});
    STAMP(11);
    half(Flag<false>{});
    STAMP(12);
    load_wo(wo, (sbid() >> 3) & 7, w, 20, 8, l);
    waitv<0>();
    STAMP(7);
    return tag;
}

__global__ void __launch_bounds__(512, 2)
fused_kernel(const u16* __restrict__ x, const u16* __restrict__ W, const u16* __restrict__ wphi, const u16* __restrict__ wo,
             u16* __restrict__ out, char* __restrict__ ws, const u16* __restrict__ u, const u16* __restrict__ Are,
             const u16* __restrict__ Aim, const u16* __restrict__ Bre, const u16* __restrict__ Bim, const u16* __restrict__ Cre,
             const u16* __restrict__ Cim, const u16* __restrict__ logdt, const u16* __restrict__ Dskip) {
    __shared__ __attribute__((aligned(16))) char smem[LDS_BYTES];
    asm volatile("" ::"s"((u32)(size_t)(LAS char*)smem));
    u32 pfd = 0;
    if (threadIdx.x < 12) {
        u64 pc; asm volatile("s_getpc_b64 %0" : "=s"(pc));
        asm volatile("global_load_dword %0, %1, %2" : "+v"(pfd) : "v"(threadIdx.x * 128), "s"(pc + min((sbid() >> 3) * 1536, CODE_PF_BYTES - 1536)));
    }
    asm volatile("" ::"s"(x), "s"(W), "s"(wphi), "s"(wo), "s"(out), "s"(ws), "s"(u), "s"(Are), "s"(Aim), "s"(Bre), "s"(Bim), "s"(Cre), "s"(Cim),
                 "s"(logdt), "s"(Dskip));
    const Role R = role_of(sbid());
    u64 tag;
    if (R.is_a) prologue_a(R.ia, Are, Aim, Bre, Bim, Cre, Cim, logdt, Dskip);
    else if (__builtin_amdgcn_readfirstlane(threadIdx.x) >= 384) loader_b<true>(x, W, wo, R.r, R.c, __builtin_amdgcn_readfirstlane(threadIdx.x >> 6) - 6, pfd);
    u32 sel = R.nb;
    asm volatile("" : "+s"(sel));
    if ((sbid() >> 3) >= sel) {
        tag = path_a(u, wo, out, ws, R.ia, pfd STAMP_PASS);
    } else {
        tag = path_b(x, W, wphi, wo, out, ws, R.r, R.c, pfd STAMP_PASS);
    }
    out_proj(out, ws, tag, R.is_a ? R.ia : -1 STAMP_PASS);
}

extern "C" void kernel_launch(void* const* d_in, const int* in_sizes, int n_in, void* d_out, int out_size, void* d_ws, size_t ws_size,
                              hipStream_t stream) {
    const u16* const* in = (const u16* const*)d_in;
    fused_kernel<<<256, 512, 0, stream>>>(in[9], in[10], in[11], in[12], (u16*)d_out, (char*)d_ws, in[0], in[1], in[2], in[3], in[4], in[5],
                                          in[6], in[7], in[8]);
}
```

```cpp
#include <hip/hip_runtime.h>
#include <hip/hip_bf16.h>

typedef unsigned short u16;
typedef unsigned u32;
typedef unsigned long long u64;
typedef __attribute__((ext_vector_type(8))) short s16x8;
typedef __attribute__((ext_vector_type(4))) short s16x4;
typedef __attribute__((ext_vector_type(16))) float f32x16;
typedef __attribute__((ext_vector_type(4))) float f32x4;
typedef __attribute__((ext_vector_type(2))) float f32x2;
typedef __attribute__((ext_vector_type(4))) u32 u32x4;
typedef __attribute__((ext_vector_type(2))) u32 u32x2;
typedef __attribute__((ext_vector_type(2))) __bf16 bf16x2;
#define LAS __attribute__((address_space(3)))
#define GAS __attribute__((address_space(1)))
#define DI __device__ __forceinline__
#define SB __builtin_amdgcn_sched_barrier(0)
DI unsigned sbid() { return __builtin_amdgcn_workgroup_id_x(); }

#define PSLEEP 4
#define STW 0
#define YSLEEP 12
#define AUXA 0
#define AUXB 0
#define RMAP 1
#define UISS 1
#define UPOS 1
#define WOPOS 0
#define YMODE 0
constexpr int LDS_BYTES = 163840;
constexpr u32 WS_EPOCH = 0, WS_ACK = 4096, WS_F1 = 65536, WS_F2 = 131072, WS_PART = 524288, WS_O = 2u << 20, WS_END = 5u << 20;
constexpr u32 WS_STAMP = 16u << 20;
constexpr u32 kSpinCap = 1u << 18;
constexpr u32 CODE_PF_BYTES = 44032;

DI u32 pk(float a, float b) { f32x2 v = {a, b}; return __builtin_bit_cast(u32, __builtin_convertvector(v, bf16x2)); }
DI float lo2f(u32 x) { return __uint_as_float(x << 16); }
DI float hi2f(u32 x) { return __uint_as_float(x & 0xffff0000u); }
DI float b2f(u16 h) { return __uint_as_float((u32)h << 16); }
DI s16x4 trd(u32 a) { return __builtin_amdgcn_ds_read_tr16_b64_v4i16((LAS s16x4*)(size_t)a); }
DI s16x8 cat(s16x4 a, s16x4 b) { return __builtin_shufflevector(a, b, 0, 1, 2, 3, 4, 5, 6, 7); }
DI s16x8 frag(u32 a, u32 b, u32 c, u32 d) { u32x4 v = {a, b, c, d}; return __builtin_bit_cast(s16x8, v); }
template <class T> DI T ldsr(u32 a) { return *(LAS T*)(size_t)a; }
template <class T> DI void ldsw(u32 a, T v) { *(LAS T*)(size_t)a = v; }
template <int N> DI void waitv() { asm volatile("s_waitcnt vmcnt(%0)" ::"i"(N) : "memory"); }
DI void bar() { asm volatile("s_waitcnt lgkmcnt(0)\n\ts_barrier" ::: "memory"); }
DI f32x16 mfma32(s16x8 a, s16x8 b, f32x16 c) { return __builtin_amdgcn_mfma_f32_32x32x16_bf16(a, b, c, 0, 0, 0); }
DI f32x4 mfma16(s16x8 a, s16x8 b, f32x4 c) { return __builtin_amdgcn_mfma_f32_16x16x32_bf16(a, b, c, 0, 0, 0); }
DI float max3(float a, float b, float c) { float r; asm("v_max3_f32 %0, %1, %2, %3" : "=v"(r) : "v"(a), "v"(b), "v"(c)); return r; }
DI float min3(float a, float b, float c) { float r; asm("v_min3_f32 %0, %1, %2, %3" : "=v"(r) : "v"(a), "v"(b), "v"(c)); return r; }
DI float dot2(u32 x, u32 sel, float c) { return __builtin_amdgcn_fdot2_f32_bf16(__builtin_bit_cast(bf16x2, sel), __builtin_bit_cast(bf16x2, x), c, false); }
template <class R> DI void st128(u32x4 v, R rs, u32 voff, u32 soff, int) { __builtin_amdgcn_raw_buffer_store_b128(v, rs, voff + soff, 0, 16); }
DI void out16(u16* out, u32 off, u32x4 v) {
    __builtin_amdgcn_raw_buffer_store_b128(v, __builtin_amdgcn_make_buffer_rsrc((void*)out, 0, 2048 * 2560, 0x00020000), off * 2, 0, AUXA);
}
DI void out8(u16* out, u32 off, u32x2 v) {
    __builtin_amdgcn_raw_buffer_store_b64(v, __builtin_amdgcn_make_buffer_rsrc((void*)out, 0, 2048 * 2560, 0x00020000), off * 2, 0, AUXB);
}
DI void dma(u32 m0v, u32 voff, const void* sbase) {
    asm volatile("s_mov_b32 m0, %0\n\ts_nop 0\n\tglobal_load_lds_dwordx4 %1, %2" ::"s"(m0v), "v"(voff), "s"(sbase) : "memory");
}
DI void dma_m(u32 m0v, u32 voff, const void* sbase, u32 mlo, u32 mhi) {
    asm volatile("s_mov_b32 exec_lo, %3\n\ts_mov_b32 exec_hi, %4\n\ts_mov_b32 m0, %0\n\ts_nop 0\n\tglobal_load_lds_dwordx4 %1, %2\n\ts_mov_b64 exec, -1"
                 ::"s"(m0v), "v"(voff), "s"(sbase), "i"(mlo), "i"(mhi) : "memory");
}
#define STAMP(i)
#define STAMP_ARG
#define STAMP_PASS

DI void post_flag(char* fl, u64 tag, int tid, int n, int me) {
    if (tid < n) __hip_atomic_store((GAS u64*)(fl + tid * 256 + me * 8), tag, __ATOMIC_RELAXED, __HIP_MEMORY_SCOPE_AGENT);
}
DI void wait_flags(const char* fl, u64 tag, int l, int n) {
    const GAS u64* p = (const GAS u64*)(fl + (l < n ? l : 0) * 8);
    u64 v0 = __hip_atomic_load(p, __ATOMIC_RELAXED, __HIP_MEMORY_SCOPE_AGENT);
#pragma unroll 1
    for (u32 s = 0; s < kSpinCap; s++) {
        __builtin_amdgcn_s_sleep(PSLEEP);
        const u64 v1 = __hip_atomic_load(p, __ATOMIC_RELAXED, __HIP_MEMORY_SCOPE_AGENT);
        if (__all(v0 == tag)) break;
        v0 = v1;
    }
}

template <class F> DI void features(const float (&v)[20], const s16x8 (&wf)[2][3], u32 (&phi)[20], F filler) {
    u32 b[10];
#pragma unroll
    for (int i = 0; i < 10; i++) b[i] = pk(v[2 * i], v[2 * i + 1]);
    const s16x8 B0 = frag(b[0], b[1], b[2], b[3]), B1 = frag(b[4], b[5], b[6], b[7]), B2 = frag(b[8], b[9], 0, 0);
    f32x16 p0 = {}, p1 = {};
    p0 = mfma32(wf[0][0], B0, p0); p1 = mfma32(wf[1][0], B0, p1);
    p0 = mfma32(wf[0][1], B1, p0); p1 = mfma32(wf[1][1], B1, p1);
    p0 = mfma32(wf[0][2], B2, p0); p1 = mfma32(wf[1][2], B2, p1);
    SB; filler(); SB;
    asm volatile("s_nop 11" : "+v"(p0), "+v"(p1));
    float p[20];
#pragma unroll
    for (int i = 0; i < 20; i++) p[i] = i < 16 ? p0[i] : p1[i - 16];
    float mxa = max3(p[0], p[1], p[2]), mna = min3(p[0], p[1], p[2]), mxb = max3(p[3], p[4], p[5]), mnb = min3(p[3], p[4], p[5]);
#pragma unroll
    for (int i = 6; i < 18; i += 4) {
        mxa = max3(mxa, p[i], p[i + 1]); mna = min3(mna, p[i], p[i + 1]);
        mxb = max3(mxb, p[i + 2], p[i + 3]); mnb = min3(mnb, p[i + 2], p[i + 3]);
    }
    mxa = max3(mxa, p[18], p[19]); mna = min3(mna, p[18], p[19]);
    const float mx = max3(mxa, mxb, mxb), mn = min3(mna, mnb, mnb);
    const float L2E = 1.4426950408889634f;
    const float cx = -mx * L2E, cn = mn * L2E;
    float e[40], s1, s2;
#pragma unroll
    for (int i = 0; i < 20; i++) {
        e[i] = __builtin_amdgcn_exp2f(__builtin_fmaf(p[i], L2E, cx));
        e[20 + i] = __builtin_amdgcn_exp2f(__builtin_fmaf(p[i], -L2E, cn));
        if (i == 0) { s1 = e[0]; s2 = e[20]; } else { s1 += e[i]; s2 += e[20 + i]; }
    }
    const float r1 = __builtin_amdgcn_rcpf(s1), r2 = __builtin_amdgcn_rcpf(s2);
#pragma unroll
    for (int i = 0; i < 10; i++) { phi[i] = pk(e[2 * i] * r1, e[2 * i + 1] * r1); phi[10 + i] = pk(e[20 + 2 * i] * r2, e[21 + 2 * i] * r2); }
}

constexpr int WBLK = 960, WSLOT = 8 * WBLK, XSLOT = 24576;
constexpr int LW = XSLOT, LIMG = 102400, LMISC = LIMG + 2 * XSLOT, LZERO = LMISC + 3200, LTAB = LZERO + 64, TROW = 56, THEAD = 40 * TROW;
DI int xslot(int j) { return j % 3 == 0 ? 0 : LIMG + (j % 3 - 1) * XSLOT; }
static_assert(LTAB + 80 * TROW <= LDS_BYTES, "LDS map");

template <bool V> struct Flag { static constexpr bool value = V; };

struct Role { bool is_a; int r, c, ia; u32 nb; };
DI Role role_of(u32 bid) {
    const int xcd = bid & 7, j = bid >> 3;
    Role R;
    const int e = xcd & 1, m = xcd >> 1, nb = e ? 20 : 24, cq = e ? (j * 52) >> 8 : (j * 43) >> 8;
    R.r = __builtin_amdgcn_readfirstlane(e ? 6 + j - 5 * cq : j - 6 * cq); R.c = __builtin_amdgcn_readfirstlane(4 * m + cq);
    R.ia = __builtin_amdgcn_readfirstlane((RMAP == 1 ? (e ? 32 + 12 * m : 8 * m) : 20 * m + (e ? 8 : 0)) + j - nb);
    R.is_a = j >= nb; R.nb = nb;
    return R;
}

DI void load_wo(const u16* wo, int cb, int wi, int end, int nw, int l) {
    u32 off[5];
#pragma unroll
    for (int i = 0; i < 5; i++) { const u32 idx = 64 * i + l, row = (idx * 6554u) >> 16; off[i] = row * 1120 + idx * 16; }
#pragma unroll 1
    for (int gq = wi; gq < end; gq += nw) {
        const char* gp = (const char*)wo + cb * 160 + gq * (32 * 1280);
#pragma unroll
        for (int i = 0; i < 5; i++) dma(gq * 5120 + i * 1024, off[i], gp);
    }
}

DI void out_proj(u16* __restrict__ out, char* __restrict__ ws, u64 tag, int ia STAMP_ARG) {
    const int bid = sbid(), tid = threadIdx.x, l = tid & 63, w = __builtin_amdgcn_readfirstlane(tid >> 6);
    const int t = 4 * (bid & 7) + (bid >> 6), cb = (bid >> 3) & 7, rs = (t * 43) >> 7, sub = t - 3 * rs;
    bar();
    if (w >= 4) {
        if (ia >= 0) {
            u32x2 v[8];
#pragma unroll
            for (int c2 = 0; c2 < 8; c2++) v[c2] = ldsr<u32x2>(131072 + c2 * 4096 + (tid - 256) * 8);
            const u32 op = 4 * (tid - 256) * 1280 + 8 * ia;
            bar();
            __builtin_amdgcn_s_sleep(YSLEEP);
#pragma unroll
            for (int i = 0; i < 4; i++) {
                const u32 sel = (i & 1) ? 0x07060302u : 0x05040100u;
                out16(out, op + i * 1280, u32x4{__builtin_amdgcn_perm(v[1][i >> 1], v[0][i >> 1], sel), __builtin_amdgcn_perm(v[3][i >> 1], v[2][i >> 1], sel),
                                                 __builtin_amdgcn_perm(v[5][i >> 1], v[4][i >> 1], sel), __builtin_amdgcn_perm(v[7][i >> 1], v[6][i >> 1], sel)});
            }
        }
        return;
    }
    constexpr int NPRE = 6;
    const auto rsw = __builtin_amdgcn_make_buffer_rsrc((void*)ws, 0, WS_END, 0x00020000);
    const int g = l >> 4, q = (l >> 2) & 3, p = l & 3, hp = g & 1, P = g >> 1;
    u32 b1 = (20 * hp + 8 * P + q) * 160 + p * 8, b2 = (80 * P + 20 * hp + 16 + q) * 160 + p * 8;
    u32 b1h = b1 + 51200, b2h = b2 + 51200;
    asm volatile("" : "+v"(b1), "+v"(b2), "+v"(b1h), "+v"(b2h));
    s16x4 wfr[NPRE][10];
    auto rdw = [&](int ks, int n) {
        const int G = ks / 5, i = ks % 5, tt = n >> 1, rd = n & 1;
        const u32 so = (G & 1) * 25600;
        wfr[ks % NPRE][n] = i < 4 ? trd((G >> 1 ? b1h : b1) + so + (40 * i + 4 * rd) * 160 + 32 * tt) : trd((G >> 1 ? b2h : b2) + so + 40 * rd * 160 + 32 * tt);
    };
#pragma unroll
    for (int ks = 0; ks < NPRE; ks++)
#pragma unroll
        for (int n = 0; n < 10; n++) rdw(ks, n);
    if (w == 0) wait_flags(ws + WS_F2 + (rs * 24 + sub * 8 + cb) * 256, tag, l, 16);
    bar();
    STAMP(8);
    const int ti = 16 * (w & 1) + (l & 15);
    const u32 ob = WS_O + (rs * 96 + 2 * sub + (w >> 1)) * 2560;
    const u32 v1 = P * 1024 + (hp * 32 + ti) * 16, v2 = 2048 + 2 * P * 15360 + (hp * 32 + ti) * 8;
    u32x4 of[16]; u32x2 ot[8];
    auto ldo = [&](int n) {
        const int G = n / 6, i = n % 6;
        if (i < 4) of[4 * G + i] = __builtin_amdgcn_raw_buffer_load_b128(rsw, v1, ob + (4 * G + i) * 15360, 16);
        else ot[2 * G + i - 4] = __builtin_amdgcn_raw_buffer_load_b64(rsw, v2, ob + (4 * G + i - 4) * 15360, 16);
    };
#pragma unroll
    for (int n = 0; n < 10; n++) ldo(n);
    if (bid == 0 && tid == 0) __hip_atomic_store((GAS u64*)(ws + WS_EPOCH), tag, __ATOMIC_RELAXED, __HIP_MEMORY_SCOPE_AGENT);
    f32x4 oc[5] = {};
#pragma unroll
    for (int ks = 0; ks < 20; ks++) {
        const int G = ks / 5, i = ks % 5;
        const s16x8 B = i < 4 ? __builtin_bit_cast(s16x8, of[4 * G + i]) : frag(ot[2 * G][0], ot[2 * G][1], ot[2 * G + 1][0], ot[2 * G + 1][1]);
#pragma unroll
        for (int tt = 0; tt < 5; tt++) {
            SB;
            oc[tt] = mfma16(cat(wfr[ks % NPRE][2 * tt], wfr[ks % NPRE][2 * tt + 1]), B, oc[tt]);
            SB;
            if (ks + NPRE < 20) { rdw(ks + NPRE, 2 * tt); rdw(ks + NPRE, 2 * tt + 1); }
            if ((tt == 0 || tt == 2) && 10 + 2 * ks + (tt >> 1) < 24) ldo(10 + 2 * ks + (tt >> 1));
        }
    }
    SB;
    const u32 op = (64 * t + 16 * w + (l & 15)) * 1280 + 640 + 80 * cb + 4 * g;
#pragma unroll
    for (int tt = 0; tt < 5; tt++) out8(out, op + 16 * tt, u32x2{pk(oc[tt][0], oc[tt][1]), pk(oc[tt][2], oc[tt][3])});
    STAMP(9);
}

template <bool FIRST>
DI void loader_b(const u16* __restrict__ x, const u16* __restrict__ W, const u16* __restrict__ wo, int r, int c, int lw, u32& pfd) {
    const int l = threadIdx.x & 63;
    const int ph = (((r + 4 * c) & 15) * 10) >> 4;
    const int pl = l >> 4, idx = l & 15, q3 = (idx * 11) >> 5;
    const int q = pl < 3 ? idx >> 2 : q3 & 3, sg = pl < 3 ? pl : idx - 3 * q3, ch = pl < 3 ? (idx & 3) + ((idx >> 1) & 1) : 2;
    const u32 wl = q * 3840 + sg * 1280 + ch * 16 + (sg == 0 ? ((c & 1) ? 0 : 160) : 80);
    const u32 xo0 = (l >> 3) * 1280 + (((l & 7) ^ (l >> 4)) << 4), xo1 = ((l >> 3) + 8) * 1280 + (((l & 7) ^ ((l >> 4) | 4)) << 4);
    u32 xr[2][3];
#pragma unroll
    for (int i = 0; i < 6; i++) { const int row = 192 * r + 96 * (i / 3) + 48 * lw + 16 * (i % 3); xr[i / 3][i % 3] = (row < 2048 ? row : row - 64) * 1280; }
    const char* wb = (const char*)W + c * 80 - 80 + lw * (16 * 3840);
    auto ld_x = [&](int hx) {
        const int jx = hx >> 1, hh = hx & 1;
        int jr = jx + ph; if (jr >= 10) jr -= 10;
        const char* g = (const char*)x + jr * 128;
        const u32 d = xslot(jx) + (96 * hh + 48 * lw) * 128;
#pragma unroll
        for (int i = 0; i < 3; i++) { dma(d + i * 2048, xo0, g + xr[hh][i]); dma(d + i * 2048 + 1024, xo1, g + xr[hh][i]); }
    };
    auto ld_w = [&](int i) {
        int ir = i + 2 * ph; if (ir >= 20) ir -= 20;
        const char* g = wb + ir * (32 * 3840);
        const u32 d = LW + (i & 3) * WSLOT + lw * 4 * WBLK;
#pragma unroll
        for (int b = 0; b < 4; b++) dma_m(d + b * WBLK, wl, g + b * (4 * 3840), 0xffffffffu, 0x0fffffffu);
    };
    if (FIRST) { ld_w(0); ld_x(0); ld_x(1); return; }
    ld_w(1); ld_x(2); ld_x(3);
    waitv<16>();
    bar();
    ld_w(2); ld_w(3); ld_x(4);
#pragma unroll
    for (int i = 0; i < 19; i++) {
        if (i == 0 || i == 2) waitv<26>(); else if (i <= 15) waitv<20>(); else if (i == 16) waitv<14>(); else if (i == 17) waitv<4>(); else waitv<2>();
        bar();
        if (i + 5 < 20) ld_x(i + 5);
        if (i + 4 < 20) ld_w(i + 4);
        if (i == 17) {
            const u32 ln = __builtin_amdgcn_readfirstlane(min((sbid() >> 3) * 256 + lw * 64, 6208u)) + l;
            asm volatile("global_load_dword %0, %1, %2\n\tglobal_load_dword %0, %1, %3" : "+v"(pfd) : "v"(ln * 128), "s"(wo), "s"((const char*)wo + 128 * 128));
        }
    }
}

DI u64 path_b(const u16* __restrict__ x, const u16* __restrict__ W, const u16* __restrict__ wphi, const u16* __restrict__ wo,
               u16* __restrict__ out, char* __restrict__ ws, int r, int c, u32& pfd STAMP_ARG) {
    const int tid = threadIdx.x, l = tid & 63;
    const int w = __builtin_amdgcn_readfirstlane(tid >> 6);
    const auto rsw = __builtin_amdgcn_make_buffer_rsrc((void*)ws, 0, WS_END, 0x00020000);
    const int g = l >> 4, q = (l >> 2) & 3, p = l & 3, h = l >> 5;
    const int rq = 2 * (g & 1) + (p >> 1), hk = g >> 1;
    const bool same = hk == (p & 1);
    u64 tag;
    u32 phiq[20];
    f32x16 acc[4] = {};
    s16x8 wfq[2][3];
    if (w >= 6) {
        loader_b<false>(x, W, wo, r, c, w - 6, pfd);
        asm volatile("s_load_dwordx2 %0, %1, 0\n\ts_waitcnt lgkmcnt(0)" : "=&s"(tag) : "s"(ws + WS_EPOCH) : "memory");
        if (sbid() == 0) wait_flags(ws + WS_ACK + (w - 6) * 320, tag + 1, l, 40);
    } else {
        if (w < 4) dma_m(LMISC + w * 800, l * 16, (const char*)wphi + ((c ^ (w >> 1)) * 2 + (w & 1)) * 800, 0xffffffffu, 0x3ffffu);
        if (tid < 2) ldsw<u64>(LZERO + tid * 8, 0ull);
        u32 bA = LW + (g >> 1) * 2 * WBLK + q * 64 + (p & 1) * 32 + (g & 1) * 16 + (p >> 1) * 8;
        u32 bD = LW + (g >> 1) * 2 * WBLK + 768 + (3 * q + 2 * (g & 1) + (p >> 1)) * 16 + (p & 1) * 8;
        asm volatile("" : "+v"(bA), "+v"(bD));
        const int trow = 32 * w + (l & 31);
        s16x8 wfk[2][3];
        auto load_wf = [&](s16x8 (&wf)[2][3], u32 base) {
#pragma unroll
            for (int to = 0; to < 2; to++)
#pragma unroll
                for (int s = 0; s < 3; s++) {
                    s16x4 t[2];
#pragma unroll
                    for (int rd = 0; rd < 2; rd++) {
                        const int mu = 2 * s + rd;
                        if (mu < 5) {
                            int dq = mu + hk; if (mu == 4) dq = hk ? 0 : 4;
                            const bool ok = same && (to == 0 || rq == 0);
                            t[rd] = trd(ok ? base + hk * 800 + (4 * dq + q) * 40 + (4 * to + rq) * 8 : LZERO);
                        } else t[rd] = s16x4{0, 0, 0, 0};
                    }
                    wf[to][s] = cat(t[0], t[1]);
                }
        };
        asm volatile("s_load_dwordx2 %0, %1, 0\n\ts_waitcnt vmcnt(0) lgkmcnt(0)" : "=&s"(tag) : "s"(ws + WS_EPOCH), "v"(pfd) : "memory");
        bar();
        STAMP(1);
        const u32 sw = ((trow & 2) << 1) | ((trow & 4) >> 1) | (trow & 1);
        const u32 rowa = LIMG + h * XSLOT + trow * 128;
        auto gemm = [&](auto HV) {
            constexpr bool hv = decltype(HV)::value;
            constexpr int NB = hv ? 3 : 1, NM = hv ? 6 : 3, NR = hv ? 11 : 7;
            f32x16 vc[3] = {};
            u32 aX[4], aXh[4];
            const int tx = hv ? 32 * (w - 2) + (l & 31) : trow;
#pragma unroll
            for (int k = 0; k < 4; k++) {
                aX[k] = tx * 128 + (((2 * k + h) ^ ((tx >> 1) & 7)) << 4); aXh[k] = aX[k] + LIMG;
                asm volatile("" : "+v"(aX[k]), "+v"(aXh[k]));
            }
            s16x4 fa[2][8]; s16x8 fb[2][NB];
            auto rd = [&](int s, int i, int ks, int n) {
                const int nb = hv ? (n == 0 ? 1 : n == 9 ? 0 : n == 10 ? 2 : -1) : (n == 0 ? 0 : -1);
                if (nb >= 0) {
                    const int sl = (i >> 1) % 3, k = (i & 1) * 2 + ks;
                    fb[s][nb] = sl == 0 ? ldsr<s16x8>(aX[k] + nb * 8192) : ldsr<s16x8>(aXh[k] + (sl - 1) * XSLOT + nb * 8192);
                } else {
                    const int na = n <= 4 ? n - 1 : n <= 6 ? n + 1 : n - 3;
                    const u32 o = (i & 3) * WSLOT + ks * 4 * WBLK + (na & 1) * WBLK;
                    fa[s][na] = na < 6 ? trd(bA + o + (na >> 1) * 256) : trd(bD + o);
                }
            };
            auto mm = [&](int s, int t) {
                if (t < 3) { const int tt = t == 2 ? 3 : t; acc[tt] = mfma32(cat(fa[s][2 * tt], fa[s][2 * tt + 1]), fb[s][hv ? 1 : 0], acc[tt]); }
                else { const int nb = t == 3 ? 1 : t == 4 ? 0 : 2; vc[nb] = mfma32(cat(fa[s][4], fa[s][5]), fb[s][hv ? nb : 0], vc[nb]); }
            };
            auto halfstep = [&](int s, int i, int ks, bool next) {
#pragma unroll
                for (int t = 0; t < NM; t++) {
                    SB; mm(s, t); SB;
                    if (t == 0 && ks == 0 && next) bar();
                    if (next)
#pragma unroll
                        for (int n = 3 * t; n < 3 * t + 3 && n < NR; n++) rd(s ^ 1, i, ks, n);
                }
            };
#pragma unroll
            for (int n = 0; n < NR; n++) rd(0, 0, 0, n);
#pragma unroll
            for (int i = 0; i < 20; i++) {
                halfstep(0, i, 1, true);
                if (i == 1) load_wf(wfk, LMISC);
                if (i == 2) load_wf(wfq, LMISC + 1600);
                halfstep(1, i + 1, 0, i < 19);
            }
            if (hv) {
#pragma unroll
                for (int nb = 0; nb < 3; nb++)
#pragma unroll
                    for (int mu = 0; mu < 4; mu++) {
                        const int pc = 10 + mu + h;
                        ldsw<u32x2>(rowa + (nb - 1) * 8192 + (((pc >> 1) ^ sw) << 4) + (pc & 1) * 8,
                                    u32x2{pk(vc[nb][4 * mu], vc[nb][4 * mu + 1]), pk(vc[nb][4 * mu + 2], vc[nb][4 * mu + 3])});
                    }
            }
        };
        if ((w & 3) >= 2) gemm(Flag<true>{}); else gemm(Flag<false>{});
        STAMP(2);
        float kr[20]; u32 phik[20];
#pragma unroll
        for (int i = 0; i < 20; i++) kr[i] = i < 16 ? acc[1][i] : acc[3][i - 12];
        features(kr, wfk, phik, [&]() {
            const int pc = h ? 10 : 14;
            ldsw<u32x2>(rowa + (((pc >> 1) ^ sw) << 4) + (pc & 1) * 8, u32x2{pk(acc[3][8], acc[3][9]), pk(acc[3][10], acc[3][11])});
            ldsw<u32x2>(rowa + ((7 ^ sw) << 4) + 8, u32x2{0x3f80u, 0u});
        });
        const u32 keep = 192 * r + 32 * w < 2048 ? 0xffffffffu : 0u;
#pragma unroll
        for (int ch = 0; ch < 5; ch++) ldsw<u32x4>(rowa + ((ch ^ sw) << 4), u32x4{phik[4 * ch], phik[4 * ch + 1], phik[4 * ch + 2], phik[4 * ch + 3]} & keep);
        STAMP(10);
    }
    tag += 1;
    bar();
    if (w >= 6) load_wo(wo, (sbid() >> 3) & 7, w - 6, 10, 2, l);
    if (w < 4) {
        const int pcn = 4 * (g & 1) + p;
        const int pa = 10 + (pcn < 6 ? pcn : 5), pb = 5 * (w & 1) + (pcn < 5 ? pcn : 4);
        u32 aa[2], ab[2];
#pragma unroll
        for (int rd = 0; rd < 2; rd++) {
            const int row = 8 * (g >> 1) + 4 * rd + q;
            const u32 sw = ((row & 2) << 1) | ((row & 4) >> 1) | (row & 1);
            const u32 base = LIMG + (w >> 1) * XSLOT + row * 128;
            aa[rd] = base + (((pa >> 1) ^ sw) << 4) + (pa & 1) * 8;
            ab[rd] = base + (((pb >> 1) ^ sw) << 4) + (pb & 1) * 8;
            asm volatile("" : "+v"(aa[rd]), "+v"(ab[rd]));
        }
        s16x4 ta[24], tb[24];
#pragma unroll
        for (int ks = 0; ks < 12; ks++) {
            ta[2 * ks] = trd(aa[0] + ks * 2048); ta[2 * ks + 1] = trd(aa[1] + ks * 2048);
            tb[2 * ks] = trd(ab[0] + ks * 2048); tb[2 * ks + 1] = trd(ab[1] + ks * 2048);
        }
        SB;
        f32x16 kv = {};
#pragma unroll
        for (int ks = 0; ks < 12; ks++) kv = mfma32(cat(ta[2 * ks], ta[2 * ks + 1]), cat(tb[2 * ks], tb[2 * ks + 1]), kv);
        if ((l & 31) < 20) {
            const u32 lam = h * 20 + (l & 31), o = WS_PART + (c * 16 + r) * 4096 + w * 960;
            st128(u32x4{pk(kv[0], kv[1]), pk(kv[2], kv[3]), pk(kv[4], kv[5]), pk(kv[6], kv[7])}, rsw, lam * 16, o, 16);
            __builtin_amdgcn_raw_buffer_store_b64(u32x2{pk(kv[8], kv[9]), pk(kv[10], kv[11])}, rsw, 640 + lam * 8, o, 16);
        }
    }
    STAMP(11);
    if (w < 6) {
        float qr[20];
#pragma unroll
        for (int i = 0; i < 20; i++) qr[i] = i < 16 ? acc[0][i] : acc[3][i - 16];
        features(qr, wfq, phiq, []() {});
#pragma unroll
        for (int i = 0; i < 20; i++) asm volatile("" : "+v"(phiq[i]));
        STAMP(17);
        waitv<0>();
    }
    bar();
    post_flag(ws + WS_F1 + c * 4096, tag, tid, 11, r);
    STAMP(3);
    if (w >= 6) load_wo(wo, (sbid() >> 3) & 7, w + 4, 20, 2, l);
    if (tid < 80) ldsw<u64>(LTAB + tid * TROW + 48, 0ull);
    if (w == 0) wait_flags(ws + WS_F1 + (c ^ 1) * 4096 + r * 256, tag, l, 11);
    bar();
    STAMP(5);
    if (w < 4) {
        const int t = tid < 240 ? tid : 239;
        const int un = (t * 1093) >> 16, it = t - un * 60;
        u32x4 pv[11];
#pragma unroll
        for (int i = 0; i < 11; i++) pv[i] = __builtin_amdgcn_raw_buffer_load_b128(rsw, t * 16, WS_PART + (c ^ 1) * 65536 + i * 4096, 16);
        float s[8] = {};
        u32 slo = 0x00003f80u, shi = 0x3f800000u;
        asm volatile("" : "+s"(slo), "+s"(shi));
#pragma unroll
        for (int i = 0; i < 11; i++)
#pragma unroll
            for (int e = 0; e < 4; e++) { s[2 * e] = dot2(pv[i][e], slo, s[2 * e]); s[2 * e + 1] = dot2(pv[i][e], shi, s[2 * e + 1]); }
        const int lam = it < 40 ? it : 2 * (it - 40);
        const int hh = lam >= 20, f = lam - 20 * hh;
        const u32 a0 = LTAB + (un >> 1) * THEAD + ((un & 1) * 20 + f) * TROW + hh * 8 + (it < 40 ? 0 : 32);
        ldsw<u32x2>(a0, u32x2{pk(s[0], s[1]), pk(s[2], s[3])});
        ldsw<u32x2>(a0 + (it < 40 ? 16 : TROW), u32x2{pk(s[4], s[5]), pk(s[6], s[7])});
    }
    bar();
    STAMP(6);
    if (w < 6 && 192 * r + 32 * w < 2048) {
        const u32 tb = LTAB + hk * THEAD + q * TROW;
        u32 t0 = tb + (same ? rq : 6) * 8, t1 = tb + (same && rq < 2 ? 4 + rq : 6) * 8;
        asm volatile("" : "+v"(t0), "+v"(t1));
        f32x16 o0 = {}, o1 = {};
#pragma unroll
        for (int s = 0; s < 5; s++) {
            const s16x8 B = frag(phiq[4 * s], phiq[4 * s + 1], phiq[4 * s + 2], phiq[4 * s + 3]);
            o0 = mfma32(cat(trd(t0 + 8 * s * TROW), trd(t0 + (8 * s + 4) * TROW)), B, o0);
            o1 = mfma32(cat(trd(t1 + 8 * s * TROW), trd(t1 + (8 * s + 4) * TROW)), B, o1);
        }
        const float rden = __builtin_amdgcn_rcpf(o1[4] + 1e-6f);
        u32 ob[10];
#pragma unroll
        for (int i = 0; i < 10; i++) ob[i] = i < 8 ? pk(o0[2 * i] * rden, o0[2 * i + 1] * rden) : pk(o1[2 * i - 16] * rden, o1[2 * i - 15] * rden);
        const u32 o = WS_O + ((r * 16 + (c ^ 1)) * 6 + w) * 2560;
        st128(u32x4{ob[0], ob[1], ob[2], ob[3]}, rsw, l * 16, o, 16);
        st128(u32x4{ob[4], ob[5], ob[6], ob[7]}, rsw, l * 16 + 1024, o, 16);
        __builtin_amdgcn_raw_buffer_store_b64(u32x2{ob[8], ob[9]}, rsw, l * 8 + 2048, o, 16);
    }
    STAMP(12);
    waitv<0>();
    asm volatile("" ::"v"(pfd));
    bar();
    post_flag(ws + WS_F2 + r * (24 * 256), tag, tid, 24, c);
    STAMP(7);
    return tag;
}

DI f32x2 cmul(f32x2 a, f32x2 b) {
    f32x2 d;
    asm("v_pk_mul_f32 %0, %1, %2 op_sel_hi:[0,1]\n\tv_pk_fma_f32 %0, %1, %2, %0 op_sel:[1,1,0] op_sel_hi:[1,0,1] neg_lo:[0,1,0]" : "=&v"(d) : "v"(a), "v"(b));
    return d;
}
DI f32x2 cmulc(f32x2 a, f32x2 b) {
    f32x2 d;
    asm("v_pk_mul_f32 %0, %1, %2 op_sel_hi:[0,1] neg_hi:[0,1]\n\tv_pk_fma_f32 %0, %1, %2, %0 op_sel:[1,1,0] op_sel_hi:[1,0,1]" : "=&v"(d) : "v"(a), "v"(b));
    return d;
}
DI f32x2 csq(f32x2 a) { return cmul(a, a); }
DI f32x2 cexp(float a, float b) {
    const float m = __builtin_amdgcn_exp2f(a * 1.4426950408889634f);
    const float c1 = 0.15915494309189535f, c2 = 6.4206382e-9f;
    const float n = __builtin_rintf(b * c1);
    const float f = __builtin_fmaf(b, c2, __builtin_fmaf(b, c1, -n));
    return f32x2{m * __builtin_amdgcn_cosf(f), m * __builtin_amdgcn_sinf(f)};
}
constexpr int LU = 131072;
DI u32 kaddr(u32 e) { return ((e >> 5) << 8) + ((e & 31) << 1); }

DI void prologue_a(int ia, const u16* __restrict__ Are, const u16* __restrict__ Aim, const u16* __restrict__ Bre, const u16* __restrict__ Bim,
                   const u16* __restrict__ Cre, const u16* __restrict__ Cim, const u16* __restrict__ logdt, const u16* __restrict__ Dskip) {
    const int l = threadIdx.x & 63, w = __builtin_amdgcn_readfirstlane(threadIdx.x >> 6);
    const u16* src = w == 0 ? Are : w == 1 ? Aim : w == 2 ? Bre : w == 3 ? Bim : w == 4 ? Cre : w == 5 ? Cim : w == 6 ? logdt : Dskip;
    if (w < 6) { dma(w * 16384 + 8192, ia * 2048 + l * 16, src); dma(w * 16384 + 9216, ia * 2048 + 1024 + l * 16, src); }
    else dma_m(w * 16384 + 8192, ia * 16, src, 1u, 0u);
}

DI u64 path_a(const u16* __restrict__ u, const u16* __restrict__ wo, u16* __restrict__ out, char* __restrict__ ws, int ia, u32 pfd STAMP_ARG) {
    const int tid = threadIdx.x, l = tid & 63;
    const int w = __builtin_amdgcn_readfirstlane(tid >> 6);
    const u32 KB = w * 16384;
    const int g = l >> 4, q = (l >> 2) & 3, p = l & 3, h = l >> 5;
    STAMP(1);
    u64 tag;
    asm volatile("s_load_dwordx2 %0, %1, 0\n\ts_waitcnt vmcnt(0) lgkmcnt(0)" : "=&s"(tag) : "s"(ws + WS_EPOCH), "v"(pfd) : "memory");
    tag += 1;
    bar();
    if (tid == 0) __hip_atomic_store((GAS u64*)(ws + WS_ACK + ia * 8), tag, __ATOMIC_RELAXED, __HIP_MEMORY_SCOPE_AGENT);
    const float ldt = b2f(ldsr<u16>(6 * 16384 + 8192 + w * 2)), dsk = b2f(ldsr<u16>(7 * 16384 + 8192 + w * 2));
    u32 prm[6];
#pragma unroll
    for (int i = 0; i < 6; i++) prm[i] = ldsr<u32>(i * 16384 + 8192 + w * 256 + l * 4);
    bar();
    u32x4 ur[4];
    auto u_in = [&]() {
#pragma unroll
        for (int i = 0; i < 4; i++) ur[i] = *(const u32x4*)((const char*)u + (size_t)(256 * ((w + ia) & 7) + 4 * l + i) * 1280 + ia * 16);
    };
    if (UISS == 0) u_in();
    const float dt = lo2f(pk(expf(ldt), 0.f));
    STAMP(2);
    f32x16 kd = {};
    u32 ra[2];
#pragma unroll
    for (int rd = 0; rd < 2; rd++) ra[rd] = KB + (8 * (g >> 1) + 4 * rd + q) * 64 + (((2 * (g & 1) + (p >> 1)) ^ ((2 * rd + (q >> 1)) & 3)) << 4) + (p & 1) * 8;
    const u32 wa = KB + l * 64, wsw = (l >> 1) & 3;
#pragma unroll
    for (int ps = 0; ps < 2; ps++) {
        float pf[6];
#pragma unroll
        for (int i = 0; i < 6; i++) pf[i] = ps ? hi2f(prm[i]) : lo2f(prm[i]);
        const float a = dt * pf[0], b = dt * pf[1];
        const f32x2 Wc = cmulc(f32x2{pf[4], -pf[5]}, f32x2{dt * pf[2], dt * pf[3]});
        const f32x2 e1 = cexp(a, b), e16 = cexp(16.f * a, 16.f * b), e256 = cexp(256.f * a, 256.f * b);
        const f32x2 e2 = csq(e1), e4 = csq(e2), e8 = csq(e4), e32 = csq(e16), e64 = csq(e32), e128 = csq(e64), e512 = csq(e256), e1024 = csq(e512);
        f32x2 X[32];
        X[0] = Wc; X[1] = cmulc(Wc, e1); X[2] = cmulc(Wc, e2); X[3] = cmulc(X[1], e2);
#pragma unroll
        for (int i = 0; i < 4; i++) X[4 + i] = cmulc(X[i], e4);
        X[8] = cmulc(Wc, e256); X[9] = cmulc(X[8], e2); X[10] = cmulc(X[8], e4);
        X[12] = X[10]; X[13] = cmulc(X[9], e4); X[14] = cmulc(X[8], e8);
        X[11] = cmulc(Wc, e512); X[16] = cmulc(X[11], e4); X[17] = cmulc(X[11], e8);
        X[15] = cmulc(X[8], e512); X[20] = cmulc(X[15], e4); X[21] = cmulc(X[15], e8);
        X[18] = cmulc(Wc, e1024); X[19] = cmulc(X[18], e8);
        X[24] = cmulc(X[8], e1024); X[25] = cmulc(X[24], e8);
        X[22] = cmulc(X[11], e1024); X[23] = cmulc(X[22], e8);
        X[28] = cmulc(X[15], e1024); X[29] = cmulc(X[28], e8);
        X[26] = X[27] = X[30] = X[31] = f32x2{0.f, 0.f};
        f32x2 Y[8];
        Y[0] = f32x2{1.f, 0.f}; Y[1] = e8; Y[2] = e16; Y[3] = cmul(e8, e16); Y[4] = e32; Y[5] = cmul(e8, e32); Y[6] = cmul(e16, e32); Y[7] = cmul(Y[3], e32);
        const f32x2 e192 = cmul(e64, e128);
#pragma unroll
        for (int ch = 0; ch < 4; ch++) {
            const f32x2* Xc = X + 8 * ch;
            const u32 o = wa + ((ch ^ wsw) << 4);
            ldsw<u32x4>(o, u32x4{pk(Xc[0].x, Xc[1].x), pk(Xc[2].x, Xc[3].x), pk(Xc[4].x, Xc[5].x), pk(Xc[6].x, Xc[7].x)});
            ldsw<u32x4>(o + 4096, u32x4{pk(Xc[0].y, Xc[1].y), pk(Xc[2].y, Xc[3].y), pk(Xc[4].y, Xc[5].y), pk(Xc[6].y, Xc[7].y)});
            f32x2 Z[8];
            const f32x2 m = ch == 1 ? e64 : ch == 2 ? e128 : e192;
#pragma unroll
            for (int i = 0; i < 8; i++) Z[i] = ch == 0 ? Y[i] : i == 0 ? m : cmul(Y[i], m);
            ldsw<u32x4>(o + 8192, u32x4{pk(Z[0].x, Z[1].x), pk(Z[2].x, Z[3].x), pk(Z[4].x, Z[5].x), pk(Z[6].x, Z[7].x)});
            ldsw<u32x4>(o + 12288, u32x4{pk(Z[0].y, Z[1].y), pk(Z[2].y, Z[3].y), pk(Z[4].y, Z[5].y), pk(Z[6].y, Z[7].y)});
        }
        s16x4 fx[16], fy[16];
#pragma unroll
        for (int ks = 0; ks < 8; ks++) {
            fx[2 * ks] = trd(ra[0] + ks * 1024); fx[2 * ks + 1] = trd(ra[1] + ks * 1024);
            fy[2 * ks] = trd(ra[0] + ks * 1024 + 8192); fy[2 * ks + 1] = trd(ra[1] + ks * 1024 + 8192);
        }
        if (ps == 0) { STAMP(4); if (UISS == 1) u_in(); }
#pragma unroll
        for (int ks = 0; ks < 8; ks++) kd = mfma32(cat(fx[2 * ks], fx[2 * ks + 1]), cat(fy[2 * ks], fy[2 * ks + 1]), kd);
    }
    STAMP(6);
    {
        const int j = l & 31;
        ldsw<u32x2>(KB + kaddr(2044 - 8 * j - 4 * h), u32x2{pk(kd[3], kd[2]), pk(kd[1], l == 0 ? kd[0] + dsk : kd[0])});
        ldsw<u32x2>(KB + kaddr(2044 - 256 - 8 * j - 4 * h), u32x2{pk(kd[6], kd[5]), pk(kd[4], kd[4])});
        ldsw<u32x4>(KB + kaddr(2040 - 512 - 256 * h - 8 * j), u32x4{pk(kd[9], kd[9]), pk(kd[8], kd[8]), pk(kd[8], kd[7]), pk(kd[7], kd[7])});
#pragma unroll
        for (int sp = 0; sp < 2; sp++) {
            const float a = kd[10 + 2 * sp], b = kd[11 + 2 * sp];
            const u32 aa = pk(a, a), bb = pk(b, b);
            ldsw<u32x4>(KB + kaddr(2040 - 1024 - 512 * h - 256 * sp - 8 * j), u32x4{bb, (j & 1) ? bb : pk(b, a), aa, aa});
        }
        u32x4 v[4]; u32x2 n[4];
        const u32 a0 = KB + kaddr(8 * l), a1 = KB + kaddr(8 * l + 8);
#pragma unroll
        for (int it = 0; it < 4; it++) { v[it] = ldsr<u32x4>(a0 + it * 4096); n[it] = ldsr<u32x2>(a1 + it * 4096); }
#pragma unroll
        for (int it = 0; it < 4; it++) {
            const u32 s0 = __builtin_amdgcn_alignbit(v[it][1], v[it][0], 16), s1 = __builtin_amdgcn_alignbit(v[it][2], v[it][1], 16),
                      s2 = __builtin_amdgcn_alignbit(v[it][3], v[it][2], 16), s3 = __builtin_amdgcn_alignbit(n[it][0], v[it][3], 16),
                      s4 = __builtin_amdgcn_alignbit(n[it][1], n[it][0], 16);
            ldsw<u32x4>(a0 + it * 4096 + 64, u32x4{s0, s1, s2, s3});
            ldsw<u32x4>(a0 + it * 4096 + 128, u32x4{v[it][1], v[it][2], v[it][3], n[it][0]});
            ldsw<u32x4>(a0 + it * 4096 + 192, u32x4{s1, s2, s3, s4});
        }
    }
    STAMP(5);
    {
        const u32 a = LU + (256 * ((w + ia) & 7) + 4 * l) * 2;
#pragma unroll
        for (int c2 = 0; c2 < 4; c2++) {
            ldsw<u32x2>(a + 2 * c2 * 4096, u32x2{__builtin_amdgcn_perm(ur[1][c2], ur[0][c2], 0x05040100u), __builtin_amdgcn_perm(ur[3][c2], ur[2][c2], 0x05040100u)});
            ldsw<u32x2>(a + (2 * c2 + 1) * 4096, u32x2{__builtin_amdgcn_perm(ur[1][c2], ur[0][c2], 0x07060302u), __builtin_amdgcn_perm(ur[3][c2], ur[2][c2], 0x07060302u)});
        }
    }
    STAMP(3);
    bar();
    STAMP(10);
    const int rr = l & 15;
    const u32 e0 = 2047 - 16 - rr + 8 * g, sh = e0 & 3;
    auto half = [&](auto HI) {
        constexpr bool hi = decltype(HI)::value;
        constexpr int NF = hi ? 8 : 4, T0 = hi ? 4 : 0, NRD = 3 * NF;
        f32x4 y[4] = {};
        u32 ka0 = KB + kaddr(e0 - sh) + sh * 64 - (NF - 1) * 2048, ka1 = KB + kaddr(e0 - sh + 4) + sh * 64 - (NF - 1) * 2048;
        u32 ua = LU + w * 4096 + 32 * rr + 16 * g - 32;
        u32 mk[4];
#pragma unroll
        for (int i = 0; i < 4; i++) mk[i] = (2 * i > 16 + rr - 8 * g ? 0u : 0xffffu) | (2 * i + 1 > 16 + rr - 8 * g ? 0u : 0xffff0000u);
        int lim = 2 * rr + g - 2;
        u32x2 A[2][2 * NF]; u32x4 B[2][NF];
        auto rdc = [&](int s, int n) {
            const int m = n / 3, k = n % 3;
            if (k == 0) B[s][m] = ldsr<u32x4>(ua + m * 512);
            else if (k == 1) A[s][2 * (NF - 1 - m)] = ldsr<u32x2>(ka0 + m * 2048);
            else A[s][2 * (NF - 1 - m) + 1] = ldsr<u32x2>(ka1 + m * 2048);
        };
        auto group = [&](int s, bool next) {
            A[s][0] &= u32x2{mk[0], mk[1]}; A[s][1] &= u32x2{mk[2], mk[3]};
            if (lim < 0) B[s][0] = u32x4{0, 0, 0, 0};
            ka0 -= 256; ka1 -= 256; ua -= 64; lim -= 4;
            mk[0] = mk[1] = mk[2] = mk[3] = 0xffffffffu;
            asm volatile("" : "+v"(ka0), "+v"(ka1), "+v"(ua));
            int n = 0;
#pragma unroll
            for (int m = NF - 1; m >= 0; m--)
#pragma unroll
                for (int ct = NF - 1 - m; ct >= 0 && ct + m >= T0; ct--) {
                    SB;
                    y[ct + m - T0] = mfma16(frag(A[s][2 * m][0], A[s][2 * m][1], A[s][2 * m + 1][0], A[s][2 * m + 1][1]), __builtin_bit_cast(s16x8, B[s][ct]), y[ct + m - T0]);
                    SB;
                    if (next && n < NRD) rdc(s ^ 1, n);
                    n++;
                    if (!hi && next && (n == 1 || n == 6)) rdc(s ^ 1, n++);
                }
        };
        asm volatile("" : "+v"(ka0), "+v"(ka1), "+v"(ua));
#pragma unroll
        for (int n = 0; n < NRD; n++) rdc(0, n);
#pragma unroll 1
        for (int gg = 0; gg < 3; gg++) { group(0, true); group(1, true); }
        group(0, true); group(1, false);
#pragma unroll
        for (int ct = 0; ct < 4; ct++) ldsw<u32x2>(LU + w * 4096 + (T0 + ct) * 512 + rr * 32 + g * 8, u32x2{pk(y[ct][0], y[ct][1]), pk(y[ct][2], y[ct][3])});
        if (hi) STAMP(14); else STAMP(16);
        bar();
        if (hi) STAMP(15); else STAMP(17);
        if (!hi) return;
        u32 v[8];
#pragma unroll
        for (int c2 = 0; c2 < 8; c2++) v[c2] = ldsr<u32>(LU + c2 * 4096 + T0 * 512 + tid * 4);
        const u32 op = (T0 * 256 + 2 * tid) * 1280 + 8 * ia;
        out16(out, op, u32x4{__builtin_amdgcn_perm(v[1], v[0], 0x05040100u), __builtin_amdgcn_perm(v[3], v[2], 0x05040100u),
                            __builtin_amdgcn_perm(v[5], v[4], 0x05040100u), __builtin_amdgcn_perm(v[7], v[6], 0x05040100u)});
        out16(out, op + 1280, u32x4{__builtin_amdgcn_perm(v[1], v[0], 0x07060302u), __builtin_amdgcn_perm(v[3], v[2], 0x07060302u),
                                     __builtin_amdgcn_perm(v[5], v[4], 0x07060302u), __builtin_amdgcn_perm(v[7], v[6], 0x07060302u)});
    };
    half(Flag<true>{});
    STAMP(11);
    half(Flag<false>{});
    STAMP(12);
    load_wo(wo, (sbid() >> 3) & 7, w, 20, 8, l);
    waitv<0>();
    STAMP(7);
    return tag;
}

__global__ void __launch_bounds__(512, 2)
fused_kernel(const u16* __restrict__ x, const u16* __restrict__ W, const u16* __restrict__ wphi, const u16* __restrict__ wo,
             u16* __restrict__ out, char* __restrict__ ws, const u16* __restrict__ u, const u16* __restrict__ Are,
             const u16* __restrict__ Aim, const u16* __restrict__ Bre, const u16* __restrict__ Bim, const u16* __restrict__ Cre,
             const u16* __restrict__ Cim, const u16* __restrict__ logdt, const u16* __restrict__ Dskip) {
    __shared__ __attribute__((aligned(16))) char smem[LDS_BYTES];
    asm volatile("" ::"s"((u32)(size_t)(LAS char*)smem));
    u32 pfd = 0;
    if (threadIdx.x < 12) {
        u64 pc; asm volatile("s_getpc_b64 %0" : "=s"(pc));
        asm volatile("global_load_dword %0, %1, %2" : "+v"(pfd) : "v"(threadIdx.x * 128), "s"(pc + min((sbid() >> 3) * 1536, CODE_PF_BYTES - 1536)));
    }
    asm volatile("" ::"s"(x), "s"(W), "s"(wphi), "s"(wo), "s"(out), "s"(ws), "s"(u), "s"(Are), "s"(Aim), "s"(Bre), "s"(Bim), "s"(Cre), "s"(Cim),
                 "s"(logdt), "s"(Dskip));
    const Role R = role_of(sbid());
    u64 tag;
    if (R.is_a) prologue_a(R.ia, Are, Aim, Bre, Bim, Cre, Cim, logdt, Dskip);
    else if (__builtin_amdgcn_readfirstlane(threadIdx.x) >= 384) loader_b<true>(x, W, wo, R.r, R.c, __builtin_amdgcn_readfirstlane(threadIdx.x >> 6) - 6, pfd);
    u32 sel = R.nb;
    asm volatile("" : "+s"(sel));
    if ((sbid() >> 3) >= sel) {
        tag = path_a(u, wo, out, ws, R.ia, pfd STAMP_PASS);
    } else {
        tag = path_b(x, W, wphi, wo, out, ws, R.r, R.c, pfd STAMP_PASS);
    }
    out_proj(out, ws, tag, R.is_a ? R.ia : -1 STAMP_PASS);
}

extern "C" void kernel_launch(void* const* d_in, const int* in_sizes, int n_in, void* d_out, int out_size, void* d_ws, size_t ws_size,
                              hipStream_t stream) {
    const u16* const* in = (const u16* const*)d_in;
    fused_kernel<<<256, 512, 0, stream>>>(in[9], in[10], in[11], in[12], (u16*)d_out, (char*)d_ws, in[0], in[1], in[2], in[3], in[4], in[5],
                                          in[6], in[7], in[8]);
}
```

```cpp
#include <hip/hip_runtime.h>
#include <hip/hip_bf16.h>

typedef unsigned short u16;
typedef unsigned u32;
typedef unsigned long long u64;
typedef __attribute__((ext_vector_type(8))) short s16x8;
typedef __attribute__((ext_vector_type(4))) short s16x4;
typedef __attribute__((ext_vector_type(16))) float f32x16;
typedef __attribute__((ext_vector_type(4))) float f32x4;
typedef __attribute__((ext_vector_type(2))) float f32x2;
typedef __attribute__((ext_vector_type(4))) u32 u32x4;
typedef __attribute__((ext_vector_type(2))) u32 u32x2;
typedef __attribute__((ext_vector_type(2))) __bf16 bf16x2;
#define LAS __attribute__((address_space(3)))
#define GAS __attribute__((address_space(1)))
#define DI __device__ __forceinline__
#define SB __builtin_amdgcn_sched_barrier(0)
DI unsigned sbid() { return __builtin_amdgcn_workgroup_id_x(); }

#define PSLEEP 4
#define STW 0
#define YSLEEP 12
#define AUXA 0
#define AUXB 0
#define RMAP 1
#define YLATE 0
#define UISS 1
#define UPOS 1
#define WOPOS 0
#define YMODE 0
constexpr int LDS_BYTES = 163840;
constexpr u32 WS_EPOCH = 0, WS_ACK = 4096, WS_F1 = 65536, WS_F2 = 131072, WS_PART = 524288, WS_O = 2u << 20, WS_END = 5u << 20;
constexpr u32 WS_STAMP = 16u << 20;
constexpr u32 kSpinCap = 1u << 18;
constexpr u32 CODE_PF_BYTES = 44032;

DI u32 pk(float a, float b) { f32x2 v = {a, b}; return __builtin_bit_cast(u32, __builtin_convertvector(v, bf16x2)); }
DI float lo2f(u32 x) { return __uint_as_float(x << 16); }
DI float hi2f(u32 x) { return __uint_as_float(x & 0xffff0000u); }
DI float b2f(u16 h) { return __uint_as_float((u32)h << 16); }
DI s16x4 trd(u32 a) { return __builtin_amdgcn_ds_read_tr16_b64_v4i16((LAS s16x4*)(size_t)a); }
DI s16x8 cat(s16x4 a, s16x4 b) { return __builtin_shufflevector(a, b, 0, 1, 2, 3, 4, 5, 6, 7); }
DI s16x8 frag(u32 a, u32 b, u32 c, u32 d) { u32x4 v = {a, b, c, d}; return __builtin_bit_cast(s16x8, v); }
template <class T> DI T ldsr(u32 a) { return *(LAS T*)(size_t)a; }
template <class T> DI void ldsw(u32 a, T v) { *(LAS T*)(size_t)a = v; }
template <int N> DI void waitv() { asm volatile("s_waitcnt vmcnt(%0)" ::"i"(N) : "memory"); }
DI void bar() { asm volatile("s_waitcnt lgkmcnt(0)\n\ts_barrier" ::: "memory"); }
DI f32x16 mfma32(s16x8 a, s16x8 b, f32x16 c) { return __builtin_amdgcn_mfma_f32_32x32x16_bf16(a, b, c, 0, 0, 0); }
DI f32x4 mfma16(s16x8 a, s16x8 b, f32x4 c) { return __builtin_amdgcn_mfma_f32_16x16x32_bf16(a, b, c, 0, 0, 0); }
DI float max3(float a, float b, float c) { float r; asm("v_max3_f32 %0, %1, %2, %3" : "=v"(r) : "v"(a), "v"(b), "v"(c)); return r; }
DI float min3(float a, float b, float c) { float r; asm("v_min3_f32 %0, %1, %2, %3" : "=v"(r) : "v"(a), "v"(b), "v"(c)); return r; }
DI float dot2(u32 x, u32 sel, float c) { return __builtin_amdgcn_fdot2_f32_bf16(__builtin_bit_cast(bf16x2, sel), __builtin_bit_cast(bf16x2, x), c, false); }
template <class R> DI void st128(u32x4 v, R rs, u32 voff, u32 soff, int) { __builtin_amdgcn_raw_buffer_store_b128(v, rs, voff + soff, 0, 16); }
DI void out16(u16* out, u32 off, u32x4 v) {
    __builtin_amdgcn_raw_buffer_store_b128(v, __builtin_amdgcn_make_buffer_rsrc((void*)out, 0, 2048 * 2560, 0x00020000), off * 2, 0, AUXA);
}
DI void out8(u16* out, u32 off, u32x2 v) {
    __builtin_amdgcn_raw_buffer_store_b64(v, __builtin_amdgcn_make_buffer_rsrc((void*)out, 0, 2048 * 2560, 0x00020000), off * 2, 0, AUXB);
}
DI void dma(u32 m0v, u32 voff, const void* sbase) {
    asm volatile("s_mov_b32 m0, %0\n\ts_nop 0\n\tglobal_load_lds_dwordx4 %1, %2" ::"s"(m0v), "v"(voff), "s"(sbase) : "memory");
}
DI void dma_m(u32 m0v, u32 voff, const void* sbase, u32 mlo, u32 mhi) {
    asm volatile("s_mov_b32 exec_lo, %3\n\ts_mov_b32 exec_hi, %4\n\ts_mov_b32 m0, %0\n\ts_nop 0\n\tglobal_load_lds_dwordx4 %1, %2\n\ts_mov_b64 exec, -1"
                 ::"s"(m0v), "v"(voff), "s"(sbase), "i"(mlo), "i"(mhi) : "memory");
}
#define STAMP(i)
#define STAMP_ARG
#define STAMP_PASS

DI void post_flag(char* fl, u64 tag, int tid, int n, int me) {
    if (tid < n) __hip_atomic_store((GAS u64*)(fl + tid * 256 + me * 8), tag, __ATOMIC_RELAXED, __HIP_MEMORY_SCOPE_AGENT);
}
DI void wait_flags(const char* fl, u64 tag, int l, int n) {
    const GAS u64* p = (const GAS u64*)(fl + (l < n ? l : 0) * 8);
    u64 v0 = __hip_atomic_load(p, __ATOMIC_RELAXED, __HIP_MEMORY_SCOPE_AGENT);
#pragma unroll 1
    for (u32 s = 0; s < kSpinCap; s++) {
        __builtin_amdgcn_s_sleep(PSLEEP);
        const u64 v1 = __hip_atomic_load(p, __ATOMIC_RELAXED, __HIP_MEMORY_SCOPE_AGENT);
        if (__all(v0 == tag)) break;
        v0 = v1;
    }
}

template <class F> DI void features(const float (&v)[20], const s16x8 (&wf)[2][3], u32 (&phi)[20], F filler) {
    u32 b[10];
#pragma unroll
    for (int i = 0; i < 10; i++) b[i] = pk(v[2 * i], v[2 * i + 1]);
    const s16x8 B0 = frag(b[0], b[1], b[2], b[3]), B1 = frag(b[4], b[5], b[6], b[7]), B2 = frag(b[8], b[9], 0, 0);
    f32x16 p0 = {}, p1 = {};
    p0 = mfma32(wf[0][0], B0, p0); p1 = mfma32(wf[1][0], B0, p1);
    p0 = mfma32(wf[0][1], B1, p0); p1 = mfma32(wf[1][1], B1, p1);
    p0 = mfma32(wf[0][2], B2, p0); p1 = mfma32(wf[1][2], B2, p1);
    SB; filler(); SB;
    asm volatile("s_nop 11" : "+v"(p0), "+v"(p1));
    float p[20];
#pragma unroll
    for (int i = 0; i < 20; i++) p[i] = i < 16 ? p0[i] : p1[i - 16];
    float mxa = max3(p[0], p[1], p[2]), mna = min3(p[0], p[1], p[2]), mxb = max3(p[3], p[4], p[5]), mnb = min3(p[3], p[4], p[5]);
#pragma unroll
    for (int i = 6; i < 18; i += 4) {
        mxa = max3(mxa, p[i], p[i + 1]); mna = min3(mna, p[i], p[i + 1]);
        mxb = max3(mxb, p[i + 2], p[i + 3]); mnb = min3(mnb, p[i + 2], p[i + 3]);
    }
    mxa = max3(mxa, p[18], p[19]); mna = min3(mna, p[18], p[19]);
    const float mx = max3(mxa, mxb, mxb), mn = min3(mna, mnb, mnb);
    const float L2E = 1.4426950408889634f;
    const float cx = -mx * L2E, cn = mn * L2E;
    float e[40], s1, s2;
#pragma unroll
    for (int i = 0; i < 20; i++) {
        e[i] = __builtin_amdgcn_exp2f(__builtin_fmaf(p[i], L2E, cx));
        e[20 + i] = __builtin_amdgcn_exp2f(__builtin_fmaf(p[i], -L2E, cn));
        if (i == 0) { s1 = e[0]; s2 = e[20]; } else { s1 += e[i]; s2 += e[20 + i]; }
    }
    const float r1 = __builtin_amdgcn_rcpf(s1), r2 = __builtin_amdgcn_rcpf(s2);
#pragma unroll
    for (int i = 0; i < 10; i++) { phi[i] = pk(e[2 * i] * r1, e[2 * i + 1] * r1); phi[10 + i] = pk(e[20 + 2 * i] * r2, e[21 + 2 * i] * r2); }
}

constexpr int WBLK = 960, WSLOT = 8 * WBLK, XSLOT = 24576;
constexpr int LW = XSLOT, LIMG = 102400, LMISC = LIMG + 2 * XSLOT, LZERO = LMISC + 3200, LTAB = LZERO + 64, TROW = 56, THEAD = 40 * TROW;
DI int xslot(int j) { return j % 3 == 0 ? 0 : LIMG + (j % 3 - 1) * XSLOT; }
static_assert(LTAB + 80 * TROW <= LDS_BYTES, "LDS map");

template <bool V> struct Flag { static constexpr bool value = V; };

struct Role { bool is_a; int r, c, ia; u32 nb; };
DI Role role_of(u32 bid) {
    const int xcd = bid & 7, j = bid >> 3;
    Role R;
    const int e = xcd & 1, m = xcd >> 1, nb = e ? 20 : 24, cq = e ? (j * 52) >> 8 : (j * 43) >> 8;
    R.r = __builtin_amdgcn_readfirstlane(e ? 6 + j - 5 * cq : j - 6 * cq); R.c = __builtin_amdgcn_readfirstlane(4 * m + cq);
    R.ia = __builtin_amdgcn_readfirstlane((RMAP == 1 ? (e ? 32 + 12 * m : 8 * m) : 20 * m + (e ? 8 : 0)) + j - nb);
    R.is_a = j >= nb; R.nb = nb;
    return R;
}

DI void load_wo(const u16* wo, int cb, int wi, int end, int nw, int l) {
    u32 off[5];
#pragma unroll
    for (int i = 0; i < 5; i++) { const u32 idx = 64 * i + l, row = (idx * 6554u) >> 16; off[i] = row * 1120 + idx * 16; }
#pragma unroll 1
    for (int gq = wi; gq < end; gq += nw) {
        const char* gp = (const char*)wo + cb * 160 + gq * (32 * 1280);
#pragma unroll
        for (int i = 0; i < 5; i++) dma(gq * 5120 + i * 1024, off[i], gp);
    }
}

DI void store_rows(u16* __restrict__ out, int ia, int t0, int tt) {
    u32x2 v[8];
#pragma unroll
    for (int c2 = 0; c2 < 8; c2++) v[c2] = ldsr<u32x2>(131072 + c2 * 4096 + t0 * 2 + tt * 8);
    const u32 op = (t0 + 4 * tt) * 1280 + 8 * ia;
#pragma unroll
    for (int i = 0; i < 4; i++) {
        const u32 sel = (i & 1) ? 0x07060302u : 0x05040100u;
        out16(out, op + i * 1280, u32x4{__builtin_amdgcn_perm(v[1][i >> 1], v[0][i >> 1], sel), __builtin_amdgcn_perm(v[3][i >> 1], v[2][i >> 1], sel),
                                        __builtin_amdgcn_perm(v[5][i >> 1], v[4][i >> 1], sel), __builtin_amdgcn_perm(v[7][i >> 1], v[6][i >> 1], sel)});
    }
}

DI void out_proj(u16* __restrict__ out, char* __restrict__ ws, u64 tag, int ia STAMP_ARG) {
    const int bid = sbid(), tid = threadIdx.x, l = tid & 63, w = __builtin_amdgcn_readfirstlane(tid >> 6);
    const int t = 4 * (bid & 7) + (bid >> 6), cb = (bid >> 3) & 7, rs = (t * 43) >> 7, sub = t - 3 * rs;
    bar();
    if (w >= 4) {
        if (YLATE && ia >= 0) {
            bar();
            __builtin_amdgcn_s_sleep(YSLEEP);
            store_rows(out, ia, 0, tid - 256);
        }
        return;
    }
    constexpr int NPRE = 6;
    const auto rsw = __builtin_amdgcn_make_buffer_rsrc((void*)ws, 0, WS_END, 0x00020000);
    const int g = l >> 4, q = (l >> 2) & 3, p = l & 3, hp = g & 1, P = g >> 1;
    u32 b1 = (20 * hp + 8 * P + q) * 160 + p * 8, b2 = (80 * P + 20 * hp + 16 + q) * 160 + p * 8;
    u32 b1h = b1 + 51200, b2h = b2 + 51200;
    asm volatile("" : "+v"(b1), "+v"(b2), "+v"(b1h), "+v"(b2h));
    s16x4 wfr[NPRE][10];
    auto rdw = [&](int ks, int n) {
        const int G = ks / 5, i = ks % 5, tt = n >> 1, rd = n & 1;
        const u32 so = (G & 1) * 25600;
        wfr[ks % NPRE][n] = i < 4 ? trd((G >> 1 ? b1h : b1) + so + (40 * i + 4 * rd) * 160 + 32 * tt) : trd((G >> 1 ? b2h : b2) + so + 40 * rd * 160 + 32 * tt);
    };
#pragma unroll
    for (int ks = 0; ks < NPRE; ks++)
#pragma unroll
        for (int n = 0; n < 10; n++) rdw(ks, n);
    if (w == 0) wait_flags(ws + WS_F2 + (rs * 24 + sub * 8 + cb) * 256, tag, l, 16);
    bar();
    STAMP(8);
    const int ti = 16 * (w & 1) + (l & 15);
    const u32 ob = WS_O + (rs * 96 + 2 * sub + (w >> 1)) * 2560;
    const u32 v1 = P * 1024 + (hp * 32 + ti) * 16, v2 = 2048 + 2 * P * 15360 + (hp * 32 + ti) * 8;
    u32x4 of[16]; u32x2 ot[8];
    auto ldo = [&](int n) {
        const int G = n / 6, i = n % 6;
        if (i < 4) of[4 * G + i] = __builtin_amdgcn_raw_buffer_load_b128(rsw, v1, ob + (4 * G + i) * 15360, 16);
        else ot[2 * G + i - 4] = __builtin_amdgcn_raw_buffer_load_b64(rsw, v2, ob + (4 * G + i - 4) * 15360, 16);
    };
#pragma unroll
    for (int n = 0; n < 10; n++) ldo(n);
    if (bid == 0 && tid == 0) __hip_atomic_store((GAS u64*)(ws + WS_EPOCH), tag, __ATOMIC_RELAXED, __HIP_MEMORY_SCOPE_AGENT);
    f32x4 oc[5] = {};
#pragma unroll
    for (int ks = 0; ks < 20; ks++) {
        const int G = ks / 5, i = ks % 5;
        const s16x8 B = i < 4 ? __builtin_bit_cast(s16x8, of[4 * G + i]) : frag(ot[2 * G][0], ot[2 * G][1], ot[2 * G + 1][0], ot[2 * G + 1][1]);
#pragma unroll
        for (int tt = 0; tt < 5; tt++) {
            SB;
            oc[tt] = mfma16(cat(wfr[ks % NPRE][2 * tt], wfr[ks % NPRE][2 * tt + 1]), B, oc[tt]);
            SB;
            if (ks + NPRE < 20) { rdw(ks + NPRE, 2 * tt); rdw(ks + NPRE, 2 * tt + 1); }
            if ((tt == 0 || tt == 2) && 10 + 2 * ks + (tt >> 1) < 24) ldo(10 + 2 * ks + (tt >> 1));
        }
    }
    SB;
    const u32 op = (64 * t + 16 * w + (l & 15)) * 1280 + 640 + 80 * cb + 4 * g;
#pragma unroll
    for (int tt = 0; tt < 5; tt++) out8(out, op + 16 * tt, u32x2{pk(oc[tt][0], oc[tt][1]), pk(oc[tt][2], oc[tt][3])});
    STAMP(9);
}

template <bool FIRST>
DI void loader_b(const u16* __restrict__ x, const u16* __restrict__ W, const u16* __restrict__ wo, int r, int c, int lw, u32& pfd) {
    const int l = threadIdx.x & 63;
    const int ph = (((r + 4 * c) & 15) * 10) >> 4;
    const int pl = l >> 4, idx = l & 15, q3 = (idx * 11) >> 5;
    const int q = pl < 3 ? idx >> 2 : q3 & 3, sg = pl < 3 ? pl : idx - 3 * q3, ch = pl < 3 ? (idx & 3) + ((idx >> 1) & 1) : 2;
    const u32 wl = q * 3840 + sg * 1280 + ch * 16 + (sg == 0 ? ((c & 1) ? 0 : 160) : 80);
    const u32 xo0 = (l >> 3) * 1280 + (((l & 7) ^ (l >> 4)) << 4), xo1 = ((l >> 3) + 8) * 1280 + (((l & 7) ^ ((l >> 4) | 4)) << 4);
    u32 xr[2][3];
#pragma unroll
    for (int i = 0; i < 6; i++) { const int row = 192 * r + 96 * (i / 3) + 48 * lw + 16 * (i % 3); xr[i / 3][i % 3] = (row < 2048 ? row : row - 64) * 1280; }
    const char* wb = (const char*)W + c * 80 - 80 + lw * (16 * 3840);
    auto ld_x = [&](int hx) {
        const int jx = hx >> 1, hh = hx & 1;
        int jr = jx + ph; if (jr >= 10) jr -= 10;
        const char* g = (const char*)x + jr * 128;
        const u32 d = xslot(jx) + (96 * hh + 48 * lw) * 128;
#pragma unroll
        for (int i = 0; i < 3; i++) { dma(d + i * 2048, xo0, g + xr[hh][i]); dma(d + i * 2048 + 1024, xo1, g + xr[hh][i]); }
    };
    auto ld_w = [&](int i) {
        int ir = i + 2 * ph; if (ir >= 20) ir -= 20;
        const char* g = wb + ir * (32 * 3840);
        const u32 d = LW + (i & 3) * WSLOT + lw * 4 * WBLK;
#pragma unroll
        for (int b = 0; b < 4; b++) dma_m(d + b * WBLK, wl, g + b * (4 * 3840), 0xffffffffu, 0x0fffffffu);
    };
    if (FIRST) { ld_w(0); ld_x(0); ld_x(1); return; }
    ld_w(1); ld_x(2); ld_x(3);
    waitv<16>();
    bar();
    ld_w(2); ld_w(3); ld_x(4);
#pragma unroll
    for (int i = 0; i < 19; i++) {
        if (i == 0 || i == 2) waitv<26>(); else if (i <= 15) waitv<20>(); else if (i == 16) waitv<14>(); else if (i == 17) waitv<4>(); else waitv<2>();
        bar();
        if (i + 5 < 20) ld_x(i + 5);
        if (i + 4 < 20) ld_w(i + 4);
        if (i == 17) {
            const u32 ln = __builtin_amdgcn_readfirstlane(min((sbid() >> 3) * 256 + lw * 64, 6208u)) + l;
            asm volatile("global_load_dword %0, %1, %2\n\tglobal_load_dword %0, %1, %3" : "+v"(pfd) : "v"(ln * 128), "s"(wo), "s"((const char*)wo + 128 * 128));
        }
    }
}

DI u64 path_b(const u16* __restrict__ x, const u16* __restrict__ W, const u16* __restrict__ wphi, const u16* __restrict__ wo,
               u16* __restrict__ out, char* __restrict__ ws, int r, int c, u32& pfd STAMP_ARG) {
    const int tid = threadIdx.x, l = tid & 63;
    const int w = __builtin_amdgcn_readfirstlane(tid >> 6);
    const auto rsw = __builtin_amdgcn_make_buffer_rsrc((void*)ws, 0, WS_END, 0x00020000);
    const int g = l >> 4, q = (l >> 2) & 3, p = l & 3, h = l >> 5;
    const int rq = 2 * (g & 1) + (p >> 1), hk = g >> 1;
    const bool same = hk == (p & 1);
    u64 tag;
    u32 phiq[20];
    f32x16 acc[4] = {};
    s16x8 wfq[2][3];
    if (w >= 6) {
        loader_b<false>(x, W, wo, r, c, w - 6, pfd);
        asm volatile("s_load_dwordx2 %0, %1, 0\n\ts_waitcnt lgkmcnt(0)" : "=&s"(tag) : "s"(ws + WS_EPOCH) : "memory");
        if (sbid() == 0) wait_flags(ws + WS_ACK + (w - 6) * 320, tag + 1, l, 40);
    } else {
        if (w < 4) dma_m(LMISC + w * 800, l * 16, (const char*)wphi + ((c ^ (w >> 1)) * 2 + (w & 1)) * 800, 0xffffffffu, 0x3ffffu);
        if (tid < 2) ldsw<u64>(LZERO + tid * 8, 0ull);
        u32 bA = LW + (g >> 1) * 2 * WBLK + q * 64 + (p & 1) * 32 + (g & 1) * 16 + (p >> 1) * 8;
        u32 bD = LW + (g >> 1) * 2 * WBLK + 768 + (3 * q + 2 * (g & 1) + (p >> 1)) * 16 + (p & 1) * 8;
        asm volatile("" : "+v"(bA), "+v"(bD));
        const int trow = 32 * w + (l & 31);
        s16x8 wfk[2][3];
        auto load_wf = [&](s16x8 (&wf)[2][3], u32 base) {
#pragma unroll
            for (int to = 0; to < 2; to++)
#pragma unroll
                for (int s = 0; s < 3; s++) {
                    s16x4 t[2];
#pragma unroll
                    for (int rd = 0; rd < 2; rd++) {
                        const int mu = 2 * s + rd;
                        if (mu < 5) {
                            int dq = mu + hk; if (mu == 4) dq = hk ? 0 : 4;
                            const bool ok = same && (to == 0 || rq == 0);
                            t[rd] = trd(ok ? base + hk * 800 + (4 * dq + q) * 40 + (4 * to + rq) * 8 : LZERO);
                        } else t[rd] = s16x4{0, 0, 0, 0};
                    }
                    wf[to][s] = cat(t[0], t[1]);
                }
        };
        asm volatile("s_load_dwordx2 %0, %1, 0\n\ts_waitcnt vmcnt(0) lgkmcnt(0)" : "=&s"(tag) : "s"(ws + WS_EPOCH), "v"(pfd) : "memory");
        bar();
        STAMP(1);
        const u32 sw = ((trow & 2) << 1) | ((trow & 4) >> 1) | (trow & 1);
        const u32 rowa = LIMG + h * XSLOT + trow * 128;
        auto gemm = [&](auto HV) {
            constexpr bool hv = decltype(HV)::value;
            constexpr int NB = hv ? 3 : 1, NM = hv ? 6 : 3, NR = hv ? 11 : 7;
            f32x16 vc[3] = {};
            u32 aX[4], aXh[4];
            const int tx = hv ? 32 * (w - 2) + (l & 31) : trow;
#pragma unroll
            for (int k = 0; k < 4; k++) {
                aX[k] = tx * 128 + (((2 * k + h) ^ ((tx >> 1) & 7)) << 4); aXh[k] = aX[k] + LIMG;
                asm volatile("" : "+v"(aX[k]), "+v"(aXh[k]));
            }
            s16x4 fa[2][8]; s16x8 fb[2][NB];
            auto rd = [&](int s, int i, int ks, int n) {
                const int nb = hv ? (n == 0 ? 1 : n == 9 ? 0 : n == 10 ? 2 : -1) : (n == 0 ? 0 : -1);
                if (nb >= 0) {
                    const int sl = (i >> 1) % 3, k = (i & 1) * 2 + ks;
                    fb[s][nb] = sl == 0 ? ldsr<s16x8>(aX[k] + nb * 8192) : ldsr<s16x8>(aXh[k] + (sl - 1) * XSLOT + nb * 8192);
                } else {
                    const int na = n <= 4 ? n - 1 : n <= 6 ? n + 1 : n - 3;
                    const u32 o = (i & 3) * WSLOT + ks * 4 * WBLK + (na & 1) * WBLK;
                    fa[s][na] = na < 6 ? trd(bA + o + (na >> 1) * 256) : trd(bD + o);
                }
            };
            auto mm = [&](int s, int t) {
                if (t < 3) { const int tt = t == 2 ? 3 : t; acc[tt] = mfma32(cat(fa[s][2 * tt], fa[s][2 * tt + 1]), fb[s][hv ? 1 : 0], acc[tt]); }
                else { const int nb = t == 3 ? 1 : t == 4 ? 0 : 2; vc[nb] = mfma32(cat(fa[s][4], fa[s][5]), fb[s][hv ? nb : 0], vc[nb]); }
            };
            auto halfstep = [&](int s, int i, int ks, bool next) {
#pragma unroll
                for (int t = 0; t < NM; t++) {
                    SB; mm(s, t); SB;
                    if (t == 0 && ks == 0 && next) bar();
                    if (next)
#pragma unroll
                        for (int n = 3 * t; n < 3 * t + 3 && n < NR; n++) rd(s ^ 1, i, ks, n);
                }
            };
#pragma unroll
            for (int n = 0; n < NR; n++) rd(0, 0, 0, n);
#pragma unroll
            for (int i = 0; i < 20; i++) {
                halfstep(0, i, 1, true);
                if (i == 1) load_wf(wfk, LMISC);
                if (i == 2) load_wf(wfq, LMISC + 1600);
                halfstep(1, i + 1, 0, i < 19);
            }
            if (hv) {
#pragma unroll
                for (int nb = 0; nb < 3; nb++)
#pragma unroll
                    for (int mu = 0; mu < 4; mu++) {
                        const int pc = 10 + mu + h;
                        ldsw<u32x2>(rowa + (nb - 1) * 8192 + (((pc >> 1) ^ sw) << 4) + (pc & 1) * 8,
                                    u32x2{pk(vc[nb][4 * mu], vc[nb][4 * mu + 1]), pk(vc[nb][4 * mu + 2], vc[nb][4 * mu + 3])});
                    }
            }
        };
        if ((w & 3) >= 2) gemm(Flag<true>{}); else gemm(Flag<false>{});
        STAMP(2);
        float kr[20]; u32 phik[20];
#pragma unroll
        for (int i = 0; i < 20; i++) kr[i] = i < 16 ? acc[1][i] : acc[3][i - 12];
        features(kr, wfk, phik, [&]() {
            const int pc = h ? 10 : 14;
            ldsw<u32x2>(rowa + (((pc >> 1) ^ sw) << 4) + (pc & 1) * 8, u32x2{pk(acc[3][8], acc[3][9]), pk(acc[3][10], acc[3][11])});
            ldsw<u32x2>(rowa + ((7 ^ sw) << 4) + 8, u32x2{0x3f80u, 0u});
        });
        const u32 keep = 192 * r + 32 * w < 2048 ? 0xffffffffu : 0u;
#pragma unroll
        for (int ch = 0; ch < 5; ch++) ldsw<u32x4>(rowa + ((ch ^ sw) << 4), u32x4{phik[4 * ch], phik[4 * ch + 1], phik[4 * ch + 2], phik[4 * ch + 3]} & keep);
        STAMP(10);
    }
    tag += 1;
    bar();
    if (w >= 6) load_wo(wo, (sbid() >> 3) & 7, w - 6, 10, 2, l);
    if (w < 4) {
        const int pcn = 4 * (g & 1) + p;
        const int pa = 10 + (pcn < 6 ? pcn : 5), pb = 5 * (w & 1) + (pcn < 5 ? pcn : 4);
        u32 aa[2], ab[2];
#pragma unroll
        for (int rd = 0; rd < 2; rd++) {
            const int row = 8 * (g >> 1) + 4 * rd + q;
            const u32 sw = ((row & 2) << 1) | ((row & 4) >> 1) | (row & 1);
            const u32 base = LIMG + (w >> 1) * XSLOT + row * 128;
            aa[rd] = base + (((pa >> 1) ^ sw) << 4) + (pa & 1) * 8;
            ab[rd] = base + (((pb >> 1) ^ sw) << 4) + (pb & 1) * 8;
            asm volatile("" : "+v"(aa[rd]), "+v"(ab[rd]));
        }
        s16x4 ta[24], tb[24];
#pragma unroll
        for (int ks = 0; ks < 12; ks++) {
            ta[2 * ks] = trd(aa[0] + ks * 2048); ta[2 * ks + 1] = trd(aa[1] + ks * 2048);
            tb[2 * ks] = trd(ab[0] + ks * 2048); tb[2 * ks + 1] = trd(ab[1] + ks * 2048);
        }
        SB;
        f32x16 kv = {};
#pragma unroll
        for (int ks = 0; ks < 12; ks++) kv = mfma32(cat(ta[2 * ks], ta[2 * ks + 1]), cat(tb[2 * ks], tb[2 * ks + 1]), kv);
        if ((l & 31) < 20) {
            const u32 lam = h * 20 + (l & 31), o = WS_PART + (c * 16 + r) * 4096 + w * 960;
            st128(u32x4{pk(kv[0], kv[1]), pk(kv[2], kv[3]), pk(kv[4], kv[5]), pk(kv[6], kv[7])}, rsw, lam * 16, o, 16);
            __builtin_amdgcn_raw_buffer_store_b64(u32x2{pk(kv[8], kv[9]), pk(kv[10], kv[11])}, rsw, 640 + lam * 8, o, 16);
        }
    }
    STAMP(11);
    if (w < 6) {
        float qr[20];
#pragma unroll
        for (int i = 0; i < 20; i++) qr[i] = i < 16 ? acc[0][i] : acc[3][i - 16];
        features(qr, wfq, phiq, []() {});
#pragma unroll
        for (int i = 0; i < 20; i++) asm volatile("" : "+v"(phiq[i]));
        STAMP(17);
        waitv<0>();
    }
    bar();
    post_flag(ws + WS_F1 + c * 4096, tag, tid, 11, r);
    STAMP(3);
    if (w >= 6) load_wo(wo, (sbid() >> 3) & 7, w + 4, 20, 2, l);
    if (tid < 80) ldsw<u64>(LTAB + tid * TROW + 48, 0ull);
    if (w == 0) wait_flags(ws + WS_F1 + (c ^ 1) * 4096 + r * 256, tag, l, 11);
    bar();
    STAMP(5);
    if (w < 4) {
        const int t = tid < 240 ? tid : 239;
        const int un = (t * 1093) >> 16, it = t - un * 60;
        u32x4 pv[11];
#pragma unroll
        for (int i = 0; i < 11; i++) pv[i] = __builtin_amdgcn_raw_buffer_load_b128(rsw, t * 16, WS_PART + (c ^ 1) * 65536 + i * 4096, 16);
        float s[8] = {};
        u32 slo = 0x00003f80u, shi = 0x3f800000u;
        asm volatile("" : "+s"(slo), "+s"(shi));
#pragma unroll
        for (int i = 0; i < 11; i++)
#pragma unroll
            for (int e = 0; e < 4; e++) { s[2 * e] = dot2(pv[i][e], slo, s[2 * e]); s[2 * e + 1] = dot2(pv[i][e], shi, s[2 * e + 1]); }
        const int lam = it < 40 ? it : 2 * (it - 40);
        const int hh = lam >= 20, f = lam - 20 * hh;
        const u32 a0 = LTAB + (un >> 1) * THEAD + ((un & 1) * 20 + f) * TROW + hh * 8 + (it < 40 ? 0 : 32);
        ldsw<u32x2>(a0, u32x2{pk(s[0], s[1]), pk(s[2], s[3])});
        ldsw<u32x2>(a0 + (it < 40 ? 16 : TROW), u32x2{pk(s[4], s[5]), pk(s[6], s[7])});
    }
    bar();
    STAMP(6);
    if (w < 6 && 192 * r + 32 * w < 2048) {
        const u32 tb = LTAB + hk * THEAD + q * TROW;
        u32 t0 = tb + (same ? rq : 6) * 8, t1 = tb + (same && rq < 2 ? 4 + rq : 6) * 8;
        asm volatile("" : "+v"(t0), "+v"(t1));
        f32x16 o0 = {}, o1 = {};
#pragma unroll
        for (int s = 0; s < 5; s++) {
            const s16x8 B = frag(phiq[4 * s], phiq[4 * s + 1], phiq[4 * s + 2], phiq[4 * s + 3]);
            o0 = mfma32(cat(trd(t0 + 8 * s * TROW), trd(t0 + (8 * s + 4) * TROW)), B, o0);
            o1 = mfma32(cat(trd(t1 + 8 * s * TROW), trd(t1 + (8 * s + 4) * TROW)), B, o1);
        }
        const float rden = __builtin_amdgcn_rcpf(o1[4] + 1e-6f);
        u32 ob[10];
#pragma unroll
        for (int i = 0; i < 10; i++) ob[i] = i < 8 ? pk(o0[2 * i] * rden, o0[2 * i + 1] * rden) : pk(o1[2 * i - 16] * rden, o1[2 * i - 15] * rden);
        const u32 o = WS_O + ((r * 16 + (c ^ 1)) * 6 + w) * 2560;
        st128(u32x4{ob[0], ob[1], ob[2], ob[3]}, rsw, l * 16, o, 16);
        st128(u32x4{ob[4], ob[5], ob[6], ob[7]}, rsw, l * 16 + 1024, o, 16);
        __builtin_amdgcn_raw_buffer_store_b64(u32x2{ob[8], ob[9]}, rsw, l * 8 + 2048, o, 16);
    }
    STAMP(12);
    waitv<0>();
    asm volatile("" ::"v"(pfd));
    bar();
    post_flag(ws + WS_F2 + r * (24 * 256), tag, tid, 24, c);
    STAMP(7);
    return tag;
}

DI f32x2 cmul(f32x2 a, f32x2 b) {
    f32x2 d;
    asm("v_pk_mul_f32 %0, %1, %2 op_sel_hi:[0,1]\n\tv_pk_fma_f32 %0, %1, %2, %0 op_sel:[1,1,0] op_sel_hi:[1,0,1] neg_lo:[0,1,0]" : "=&v"(d) : "v"(a), "v"(b));
    return d;
}
DI f32x2 cmulc(f32x2 a, f32x2 b) {
    f32x2 d;
    asm("v_pk_mul_f32 %0, %1, %2 op_sel_hi:[0,1] neg_hi:[0,1]\n\tv_pk_fma_f32 %0, %1, %2, %0 op_sel:[1,1,0] op_sel_hi:[1,0,1]" : "=&v"(d) : "v"(a), "v"(b));
    return d;
}
DI f32x2 csq(f32x2 a) { return cmul(a, a); }
DI f32x2 cexp(float a, float b) {
    const float m = __builtin_amdgcn_exp2f(a * 1.4426950408889634f);
    const float c1 = 0.15915494309189535f, c2 = 6.4206382e-9f;
    const float n = __builtin_rintf(b * c1);
    const float f = __builtin_fmaf(b, c2, __builtin_fmaf(b, c1, -n));
    return f32x2{m * __builtin_amdgcn_cosf(f), m * __builtin_amdgcn_sinf(f)};
}
constexpr int LOWB = 7168, LQ = 8 * LOWB, QB = 8448, LU = 131072, LPRM = LQ + 8 * QB;
static_assert(LPRM + 6144 <= LU, "LDS map");
DI u32 kaddr(u32 e) { return ((e >> 5) << 8) + ((e & 31) << 1); }

DI void prologue_a(int ia, const u16* __restrict__ Are, const u16* __restrict__ Aim, const u16* __restrict__ Bre, const u16* __restrict__ Bim,
                   const u16* __restrict__ Cre, const u16* __restrict__ Cim, const u16* __restrict__ logdt, const u16* __restrict__ Dskip) {
    const int l = threadIdx.x & 63, w = __builtin_amdgcn_readfirstlane(threadIdx.x >> 6);
    const u16* src = w == 0 ? Are : w == 1 ? Aim : w == 2 ? Bre : w == 3 ? Bim : w == 4 ? Cre : w == 5 ? Cim : w == 6 ? logdt : Dskip;
    if (w < 6) { dma(w * 2048, ia * 2048 + l * 16, src); dma(w * 2048 + 1024, ia * 2048 + 1024 + l * 16, src); }
    else dma_m(w * 2048, ia * 16, src, 1u, 0u);
}

DI u64 path_a(const u16* __restrict__ u, const u16* __restrict__ wo, u16* __restrict__ out, char* __restrict__ ws, int ia, u32 pfd STAMP_ARG) {
    const int tid = threadIdx.x, l = tid & 63;
    const int w = __builtin_amdgcn_readfirstlane(tid >> 6);
    const u32 LB = w * LOWB, KB = LQ + w * QB, UB = LU + w * 4096;
    const int g = l >> 4, q = (l >> 2) & 3, p = l & 3, h = l >> 5;
    STAMP(1);
    u64 tag;
    asm volatile("s_load_dwordx2 %0, %1, 0\n\ts_waitcnt vmcnt(0) lgkmcnt(0)" : "=&s"(tag) : "s"(ws + WS_EPOCH), "v"(pfd) : "memory");
    tag += 1;
    bar();
    if (tid == 0) __hip_atomic_store((GAS u64*)(ws + WS_ACK + ia * 8), tag, __ATOMIC_RELAXED, __HIP_MEMORY_SCOPE_AGENT);
    const float ldt = b2f(ldsr<u16>(6 * 2048 + w * 2)), dsk = b2f(ldsr<u16>(7 * 2048 + w * 2));
    u32 prm[6];
#pragma unroll
    for (int i = 0; i < 6; i++) prm[i] = ldsr<u32>(i * 2048 + w * 256 + l * 4);
    bar();
    u32x4 ur[4];
    auto u_in = [&]() {
#pragma unroll
        for (int i = 0; i < 4; i++) ur[i] = *(const u32x4*)((const char*)u + (size_t)(256 * ((w + ia) & 7) + 4 * l + i) * 1280 + ia * 16);
    };
    if (UISS == 0) u_in();
    const float dt = lo2f(pk(expf(ldt), 0.f));
    STAMP(2);
    f32x16 kd = {};
    u32 rx, ry[2];
    {
        const int pc = 4 * (g & 1) + p;
        rx = LB + (8 * (g >> 1) + q) * 48 + (pc < 6 ? pc : 5) * 8;
#pragma unroll
        for (int rd = 0; rd < 2; rd++) ry[rd] = KB + (8 * (g >> 1) + 4 * rd + q) * 64 + (((pc >> 1) ^ ((2 * rd + (q >> 1)) & 3)) << 4) + (p & 1) * 8;
    }
    const u32 wx = LB + l * 48, wy = KB + l * 64, wsw = (l >> 1) & 3;
#pragma unroll
    for (int ps = 0; ps < 2; ps++) {
        float pf[6];
#pragma unroll
        for (int i = 0; i < 6; i++) pf[i] = ps ? hi2f(prm[i]) : lo2f(prm[i]);
        const float a = dt * pf[0], b = dt * pf[1];
        const f32x2 Wc = cmulc(f32x2{pf[4], -pf[5]}, f32x2{dt * pf[2], dt * pf[3]});
        const f32x2 e1 = cexp(a, b), e16 = cexp(16.f * a, 16.f * b), e256 = cexp(256.f * a, 256.f * b);
        const f32x2 e2 = csq(e1), e4 = csq(e2), e8 = csq(e4), e32 = csq(e16), e64 = csq(e32), e128 = csq(e64), e512 = csq(e256), e1024 = csq(e512);
        f32x2 X[24];
        X[0] = Wc; X[1] = cmulc(Wc, e1); X[2] = cmulc(Wc, e2); X[3] = cmulc(X[1], e2);
#pragma unroll
        for (int i = 0; i < 4; i++) X[4 + i] = cmulc(X[i], e4);
        X[8] = cmulc(Wc, e256); X[9] = cmulc(X[8], e2); X[10] = cmulc(X[8], e4);
        X[12] = X[10]; X[13] = cmulc(X[9], e4); X[14] = cmulc(X[8], e8);
        X[11] = cmulc(Wc, e512); X[16] = cmulc(X[11], e4); X[17] = cmulc(X[11], e8);
        X[15] = cmulc(X[8], e512); X[20] = cmulc(X[15], e4); X[21] = cmulc(X[15], e8);
        X[18] = cmulc(cmulc(Wc, e8), e1024); X[19] = cmulc(X[14], e1024);
        X[22] = cmulc(X[17], e1024); X[23] = cmulc(X[21], e1024);
        f32x2 Y[8];
        Y[0] = f32x2{1.f, 0.f}; Y[1] = e8; Y[2] = e16; Y[3] = cmul(e8, e16); Y[4] = e32; Y[5] = cmul(e8, e32); Y[6] = cmul(e16, e32); Y[7] = cmul(Y[3], e32);
        const f32x2 e192 = cmul(e64, e128);
#pragma unroll
        for (int ch = 0; ch < 4; ch++) {
            if (ch < 3) {
                const f32x2* Xc = X + 8 * ch;
                ldsw<u32x4>(wx + ch * 16, u32x4{pk(Xc[0].x, Xc[1].x), pk(Xc[2].x, Xc[3].x), pk(Xc[4].x, Xc[5].x), pk(Xc[6].x, Xc[7].x)});
                ldsw<u32x4>(wx + ch * 16 + 3072, u32x4{pk(Xc[0].y, Xc[1].y), pk(Xc[2].y, Xc[3].y), pk(Xc[4].y, Xc[5].y), pk(Xc[6].y, Xc[7].y)});
            }
            f32x2 Z[8];
            const f32x2 m = ch == 1 ? e64 : ch == 2 ? e128 : e192;
#pragma unroll
            for (int i = 0; i < 8; i++) Z[i] = ch == 0 ? Y[i] : i == 0 ? m : cmul(Y[i], m);
            const u32 o = wy + ((ch ^ wsw) << 4);
            ldsw<u32x4>(o, u32x4{pk(Z[0].x, Z[1].x), pk(Z[2].x, Z[3].x), pk(Z[4].x, Z[5].x), pk(Z[6].x, Z[7].x)});
            ldsw<u32x4>(o + 4096, u32x4{pk(Z[0].y, Z[1].y), pk(Z[2].y, Z[3].y), pk(Z[4].y, Z[5].y), pk(Z[6].y, Z[7].y)});
        }
        s16x4 fx[16], fy[16];
#pragma unroll
        for (int ks = 0; ks < 8; ks++) {
            fx[2 * ks] = trd(rx + ks * 768); fx[2 * ks + 1] = trd(rx + ks * 768 + 192);
            fy[2 * ks] = trd(ry[0] + ks * 1024); fy[2 * ks + 1] = trd(ry[1] + ks * 1024);
        }
        if (ps == 0) { STAMP(4); if (UISS == 1) u_in(); }
#pragma unroll
        for (int ks = 0; ks < 8; ks++) kd = mfma32(cat(fx[2 * ks], fx[2 * ks + 1]), cat(fy[2 * ks], fy[2 * ks + 1]), kd);
    }
    STAMP(6);
    constexpr int LR = 4096;
    {
        const int j = l & 31;
        ldsw<u32x2>(KB + kaddr(1052 - 8 * j - 4 * h), u32x2{pk(kd[3], kd[2]), pk(kd[1], l == 0 ? kd[0] + dsk : kd[0])});
        ldsw<u32x2>(KB + kaddr(1052 - 256 - 8 * j - 4 * h), u32x2{pk(kd[6], kd[5]), pk(kd[4], kd[4])});
        const u32 cc = pk(kd[9], kd[9]);
        ldsw<u32x4>(KB + kaddr(1048 - 512 - 256 * h - 8 * j), u32x4{cc, pk(kd[8], kd[8]), pk(kd[8], kd[7]), pk(kd[7], kd[7])});
        if (l >= 60) ldsw<u32x4>(KB + (l - 60) * 16, l == 63 ? u32x4{0u, cc & 0xffff0000u, cc, cc} : u32x4{0u, 0u, 0u, 0u});
        const u32 far = pk(kd[10], kd[11]);
        ldsw<u16>(LB + LR + (127 - 64 * h - j) * 2, (u16)far);
        ldsw<u16>(LB + LR + (95 - 64 * h - j) * 2, (u16)(far >> 16));
        if (l < 16) ldsw<u32>(LB + LR + 256 + l * 4, 0u);
        u32x4 v[3]; u32x2 n[3];
        u32 a0[3];
#pragma unroll
        for (int it = 0; it < 3; it++) {
            const u32 i = it < 2 ? 64 * it + l : 128 + (l & 3);
            a0[it] = KB + kaddr(8 * i);
            v[it] = ldsr<u32x4>(a0[it]); n[it] = ldsr<u32x2>(KB + kaddr(8 * i + 8));
        }
#pragma unroll
        for (int it = 0; it < 3; it++) {
            const u32 s0 = __builtin_amdgcn_alignbit(v[it][1], v[it][0], 16), s1 = __builtin_amdgcn_alignbit(v[it][2], v[it][1], 16),
                      s2 = __builtin_amdgcn_alignbit(v[it][3], v[it][2], 16), s3 = __builtin_amdgcn_alignbit(n[it][0], v[it][3], 16),
                      s4 = __builtin_amdgcn_alignbit(n[it][1], n[it][0], 16);
            if (it < 2 || l < 4) {
                ldsw<u32x4>(a0[it] + 64, u32x4{s0, s1, s2, s3});
                ldsw<u32x4>(a0[it] + 128, u32x4{v[it][1], v[it][2], v[it][3], n[it][0]});
                ldsw<u32x4>(a0[it] + 192, u32x4{s1, s2, s3, s4});
            }
        }
    }
    STAMP(5);
    {
        const u32 a = LU + (256 * ((w + ia) & 7) + 4 * l) * 2;
#pragma unroll
        for (int c2 = 0; c2 < 4; c2++) {
            ldsw<u32x2>(a + 2 * c2 * 4096, u32x2{__builtin_amdgcn_perm(ur[1][c2], ur[0][c2], 0x05040100u), __builtin_amdgcn_perm(ur[3][c2], ur[2][c2], 0x05040100u)});
            ldsw<u32x2>(a + (2 * c2 + 1) * 4096, u32x2{__builtin_amdgcn_perm(ur[1][c2], ur[0][c2], 0x07060302u), __builtin_amdgcn_perm(ur[3][c2], ur[2][c2], 0x07060302u)});
        }
    }
    STAMP(3);
    bar();
    STAMP(10);
    const int rr = l & 15;
    f32x4 y[4] = {};
    {
        float sm[36];
        sm[0] = 0.f;
#pragma unroll
        for (int i = 0; i < 5; i++) {
            u32x4 t = ldsr<u32x4>(UB + l * 32 - 48 + i * 16);
            if (i < 3 && 2 * l + i < 3) t = u32x4{0, 0, 0, 0};
#pragma unroll
            for (int e = 0; e < 8; e++)
                if (8 * i + e < 35) sm[8 * i + e + 1] = sm[8 * i + e] + ((e & 1) ? hi2f(t[e >> 1]) : lo2f(t[e >> 1]));
        }
        const u32 wb = LB + l * 64, sw = (l >> 1) & 3;
#pragma unroll
        for (int ch = 0; ch < 4; ch++) {
            const int o1 = ch < 2 ? 13 : 20, o0 = ch < 2 ? 4 : 13, b0 = 8 * (ch & 1);
            ldsw<u32x4>(wb + ((ch ^ sw) << 4), u32x4{pk(sm[b0 + o1] - sm[b0 + o0], sm[b0 + 1 + o1] - sm[b0 + 1 + o0]), pk(sm[b0 + 2 + o1] - sm[b0 + 2 + o0], sm[b0 + 3 + o1] - sm[b0 + 3 + o0]),
                                                     pk(sm[b0 + 4 + o1] - sm[b0 + 4 + o0], sm[b0 + 5 + o1] - sm[b0 + 5 + o0]), pk(sm[b0 + 6 + o1] - sm[b0 + 6 + o0], sm[b0 + 7 + o1] - sm[b0 + 7 + o0])});
        }
        s16x8 fw[4], ft[4];
#pragma unroll
        for (int s = 0; s < 4; s++) {
            s16x4 t[2];
#pragma unroll
            for (int rd = 0; rd < 2; rd++) {
                const int blk = 4 * g + 2 * rd + (q >> 1);
                t[rd] = trd(LB + s * 1024 + blk * 64 + (((2 * (q & 1) + (p >> 1)) ^ ((blk >> 1) & 3)) << 4) + (p & 1) * 8);
            }
            fw[s] = cat(t[0], t[1]);
            const u32 a = LB + LR + (126 - 32 * s - 2 * rr + 8 * g) * 2;
            ft[s] = frag(ldsr<u32>(a), ldsr<u32>(a + 4), ldsr<u32>(a + 8), ldsr<u32>(a + 12));
        }
#pragma unroll
        for (int s = 0; s < 4; s++)
#pragma unroll
            for (int ct = s; ct < 4; ct++) y[ct] = mfma16(fw[s], ft[ct - s], y[ct]);
    }
    STAMP(13);
    const u32 e0 = 1055 - 16 - rr + 8 * g, sh = e0 & 3;
    auto half = [&](auto HI) {
        constexpr bool hi = decltype(HI)::value;
        constexpr int T0 = hi ? 4 : 0, B0 = hi ? 1 : 0, NB = hi ? 7 : 4, NRD = NB + 8;
        u32 ka0 = KB + kaddr(e0 - sh) + sh * 64 - 3 * 2048, ka1 = KB + kaddr(e0 - sh + 4) + sh * 64 - 3 * 2048;
        u32 ua = UB + 32 * rr + 16 * g - 32;
        u32 mk[4];
#pragma unroll
        for (int i = 0; i < 4; i++) mk[i] = (2 * i > 16 + rr - 8 * g ? 0u : 0xffffu) | (2 * i + 1 > 16 + rr - 8 * g ? 0u : 0xffff0000u);
        int lim = 2 * rr + g - 2;
        u32x2 A[2][8]; u32x4 B[2][NB];
        auto rdc = [&](int s, int n) {
            if (n < 12) {
                const int m = n / 3, k = n % 3;
                if (k == 0) B[s][m] = ldsr<u32x4>(ua + (B0 + m) * 512);
                else if (k == 1) A[s][2 * (3 - m)] = ldsr<u32x2>(ka0 + m * 2048);
                else A[s][2 * (3 - m) + 1] = ldsr<u32x2>(ka1 + m * 2048);
            } else B[s][n - 8] = ldsr<u32x4>(ua + (B0 + n - 8) * 512);
        };
        auto group = [&](int s, bool next) {
            A[s][0] &= u32x2{mk[0], mk[1]}; A[s][1] &= u32x2{mk[2], mk[3]};
            if (!hi && lim < 0) B[s][0] = u32x4{0, 0, 0, 0};
            ka0 -= 256; ka1 -= 256; ua -= 64; lim -= 4;
            mk[0] = mk[1] = mk[2] = mk[3] = 0xffffffffu;
            asm volatile("" : "+v"(ka0), "+v"(ka1), "+v"(ua));
            int n = 0;
#pragma unroll
            for (int m = 3; m >= 0; m--)
#pragma unroll
                for (int t = 0; t < 4; t++) {
                    const int ct = T0 + t - m;
                    if (ct < 0) continue;
                    SB;
                    y[t] = mfma16(frag(A[s][2 * m][0], A[s][2 * m][1], A[s][2 * m + 1][0], A[s][2 * m + 1][1]), __builtin_bit_cast(s16x8, B[s][ct - B0]), y[t]);
                    SB;
                    if (next && n < NRD) rdc(s ^ 1, n);
                    n++;
                    if (!hi && next && (n == 1 || n == 6)) rdc(s ^ 1, n++);
                }
        };
        asm volatile("" : "+v"(ka0), "+v"(ka1), "+v"(ua));
#pragma unroll
        for (int n = 0; n < NRD; n++) rdc(0, n);
        if (hi) {
            const u32x2 x0 = ldsr<u32x2>(ka0 - 2048), x1 = ldsr<u32x2>(ka1 - 2048);
            u32x4 b0 = ldsr<u32x4>(ua);
            if (lim < 0) b0 = u32x4{0, 0, 0, 0};
            const s16x8 xa = frag(x0[0], x0[1], x1[0], x1[1]);
            y[0] = mfma16(xa, __builtin_bit_cast(s16x8, b0), y[0]);
#pragma unroll
            for (int t = 1; t < 4; t++) y[t] = mfma16(xa, __builtin_bit_cast(s16x8, B[0][t - 1]), y[t]);
        }
        if (hi) {
#pragma unroll 1
            for (int gg = 0; gg < 3; gg++) { group(0, true); group(1, true); }
            group(0, true); group(1, false);
        } else {
            u32 v[8];
#pragma unroll
            for (int c2 = 0; c2 < 8; c2++) v[c2] = ldsr<u32>(LU + c2 * 4096 + 2048 + tid * 4);
            const u32 op = (1024 + 2 * tid) * 1280 + 8 * ia;
            const u32 idx = tid, row = (idx * 6554u) >> 16, wof = row * 1120 + idx * 16;
            const char* wob = (const char*)wo + ((sbid() >> 3) & 7) * 160;
#pragma unroll
            for (int gg = 0; gg < 8; gg++) {
                if (gg == 0 || gg == 4) {
                    const u32 sel = gg ? 0x07060302u : 0x05040100u;
                    out16(out, op + (gg ? 1280 : 0), u32x4{__builtin_amdgcn_perm(v[1], v[0], sel), __builtin_amdgcn_perm(v[3], v[2], sel),
                                                           __builtin_amdgcn_perm(v[5], v[4], sel), __builtin_amdgcn_perm(v[7], v[6], sel)});
                }
                if (w < 5) {
                    constexpr int G0[9] = {0, 0, 2, 4, 6, 6, 8, 10, 11};
#pragma unroll
                    for (int gq = G0[gg]; gq < G0[gg + 1]; gq++) dma(gq * 5120 + w * 1024, wof, wob + gq * (32 * 1280));
                }
                group(gg & 1, gg < 7);
            }
        }
#pragma unroll
        for (int ct = 0; ct < 4; ct++) {
            ldsw<u32x2>(UB + (T0 + ct) * 512 + rr * 32 + g * 8, u32x2{pk(y[ct][0], y[ct][1]), pk(y[ct][2], y[ct][3])});
            y[ct] = f32x4{0.f, 0.f, 0.f, 0.f};
        }
        if (hi) STAMP(14); else STAMP(16);
        bar();
        if (hi) STAMP(15); else STAMP(17);
        if (!hi) {
            load_wo(wo, (sbid() >> 3) & 7, 11 + w, 20, 8, l);
            if (w >= 4 && !YLATE) store_rows(out, ia, 0, tid - 256);
        }
    };
    half(Flag<true>{});
    STAMP(11);
    half(Flag<false>{});
    STAMP(12);
    if (w >= 4 && !YLATE) waitv<4>(); else waitv<0>();
    STAMP(7);
    return tag;
}

__global__ void __launch_bounds__(512, 2)
fused_kernel(const u16* __restrict__ x, const u16* __restrict__ W, const u16* __restrict__ wphi, const u16* __restrict__ wo,
             u16* __restrict__ out, char* __restrict__ ws, const u16* __restrict__ u, const u16* __restrict__ Are,
             const u16* __restrict__ Aim, const u16* __restrict__ Bre, const u16* __restrict__ Bim, const u16* __restrict__ Cre,
             const u16* __restrict__ Cim, const u16* __restrict__ logdt, const u16* __restrict__ Dskip) {
    __shared__ __attribute__((aligned(16))) char smem[LDS_BYTES];
    asm volatile("" ::"s"((u32)(size_t)(LAS char*)smem));
    u32 pfd = 0;
    if (threadIdx.x < 12) {
        u64 pc; asm volatile("s_getpc_b64 %0" : "=s"(pc));
        asm volatile("global_load_dword %0, %1, %2" : "+v"(pfd) : "v"(threadIdx.x * 128), "s"(pc + min((sbid() >> 3) * 1536, CODE_PF_BYTES - 1536)));
    }
    asm volatile("" ::"s"(x), "s"(W), "s"(wphi), "s"(wo), "s"(out), "s"(ws), "s"(u), "s"(Are), "s"(Aim), "s"(Bre), "s"(Bim), "s"(Cre), "s"(Cim),
                 "s"(logdt), "s"(Dskip));
    const Role R = role_of(sbid());
    u64 tag;
    if (R.is_a) prologue_a(R.ia, Are, Aim, Bre, Bim, Cre, Cim, logdt, Dskip);
    else if (__builtin_amdgcn_readfirstlane(threadIdx.x) >= 384) loader_b<true>(x, W, wo, R.r, R.c, __builtin_amdgcn_readfirstlane(threadIdx.x >> 6) - 6, pfd);
    u32 sel = R.nb;
    asm volatile("" : "+s"(sel));
    if ((sbid() >> 3) >= sel) {
        tag = path_a(u, wo, out, ws, R.ia, pfd STAMP_PASS);
    } else {
        tag = path_b(x, W, wphi, wo, out, ws, R.r, R.c, pfd STAMP_PASS);
    }
    out_proj(out, ws, tag, R.is_a ? R.ia : -1 STAMP_PASS);
}

extern "C" void kernel_launch(void* const* d_in, const int* in_sizes, int n_in, void* d_out, int out_size, void* d_ws, size_t ws_size,
                              hipStream_t stream) {
    const u16* const* in = (const u16* const*)d_in;
    fused_kernel<<<256, 512, 0, stream>>>(in[9], in[10], in[11], in[12], (u16*)d_out, (char*)d_ws, in[0], in[1], in[2], in[3], in[4], in[5],
                                          in[6], in[7], in[8]);
}
```

```cpp
#include <hip/hip_runtime.h>
#include <hip/hip_bf16.h>

typedef unsigned short u16;
typedef unsigned u32;
typedef unsigned long long u64;
typedef __attribute__((ext_vector_type(8))) short s16x8;
typedef __attribute__((ext_vector_type(4))) short s16x4;
typedef __attribute__((ext_vector_type(16))) float f32x16;
typedef __attribute__((ext_vector_type(4))) float f32x4;
typedef __attribute__((ext_vector_type(2))) float f32x2;
typedef __attribute__((ext_vector_type(4))) u32 u32x4;
typedef __attribute__((ext_vector_type(2))) u32 u32x2;
typedef __attribute__((ext_vector_type(2))) __bf16 bf16x2;
#define LAS __attribute__((address_space(3)))
#define GAS __attribute__((address_space(1)))
#define DI __device__ __forceinline__
#define SB __builtin_amdgcn_sched_barrier(0)
DI unsigned sbid() { return __builtin_amdgcn_workgroup_id_x(); }

#define PSLEEP 4
#define STW 0
#define YSLEEP 12
#define AUXA 0
#define AUXB 0
#define RMAP 1
#define YLATE 0
#define NPRE_ 8
#define NLD0 8
#define TLBW 0
#define PRE 6
#define LATE 1
#define UISS 1
#define UPOS 1
#define WOPOS 0
#define YMODE 0
constexpr int LDS_BYTES = 163840;
constexpr u32 WS_EPOCH = 0, WS_ACK = 4096, WS_F1 = 65536, WS_F2 = 131072, WS_PART = 524288, WS_O = 2u << 20, WS_END = 5u << 20;
constexpr u32 WS_STAMP = 16u << 20;
constexpr u32 kSpinCap = 1u << 18;
constexpr u32 CODE_PF_BYTES = 44032;

DI u32 pk(float a, float b) { f32x2 v = {a, b}; return __builtin_bit_cast(u32, __builtin_convertvector(v, bf16x2)); }
DI float lo2f(u32 x) { return __uint_as_float(x << 16); }
DI float hi2f(u32 x) { return __uint_as_float(x & 0xffff0000u); }
DI float b2f(u16 h) { return __uint_as_float((u32)h << 16); }
DI s16x4 trd(u32 a) { return __builtin_amdgcn_ds_read_tr16_b64_v4i16((LAS s16x4*)(size_t)a); }
DI s16x8 cat(s16x4 a, s16x4 b) { return __builtin_shufflevector(a, b, 0, 1, 2, 3, 4, 5, 6, 7); }
DI s16x8 frag(u32 a, u32 b, u32 c, u32 d) { u32x4 v = {a, b, c, d}; return __builtin_bit_cast(s16x8, v); }
template <class T> DI T ldsr(u32 a) { return *(LAS T*)(size_t)a; }
template <class T> DI void ldsw(u32 a, T v) { *(LAS T*)(size_t)a = v; }
template <int N> DI void waitv() { asm volatile("s_waitcnt vmcnt(%0)" ::"i"(N) : "memory"); }
DI void bar() { asm volatile("s_waitcnt lgkmcnt(0)\n\ts_barrier" ::: "memory"); }
DI f32x16 mfma32(s16x8 a, s16x8 b, f32x16 c) { return __builtin_amdgcn_mfma_f32_32x32x16_bf16(a, b, c, 0, 0, 0); }
DI f32x4 mfma16(s16x8 a, s16x8 b, f32x4 c) { return __builtin_amdgcn_mfma_f32_16x16x32_bf16(a, b, c, 0, 0, 0); }
DI float max3(float a, float b, float c) { float r; asm("v_max3_f32 %0, %1, %2, %3" : "=v"(r) : "v"(a), "v"(b), "v"(c)); return r; }
DI float min3(float a, float b, float c) { float r; asm("v_min3_f32 %0, %1, %2, %3" : "=v"(r) : "v"(a), "v"(b), "v"(c)); return r; }
DI float dot2(u32 x, u32 sel, float c) { return __builtin_amdgcn_fdot2_f32_bf16(__builtin_bit_cast(bf16x2, sel), __builtin_bit_cast(bf16x2, x), c, false); }
template <class R> DI void st128(u32x4 v, R rs, u32 voff, u32 soff, int) { __builtin_amdgcn_raw_buffer_store_b128(v, rs, voff + soff, 0, 16); }
DI void out16(u16* out, u32 off, u32x4 v) {
    __builtin_amdgcn_raw_buffer_store_b128(v, __builtin_amdgcn_make_buffer_rsrc((void*)out, 0, 2048 * 2560, 0x00020000), off * 2, 0, AUXA);
}
DI void out8(u16* out, u32 off, u32x2 v) {
    __builtin_amdgcn_raw_buffer_store_b64(v, __builtin_amdgcn_make_buffer_rsrc((void*)out, 0, 2048 * 2560, 0x00020000), off * 2, 0, AUXB);
}
DI void dma(u32 m0v, u32 voff, const void* sbase) {
    asm volatile("s_mov_b32 m0, %0\n\ts_nop 0\n\tglobal_load_lds_dwordx4 %1, %2" ::"s"(m0v), "v"(voff), "s"(sbase) : "memory");
}
DI void dma_m(u32 m0v, u32 voff, const void* sbase, u32 mlo, u32 mhi) {
    asm volatile("s_mov_b32 exec_lo, %3\n\ts_mov_b32 exec_hi, %4\n\ts_mov_b32 m0, %0\n\ts_nop 0\n\tglobal_load_lds_dwordx4 %1, %2\n\ts_mov_b64 exec, -1"
                 ::"s"(m0v), "v"(voff), "s"(sbase), "i"(mlo), "i"(mhi) : "memory");
}
#define STAMP(i)
#define STAMP_ARG
#define STAMP_PASS

DI void post_flag(char* fl, u64 tag, int tid, int n, int me) {
    if (tid < n) __hip_atomic_store((GAS u64*)(fl + tid * 256 + me * 8), tag, __ATOMIC_RELAXED, __HIP_MEMORY_SCOPE_AGENT);
}
DI void wait_flags(const char* fl, u64 tag, int l, int n) {
    const GAS u64* p = (const GAS u64*)(fl + (l < n ? l : 0) * 8);
    u64 v0 = __hip_atomic_load(p, __ATOMIC_RELAXED, __HIP_MEMORY_SCOPE_AGENT);
#pragma unroll 1
    for (u32 s = 0; s < kSpinCap; s++) {
        __builtin_amdgcn_s_sleep(PSLEEP);
        const u64 v1 = __hip_atomic_load(p, __ATOMIC_RELAXED, __HIP_MEMORY_SCOPE_AGENT);
        if (__all(v0 == tag)) break;
        v0 = v1;
    }
}

template <class F> DI void features(const float (&v)[20], const s16x8 (&wf)[2][3], u32 (&phi)[20], F filler) {
    u32 b[10];
#pragma unroll
    for (int i = 0; i < 10; i++) b[i] = pk(v[2 * i], v[2 * i + 1]);
    const s16x8 B0 = frag(b[0], b[1], b[2], b[3]), B1 = frag(b[4], b[5], b[6], b[7]), B2 = frag(b[8], b[9], 0, 0);
    f32x16 p0 = {}, p1 = {};
    p0 = mfma32(wf[0][0], B0, p0); p1 = mfma32(wf[1][0], B0, p1);
    p0 = mfma32(wf[0][1], B1, p0); p1 = mfma32(wf[1][1], B1, p1);
    p0 = mfma32(wf[0][2], B2, p0); p1 = mfma32(wf[1][2], B2, p1);
    SB; filler(); SB;
    asm volatile("s_nop 11" : "+v"(p0), "+v"(p1));
    float p[20];
#pragma unroll
    for (int i = 0; i < 20; i++) p[i] = i < 16 ? p0[i] : p1[i - 16];
    float mxa = max3(p[0], p[1], p[2]), mna = min3(p[0], p[1], p[2]), mxb = max3(p[3], p[4], p[5]), mnb = min3(p[3], p[4], p[5]);
#pragma unroll
    for (int i = 6; i < 18; i += 4) {
        mxa = max3(mxa, p[i], p[i + 1]); mna = min3(mna, p[i], p[i + 1]);
        mxb = max3(mxb, p[i + 2], p[i + 3]); mnb = min3(mnb, p[i + 2], p[i + 3]);
    }
    mxa = max3(mxa, p[18], p[19]); mna = min3(mna, p[18], p[19]);
    const float mx = max3(mxa, mxb, mxb), mn = min3(mna, mnb, mnb);
    const float L2E = 1.4426950408889634f;
    const float cx = -mx * L2E, cn = mn * L2E;
    float e[40], s1, s2;
#pragma unroll
    for (int i = 0; i < 20; i++) {
        e[i] = __builtin_amdgcn_exp2f(__builtin_fmaf(p[i], L2E, cx));
        e[20 + i] = __builtin_amdgcn_exp2f(__builtin_fmaf(p[i], -L2E, cn));
        if (i == 0) { s1 = e[0]; s2 = e[20]; } else { s1 += e[i]; s2 += e[20 + i]; }
    }
    const float r1 = __builtin_amdgcn_rcpf(s1), r2 = __builtin_amdgcn_rcpf(s2);
#pragma unroll
    for (int i = 0; i < 10; i++) { phi[i] = pk(e[2 * i] * r1, e[2 * i + 1] * r1); phi[10 + i] = pk(e[20 + 2 * i] * r2, e[21 + 2 * i] * r2); }
}

constexpr int WBLK = 960, WSLOT = 8 * WBLK, XSLOT = 24576;
constexpr int LW = XSLOT, LIMG = 102400, LMISC = LIMG + 2 * XSLOT, LZERO = LMISC + 3200, LTAB = LZERO + 64, TROW = 56, THEAD = 40 * TROW;
DI int xslot(int j) { return j % 3 == 0 ? 0 : LIMG + (j % 3 - 1) * XSLOT; }
static_assert(LTAB + 80 * TROW <= LDS_BYTES, "LDS map");

template <bool V> struct Flag { static constexpr bool value = V; };

struct Role { bool is_a; int r, c, ia; u32 nb; };
DI Role role_of(u32 bid) {
    const int xcd = bid & 7, j = bid >> 3;
    Role R;
    const int e = xcd & 1, m = xcd >> 1, nb = e ? 20 : 24, cq = e ? (j * 52) >> 8 : (j * 43) >> 8;
    R.r = __builtin_amdgcn_readfirstlane(e ? 6 + j - 5 * cq : j - 6 * cq); R.c = __builtin_amdgcn_readfirstlane(4 * m + cq);
    R.ia = __builtin_amdgcn_readfirstlane((RMAP == 1 ? (e ? 32 + 12 * m : 8 * m) : 20 * m + (e ? 8 : 0)) + j - nb);
    R.is_a = j >= nb; R.nb = nb;
    return R;
}

DI void load_wo(const u16* wo, int cb, int wi, int end, int nw, int l) {
    u32 off[5];
#pragma unroll
    for (int i = 0; i < 5; i++) { const u32 idx = 64 * i + l, row = (idx * 6554u) >> 16; off[i] = row * 1120 + idx * 16; }
#pragma unroll 1
    for (int gq = wi; gq < end; gq += nw) {
        const char* gp = (const char*)wo + cb * 160 + gq * (32 * 1280);
#pragma unroll
        for (int i = 0; i < 5; i++) dma(gq * 5120 + i * 1024, off[i], gp);
    }
}

DI void store_rows(u16* __restrict__ out, int ia, int t0, int tt) {
    u32x2 v[8];
#pragma unroll
    for (int c2 = 0; c2 < 8; c2++) v[c2] = ldsr<u32x2>(131072 + c2 * 4096 + t0 * 2 + tt * 8);
    const u32 op = (t0 + 4 * tt) * 1280 + 8 * ia;
#pragma unroll
    for (int i = 0; i < 4; i++) {
        const u32 sel = (i & 1) ? 0x07060302u : 0x05040100u;
        out16(out, op + i * 1280, u32x4{__builtin_amdgcn_perm(v[1][i >> 1], v[0][i >> 1], sel), __builtin_amdgcn_perm(v[3][i >> 1], v[2][i >> 1], sel),
                                        __builtin_amdgcn_perm(v[5][i >> 1], v[4][i >> 1], sel), __builtin_amdgcn_perm(v[7][i >> 1], v[6][i >> 1], sel)});
    }
}

DI void out_proj(u16* __restrict__ out, char* __restrict__ ws, u64 tag, int ia STAMP_ARG) {
    const int bid = sbid(), tid = threadIdx.x, l = tid & 63, w = __builtin_amdgcn_readfirstlane(tid >> 6);
    const int t = 4 * (bid & 7) + (bid >> 6), cb = (bid >> 3) & 7, rs = (t * 43) >> 7, sub = t - 3 * rs;
    bar();
    if (w >= 4) {
        if (YLATE && ia >= 0) {
            bar();
            __builtin_amdgcn_s_sleep(YSLEEP);
            store_rows(out, ia, 0, tid - 256);
        }
        return;
    }
    constexpr int NPRE = NPRE_;
    const auto rsw = __builtin_amdgcn_make_buffer_rsrc((void*)ws, 0, WS_END, 0x00020000);
    const int g = l >> 4, q = (l >> 2) & 3, p = l & 3, hp = g & 1, P = g >> 1;
    u32 b1 = (20 * hp + 8 * P + q) * 160 + p * 8, b2 = (80 * P + 20 * hp + 16 + q) * 160 + p * 8;
    u32 b1h = b1 + 51200, b2h = b2 + 51200;
    asm volatile("" : "+v"(b1), "+v"(b2), "+v"(b1h), "+v"(b2h));
    s16x4 wfr[NPRE][10];
    auto rdw = [&](int ks, int n) {
        const int G = ks / 5, i = ks % 5, tt = n >> 1, rd = n & 1;
        const u32 so = (G & 1) * 25600;
        wfr[ks % NPRE][n] = i < 4 ? trd((G >> 1 ? b1h : b1) + so + (40 * i + 4 * rd) * 160 + 32 * tt) : trd((G >> 1 ? b2h : b2) + so + 40 * rd * 160 + 32 * tt);
    };
#pragma unroll
    for (int ks = 0; ks < NPRE; ks++)
#pragma unroll
        for (int n = 0; n < 10; n++) rdw(ks, n);
    if (w == 0) wait_flags(ws + WS_F2 + (rs * 24 + sub * 8 + cb) * 256, tag, l, 16);
    bar();
    STAMP(8);
    const int ti = 16 * (w & 1) + (l & 15);
    const u32 ob = WS_O + (rs * 96 + 2 * sub + (w >> 1)) * 2560;
    const u32 v1 = P * 1024 + (hp * 32 + ti) * 16, v2 = 2048 + 2 * P * 15360 + (hp * 32 + ti) * 8;
    u32x4 of[16]; u32x2 ot[8];
    auto ldo = [&](int n) {
        const int G = n / 6, i = n % 6;
        if (i < 4) of[4 * G + i] = __builtin_amdgcn_raw_buffer_load_b128(rsw, v1, ob + (4 * G + i) * 15360, 16);
        else ot[2 * G + i - 4] = __builtin_amdgcn_raw_buffer_load_b64(rsw, v2, ob + (4 * G + i - 4) * 15360, 16);
    };
#pragma unroll
    for (int n = 0; n < NLD0; n++) ldo(n);
    if (bid == 0 && tid == 0) __hip_atomic_store((GAS u64*)(ws + WS_EPOCH), tag, __ATOMIC_RELAXED, __HIP_MEMORY_SCOPE_AGENT);
    f32x4 oc[5] = {};
#pragma unroll
    for (int ks = 0; ks < 20; ks++) {
        const int G = ks / 5, i = ks % 5;
        const s16x8 B = i < 4 ? __builtin_bit_cast(s16x8, of[4 * G + i]) : frag(ot[2 * G][0], ot[2 * G][1], ot[2 * G + 1][0], ot[2 * G + 1][1]);
#pragma unroll
        for (int tt = 0; tt < 5; tt++) {
            SB;
            oc[tt] = mfma16(cat(wfr[ks % NPRE][2 * tt], wfr[ks % NPRE][2 * tt + 1]), B, oc[tt]);
            SB;
            if (ks + NPRE < 20) { rdw(ks + NPRE, 2 * tt); rdw(ks + NPRE, 2 * tt + 1); }
            if ((tt == 0 || tt == 2) && NLD0 + 2 * ks + (tt >> 1) < 24) ldo(NLD0 + 2 * ks + (tt >> 1));
        }
    }
    SB;
    const u32 op = (64 * t + 16 * w + (l & 15)) * 1280 + 640 + 80 * cb + 4 * g;
#pragma unroll
    for (int tt = 0; tt < 5; tt++) out8(out, op + 16 * tt, u32x2{pk(oc[tt][0], oc[tt][1]), pk(oc[tt][2], oc[tt][3])});
    STAMP(9);
}

template <bool FIRST>
DI void loader_b(const u16* __restrict__ x, const u16* __restrict__ W, const u16* __restrict__ wo, char* __restrict__ ws, int r, int c, int lw, u32& pfd, u64& tag STAMP_ARG) {
    const int l = threadIdx.x & 63;
    int ph = r + (((c & 3) * 10) >> 2); if (ph >= 10) ph -= 10; if (ph >= 10) ph -= 10;
    const int pl = l >> 4, idx = l & 15, q3 = (idx * 11) >> 5;
    const int q = pl < 3 ? idx >> 2 : q3 & 3, sg = pl < 3 ? pl : idx - 3 * q3, ch = pl < 3 ? (idx & 3) + ((idx >> 1) & 1) : 2;
    const u32 wl = q * 3840 + sg * 1280 + ch * 16 + (sg == 0 ? ((c & 1) ? 0 : 160) : 80);
    const u32 xo0 = (l >> 3) * 1280 + (((l & 7) ^ (l >> 4)) << 4), xo1 = ((l >> 3) + 8) * 1280 + (((l & 7) ^ ((l >> 4) | 4)) << 4);
    u32 xr[2][3];
#pragma unroll
    for (int i = 0; i < 6; i++) { const int row = 192 * r + 96 * (i / 3) + 48 * lw + 16 * (i % 3); xr[i / 3][i % 3] = (row < 2048 ? row : row - 64) * 1280; }
    const char* wb = (const char*)W + c * 80 - 80 + lw * (16 * 3840);
    auto ld_x = [&](int hx) {
        const int jx = hx >> 1, hh = hx & 1;
        int jr = jx + ph; if (jr >= 10) jr -= 10;
        const char* g = (const char*)x + jr * 128;
        const u32 d = xslot(jx) + (96 * hh + 48 * lw) * 128;
#pragma unroll
        for (int i = 0; i < 3; i++) { dma(d + i * 2048, xo0, g + xr[hh][i]); dma(d + i * 2048 + 1024, xo1, g + xr[hh][i]); }
    };
    auto ld_w = [&](int i) {
        int ir = i + 2 * ph; if (ir >= 20) ir -= 20;
        const u32 d = LW + (i & 3) * WSLOT + lw * 4 * WBLK;
        const char* g = wb + ir * (32 * 3840);
#pragma unroll
        for (int b = 0; b < 4; b++) dma_m(d + b * WBLK, wl, g + b * (4 * 3840), 0xffffffffu, 0x0fffffffu);
    };
    if (FIRST) { STAMP(4); ld_w(0); STAMP(14); ld_x(0); ld_x(1); STAMP(15); return; }
    if (PRE >= 4) ld_w(1);
    if (PRE >= 5) ld_x(2);
    if (PRE >= 6) ld_x(3);
    if (PRE >= 7) ld_w(2);
    STAMP(19);
    asm volatile("s_load_dwordx2 %0, %1, 0\n\ts_waitcnt vmcnt(%2) lgkmcnt(0)" : "=&s"(tag) : "s"(ws + WS_EPOCH),
                 "i"(PRE == 3 ? 0 : PRE == 4 ? 4 : PRE == 5 ? 10 : PRE == 6 ? 16 : 20) : "memory");
    STAMP(1);
    bar();
    if (PRE < 4) ld_w(1);
    if (PRE < 5) ld_x(2);
    if (PRE < 6) ld_x(3);
    if (PRE < 7) ld_w(2);
    if (LATE == 0) { ld_w(3); ld_x(4); }
#pragma unroll
    for (int i = 0; i < 19; i++) {
        if (i == 0) waitv<LATE ? 16 : 26>(); else if (i == 2) waitv<26>();
        else if (i <= 15) waitv<20>(); else if (i == 16) waitv<14>(); else if (i == 17) waitv<4>(); else waitv<2>();
        bar();
        if (i == 0 && LATE) { ld_w(3); ld_x(4); }
        if (i + 5 < 20) ld_x(i + 5);
        if (i + 4 < 20) ld_w(i + 4);
        if (i == 17) {
            const u32 ln = __builtin_amdgcn_readfirstlane(min((sbid() >> 3) * 256 + lw * 64, 6208u)) + l;
            asm volatile("global_load_dword %0, %1, %2\n\tglobal_load_dword %0, %1, %3" : "+v"(pfd) : "v"(ln * 128), "s"(wo), "s"((const char*)wo + 128 * 128));
        }
    }
    bar();
    load_wo(wo, (sbid() >> 3) & 7, lw, 20, 2, l);
}

DI u64 path_b(const u16* __restrict__ x, const u16* __restrict__ W, const u16* __restrict__ wphi, const u16* __restrict__ wo,
               u16* __restrict__ out, char* __restrict__ ws, int r, int c, u32& pfd STAMP_ARG) {
    const int tid = threadIdx.x, l = tid & 63;
    const int w = __builtin_amdgcn_readfirstlane(tid >> 6);
    const auto rsw = __builtin_amdgcn_make_buffer_rsrc((void*)ws, 0, WS_END, 0x00020000);
    const int g = l >> 4, q = (l >> 2) & 3, p = l & 3, h = l >> 5;
    const int rq = 2 * (g & 1) + (p >> 1), hk = g >> 1;
    const bool same = hk == (p & 1);
    u64 tag;
    u32 phiq[20];
    f32x16 acc[4] = {};
    s16x8 wfq[2][3];
    if (w >= 6) {
        loader_b<false>(x, W, wo, ws, r, c, w - 6, pfd, tag STAMP_PASS);
        if (sbid() == 0) wait_flags(ws + WS_ACK + (w - 6) * 320, tag + 1, l, 40);
    } else {
        if (w < 4) dma_m(LMISC + w * 800, l * 16, (const char*)wphi + ((c ^ (w >> 1)) * 2 + (w & 1)) * 800, 0xffffffffu, 0x3ffffu);
        if (tid < 2) ldsw<u64>(LZERO + tid * 8, 0ull);
        u32 bA = LW + (g >> 1) * 2 * WBLK + q * 64 + (p & 1) * 32 + (g & 1) * 16 + (p >> 1) * 8;
        u32 bD = LW + (g >> 1) * 2 * WBLK + 768 + (3 * q + 2 * (g & 1) + (p >> 1)) * 16 + (p & 1) * 8;
        asm volatile("" : "+v"(bA), "+v"(bD));
        const int trow = 32 * w + (l & 31);
        s16x8 wfk[2][3];
        auto load_wf = [&](s16x8 (&wf)[2][3], u32 base) {
#pragma unroll
            for (int to = 0; to < 2; to++)
#pragma unroll
                for (int s = 0; s < 3; s++) {
                    s16x4 t[2];
#pragma unroll
                    for (int rd = 0; rd < 2; rd++) {
                        const int mu = 2 * s + rd;
                        if (mu < 5) {
                            int dq = mu + hk; if (mu == 4) dq = hk ? 0 : 4;
                            const bool ok = same && (to == 0 || rq == 0);
                            t[rd] = trd(ok ? base + hk * 800 + (4 * dq + q) * 40 + (4 * to + rq) * 8 : LZERO);
                        } else t[rd] = s16x4{0, 0, 0, 0};
                    }
                    wf[to][s] = cat(t[0], t[1]);
                }
        };
        asm volatile("s_load_dwordx2 %0, %1, 0\n\ts_waitcnt vmcnt(0) lgkmcnt(0)" : "=&s"(tag) : "s"(ws + WS_EPOCH), "v"(pfd) : "memory");
        bar();
        STAMP(1);
        const u32 sw = ((trow & 2) << 1) | ((trow & 4) >> 1) | (trow & 1);
        const u32 rowa = LIMG + h * XSLOT + trow * 128;
        auto gemm = [&](auto HV) {
            constexpr bool hv = decltype(HV)::value;
            constexpr int NB = hv ? 3 : 1, NM = hv ? 6 : 3, NR = hv ? 11 : 7;
            f32x16 vc[3] = {};
            u32 aX[4], aXh[4];
            const int tx = hv ? 32 * (w - 2) + (l & 31) : trow;
#pragma unroll
            for (int k = 0; k < 4; k++) {
                aX[k] = tx * 128 + (((2 * k + h) ^ ((tx >> 1) & 7)) << 4); aXh[k] = aX[k] + LIMG;
                asm volatile("" : "+v"(aX[k]), "+v"(aXh[k]));
            }
            s16x4 fa[2][8]; s16x8 fb[2][NB];
            auto rd = [&](int s, int i, int ks, int n) {
                const int nb = hv ? (n == 0 ? 1 : n == 9 ? 0 : n == 10 ? 2 : -1) : (n == 0 ? 0 : -1);
                if (nb >= 0) {
                    const int sl = (i >> 1) % 3, k = (i & 1) * 2 + ks;
                    fb[s][nb] = sl == 0 ? ldsr<s16x8>(aX[k] + nb * 8192) : ldsr<s16x8>(aXh[k] + (sl - 1) * XSLOT + nb * 8192);
                } else {
                    const int na = n <= 4 ? n - 1 : n <= 6 ? n + 1 : n - 3;
                    const u32 o = (i & 3) * WSLOT + ks * 4 * WBLK + (na & 1) * WBLK;
                    fa[s][na] = na < 6 ? trd(bA + o + (na >> 1) * 256) : trd(bD + o);
                }
            };
            auto mm = [&](int s, int t) {
                if (t < 3) { const int tt = t == 2 ? 3 : t; acc[tt] = mfma32(cat(fa[s][2 * tt], fa[s][2 * tt + 1]), fb[s][hv ? 1 : 0], acc[tt]); }
                else { const int nb = t == 3 ? 1 : t == 4 ? 0 : 2; vc[nb] = mfma32(cat(fa[s][4], fa[s][5]), fb[s][hv ? nb : 0], vc[nb]); }
            };
            auto halfstep = [&](int s, int i, int ks, bool next) {
#pragma unroll
                for (int t = 0; t < NM; t++) {
                    SB; mm(s, t); SB;
                    if (t == 0 && ks == 0) bar();
                    if (next)
#pragma unroll
                        for (int n = 3 * t; n < 3 * t + 3 && n < NR; n++) rd(s ^ 1, i, ks, n);
                }
            };
#pragma unroll
            for (int n = 0; n < NR; n++) rd(0, 0, 0, n);
#pragma unroll
            for (int i = 0; i < 20; i++) {
                halfstep(0, i, 1, true);
                if (i == 1) load_wf(wfk, LMISC);
                if (i == 2) load_wf(wfq, LMISC + 1600);
                halfstep(1, i + 1, 0, i < 19);
            }
            if (hv) {
#pragma unroll
                for (int nb = 0; nb < 3; nb++)
#pragma unroll
                    for (int mu = 0; mu < 4; mu++) {
                        const int pc = 10 + mu + h;
                        ldsw<u32x2>(rowa + (nb - 1) * 8192 + (((pc >> 1) ^ sw) << 4) + (pc & 1) * 8,
                                    u32x2{pk(vc[nb][4 * mu], vc[nb][4 * mu + 1]), pk(vc[nb][4 * mu + 2], vc[nb][4 * mu + 3])});
                    }
            }
        };
        if ((w & 3) >= 2) gemm(Flag<true>{}); else gemm(Flag<false>{});
        STAMP(2);
        float kr[20]; u32 phik[20];
#pragma unroll
        for (int i = 0; i < 20; i++) kr[i] = i < 16 ? acc[1][i] : acc[3][i - 12];
        features(kr, wfk, phik, [&]() {
            const int pc = h ? 10 : 14;
            ldsw<u32x2>(rowa + (((pc >> 1) ^ sw) << 4) + (pc & 1) * 8, u32x2{pk(acc[3][8], acc[3][9]), pk(acc[3][10], acc[3][11])});
            ldsw<u32x2>(rowa + ((7 ^ sw) << 4) + 8, u32x2{0x3f80u, 0u});
        });
        const u32 keep = 192 * r + 32 * w < 2048 ? 0xffffffffu : 0u;
#pragma unroll
        for (int ch = 0; ch < 5; ch++) ldsw<u32x4>(rowa + ((ch ^ sw) << 4), u32x4{phik[4 * ch], phik[4 * ch + 1], phik[4 * ch + 2], phik[4 * ch + 3]} & keep);
        STAMP(10);
    }
    tag += 1;
    bar();
    if (w < 4) {
        const int pcn = 4 * (g & 1) + p;
        const int pa = 10 + (pcn < 6 ? pcn : 5), pb = 5 * (w & 1) + (pcn < 5 ? pcn : 4);
        u32 aa[2], ab[2];
#pragma unroll
        for (int rd = 0; rd < 2; rd++) {
            const int row = 8 * (g >> 1) + 4 * rd + q;
            const u32 sw = ((row & 2) << 1) | ((row & 4) >> 1) | (row & 1);
            const u32 base = LIMG + (w >> 1) * XSLOT + row * 128;
            aa[rd] = base + (((pa >> 1) ^ sw) << 4) + (pa & 1) * 8;
            ab[rd] = base + (((pb >> 1) ^ sw) << 4) + (pb & 1) * 8;
            asm volatile("" : "+v"(aa[rd]), "+v"(ab[rd]));
        }
        s16x4 ta[24], tb[24];
#pragma unroll
        for (int ks = 0; ks < 12; ks++) {
            ta[2 * ks] = trd(aa[0] + ks * 2048); ta[2 * ks + 1] = trd(aa[1] + ks * 2048);
            tb[2 * ks] = trd(ab[0] + ks * 2048); tb[2 * ks + 1] = trd(ab[1] + ks * 2048);
        }
        SB;
        f32x16 kv = {};
#pragma unroll
        for (int ks = 0; ks < 12; ks++) kv = mfma32(cat(ta[2 * ks], ta[2 * ks + 1]), cat(tb[2 * ks], tb[2 * ks + 1]), kv);
        if ((l & 31) < 20) {
            const u32 lam = h * 20 + (l & 31), o = WS_PART + (c * 16 + r) * 4096 + w * 960;
            st128(u32x4{pk(kv[0], kv[1]), pk(kv[2], kv[3]), pk(kv[4], kv[5]), pk(kv[6], kv[7])}, rsw, lam * 16, o, 16);
            __builtin_amdgcn_raw_buffer_store_b64(u32x2{pk(kv[8], kv[9]), pk(kv[10], kv[11])}, rsw, 640 + lam * 8, o, 16);
        }
    }
    STAMP(11);
    auto qfeat = [&]() {
        float qr[20];
#pragma unroll
        for (int i = 0; i < 20; i++) qr[i] = i < 16 ? acc[0][i] : acc[3][i - 16];
        features(qr, wfq, phiq, []() {});
#pragma unroll
        for (int i = 0; i < 20; i++) asm volatile("" : "+v"(phiq[i]));
    };
#pragma unroll 1
    for (int st = 0; st < 2; st++) {
        if (w < 6 && (w < 4) == (st == 1)) qfeat();
        if (st == 1) break;
        if (w < 4) waitv<0>();
        bar();
        post_flag(ws + WS_F1 + c * 4096, tag, tid, 11, r);
        STAMP(3);
    }
    STAMP(17);
    if (tid < 80) ldsw<u64>(LTAB + tid * TROW + 48, 0ull);
    if (w == 6) wait_flags(ws + WS_F1 + (c ^ 1) * 4096 + r * 256, tag, l, 11);
    bar();
    STAMP(5);
    if (w < 4) {
        const int t = tid < 240 ? tid : 239;
        const int un = (t * 1093) >> 16, it = t - un * 60;
        u32x4 pv[11];
#pragma unroll
        for (int i = 0; i < 11; i++) pv[i] = __builtin_amdgcn_raw_buffer_load_b128(rsw, t * 16, WS_PART + (c ^ 1) * 65536 + i * 4096, 16);
        float s[8] = {};
        u32 slo = 0x00003f80u, shi = 0x3f800000u;
        asm volatile("" : "+s"(slo), "+s"(shi));
#pragma unroll
        for (int i = 0; i < 11; i++)
#pragma unroll
            for (int e = 0; e < 4; e++) { s[2 * e] = dot2(pv[i][e], slo, s[2 * e]); s[2 * e + 1] = dot2(pv[i][e], shi, s[2 * e + 1]); }
        const int lam = it < 40 ? it : 2 * (it - 40);
        const int hh = lam >= 20, f = lam - 20 * hh;
        const u32 a0 = LTAB + (un >> 1) * THEAD + ((un & 1) * 20 + f) * TROW + hh * 8 + (it < 40 ? 0 : 32);
        ldsw<u32x2>(a0, u32x2{pk(s[0], s[1]), pk(s[2], s[3])});
        ldsw<u32x2>(a0 + (it < 40 ? 16 : TROW), u32x2{pk(s[4], s[5]), pk(s[6], s[7])});
    }
    bar();
    STAMP(6);
    if (w < 6 && 192 * r + 32 * w < 2048) {
        const u32 tb = LTAB + hk * THEAD + q * TROW;
        u32 t0 = tb + (same ? rq : 6) * 8, t1 = tb + (same && rq < 2 ? 4 + rq : 6) * 8;
        asm volatile("" : "+v"(t0), "+v"(t1));
        f32x16 o0 = {}, o1 = {};
#pragma unroll
        for (int s = 0; s < 5; s++) {
            const s16x8 B = frag(phiq[4 * s], phiq[4 * s + 1], phiq[4 * s + 2], phiq[4 * s + 3]);
            o0 = mfma32(cat(trd(t0 + 8 * s * TROW), trd(t0 + (8 * s + 4) * TROW)), B, o0);
            o1 = mfma32(cat(trd(t1 + 8 * s * TROW), trd(t1 + (8 * s + 4) * TROW)), B, o1);
        }
        const float rden = __builtin_amdgcn_rcpf(o1[4] + 1e-6f);
        u32 ob[10];
#pragma unroll
        for (int i = 0; i < 10; i++) ob[i] = i < 8 ? pk(o0[2 * i] * rden, o0[2 * i + 1] * rden) : pk(o1[2 * i - 16] * rden, o1[2 * i - 15] * rden);
        const u32 o = WS_O + ((r * 16 + (c ^ 1)) * 6 + w) * 2560;
        st128(u32x4{ob[0], ob[1], ob[2], ob[3]}, rsw, l * 16, o, 16);
        st128(u32x4{ob[4], ob[5], ob[6], ob[7]}, rsw, l * 16 + 1024, o, 16);
        __builtin_amdgcn_raw_buffer_store_b64(u32x2{ob[8], ob[9]}, rsw, l * 8 + 2048, o, 16);
    }
    STAMP(12);
    waitv<0>();
    asm volatile("" ::"v"(pfd));
    bar();
    post_flag(ws + WS_F2 + r * (24 * 256), tag, tid, 24, c);
    STAMP(7);
    return tag;
}

DI f32x2 cmul(f32x2 a, f32x2 b) {
    f32x2 d;
    asm("v_pk_mul_f32 %0, %1, %2 op_sel_hi:[0,1]\n\tv_pk_fma_f32 %0, %1, %2, %0 op_sel:[1,1,0] op_sel_hi:[1,0,1] neg_lo:[0,1,0]" : "=&v"(d) : "v"(a), "v"(b));
    return d;
}
DI f32x2 cmulc(f32x2 a, f32x2 b) {
    f32x2 d;
    asm("v_pk_mul_f32 %0, %1, %2 op_sel_hi:[0,1] neg_hi:[0,1]\n\tv_pk_fma_f32 %0, %1, %2, %0 op_sel:[1,1,0] op_sel_hi:[1,0,1]" : "=&v"(d) : "v"(a), "v"(b));
    return d;
}
DI f32x2 csq(f32x2 a) { return cmul(a, a); }
DI f32x2 cexp(float a, float b) {
    const float m = __builtin_amdgcn_exp2f(a * 1.4426950408889634f);
    const float c1 = 0.15915494309189535f, c2 = 6.4206382e-9f;
    const float n = __builtin_rintf(b * c1);
    const float f = __builtin_fmaf(b, c2, __builtin_fmaf(b, c1, -n));
    return f32x2{m * __builtin_amdgcn_cosf(f), m * __builtin_amdgcn_sinf(f)};
}
constexpr int LOWB = 7168, LQ = 8 * LOWB, QB = 8448, LU = 131072, LPRM = LQ + 8 * QB;
static_assert(LPRM + 6144 <= LU, "LDS map");
DI u32 kaddr(u32 e) { return ((e >> 5) << 8) + ((e & 31) << 1); }

DI void prologue_a(int ia, const u16* __restrict__ Are, const u16* __restrict__ Aim, const u16* __restrict__ Bre, const u16* __restrict__ Bim,
                   const u16* __restrict__ Cre, const u16* __restrict__ Cim, const u16* __restrict__ logdt, const u16* __restrict__ Dskip) {
    const int l = threadIdx.x & 63, w = __builtin_amdgcn_readfirstlane(threadIdx.x >> 6);
    const u16* src = w == 0 ? Are : w == 1 ? Aim : w == 2 ? Bre : w == 3 ? Bim : w == 4 ? Cre : w == 5 ? Cim : w == 6 ? logdt : Dskip;
    if (w < 6) { dma(w * 2048, ia * 2048 + l * 16, src); dma(w * 2048 + 1024, ia * 2048 + 1024 + l * 16, src); }
    else dma_m(w * 2048, ia * 16, src, 1u, 0u);
}

DI u64 path_a(const u16* __restrict__ u, const u16* __restrict__ wo, u16* __restrict__ out, char* __restrict__ ws, int ia, u32 pfd STAMP_ARG) {
    const int tid = threadIdx.x, l = tid & 63;
    const int w = __builtin_amdgcn_readfirstlane(tid >> 6);
    const u32 LB = w * LOWB, KB = LQ + w * QB, UB = LU + w * 4096;
    const int g = l >> 4, q = (l >> 2) & 3, p = l & 3, h = l >> 5;
    STAMP(1);
    u64 tag;
    asm volatile("s_load_dwordx2 %0, %1, 0\n\ts_waitcnt vmcnt(0) lgkmcnt(0)" : "=&s"(tag) : "s"(ws + WS_EPOCH), "v"(pfd) : "memory");
    tag += 1;
    bar();
    if (tid == 0) __hip_atomic_store((GAS u64*)(ws + WS_ACK + ia * 8), tag, __ATOMIC_RELAXED, __HIP_MEMORY_SCOPE_AGENT);
    const float ldt = b2f(ldsr<u16>(6 * 2048 + w * 2)), dsk = b2f(ldsr<u16>(7 * 2048 + w * 2));
    u32 prm[6];
#pragma unroll
    for (int i = 0; i < 6; i++) prm[i] = ldsr<u32>(i * 2048 + w * 256 + l * 4);
    bar();
    u32x4 ur[4];
    auto u_in = [&]() {
#pragma unroll
        for (int i = 0; i < 4; i++) ur[i] = *(const u32x4*)((const char*)u + (size_t)(256 * ((w + ia) & 7) + 4 * l + i) * 1280 + ia * 16);
    };
    if (UISS == 0) u_in();
    const float dt = lo2f(pk(expf(ldt), 0.f));
    STAMP(2);
    f32x16 kd = {};
    u32 rx, ry[2];
    {
        const int pc = 4 * (g & 1) + p;
        rx = LB + (8 * (g >> 1) + q) * 48 + (pc < 6 ? pc : 5) * 8;
#pragma unroll
        for (int rd = 0; rd < 2; rd++) ry[rd] = KB + (8 * (g >> 1) + 4 * rd + q) * 64 + (((pc >> 1) ^ ((2 * rd + (q >> 1)) & 3)) << 4) + (p & 1) * 8;
    }
    const u32 wx = LB + l * 48, wy = KB + l * 64, wsw = (l >> 1) & 3;
#pragma unroll
    for (int ps = 0; ps < 2; ps++) {
        float pf[6];
#pragma unroll
        for (int i = 0; i < 6; i++) pf[i] = ps ? hi2f(prm[i]) : lo2f(prm[i]);
        const float a = dt * pf[0], b = dt * pf[1];
        const f32x2 Wc = cmulc(f32x2{pf[4], -pf[5]}, f32x2{dt * pf[2], dt * pf[3]});
        const f32x2 e1 = cexp(a, b), e16 = cexp(16.f * a, 16.f * b), e256 = cexp(256.f * a, 256.f * b);
        const f32x2 e2 = csq(e1), e4 = csq(e2), e8 = csq(e4), e32 = csq(e16), e64 = csq(e32), e128 = csq(e64), e512 = csq(e256), e1024 = csq(e512);
        f32x2 X[24];
        X[0] = Wc; X[1] = cmulc(Wc, e1); X[2] = cmulc(Wc, e2); X[3] = cmulc(X[1], e2);
#pragma unroll
        for (int i = 0; i < 4; i++) X[4 + i] = cmulc(X[i], e4);
        X[8] = cmulc(Wc, e256); X[9] = cmulc(X[8], e2); X[10] = cmulc(X[8], e4);
        X[12] = X[10]; X[13] = cmulc(X[9], e4); X[14] = cmulc(X[8], e8);
        X[11] = cmulc(Wc, e512); X[16] = cmulc(X[11], e4); X[17] = cmulc(X[11], e8);
        X[15] = cmulc(X[8], e512); X[20] = cmulc(X[15], e4); X[21] = cmulc(X[15], e8);
        X[18] = cmulc(cmulc(Wc, e8), e1024); X[19] = cmulc(X[14], e1024);
        X[22] = cmulc(X[17], e1024); X[23] = cmulc(X[21], e1024);
        f32x2 Y[8];
        Y[0] = f32x2{1.f, 0.f}; Y[1] = e8; Y[2] = e16; Y[3] = cmul(e8, e16); Y[4] = e32; Y[5] = cmul(e8, e32); Y[6] = cmul(e16, e32); Y[7] = cmul(Y[3], e32);
        const f32x2 e192 = cmul(e64, e128);
#pragma unroll
        for (int ch = 0; ch < 4; ch++) {
            if (ch < 3) {
                const f32x2* Xc = X + 8 * ch;
                ldsw<u32x4>(wx + ch * 16, u32x4{pk(Xc[0].x, Xc[1].x), pk(Xc[2].x, Xc[3].x), pk(Xc[4].x, Xc[5].x), pk(Xc[6].x, Xc[7].x)});
                ldsw<u32x4>(wx + ch * 16 + 3072, u32x4{pk(Xc[0].y, Xc[1].y), pk(Xc[2].y, Xc[3].y), pk(Xc[4].y, Xc[5].y), pk(Xc[6].y, Xc[7].y)});
            }
            f32x2 Z[8];
            const f32x2 m = ch == 1 ? e64 : ch == 2 ? e128 : e192;
#pragma unroll
            for (int i = 0; i < 8; i++) Z[i] = ch == 0 ? Y[i] : i == 0 ? m : cmul(Y[i], m);
            const u32 o = wy + ((ch ^ wsw) << 4);
            ldsw<u32x4>(o, u32x4{pk(Z[0].x, Z[1].x), pk(Z[2].x, Z[3].x), pk(Z[4].x, Z[5].x), pk(Z[6].x, Z[7].x)});
            ldsw<u32x4>(o + 4096, u32x4{pk(Z[0].y, Z[1].y), pk(Z[2].y, Z[3].y), pk(Z[4].y, Z[5].y), pk(Z[6].y, Z[7].y)});
        }
        s16x4 fx[16], fy[16];
#pragma unroll
        for (int ks = 0; ks < 8; ks++) {
            fx[2 * ks] = trd(rx + ks * 768); fx[2 * ks + 1] = trd(rx + ks * 768 + 192);
            fy[2 * ks] = trd(ry[0] + ks * 1024); fy[2 * ks + 1] = trd(ry[1] + ks * 1024);
        }
        if (ps == 0) { STAMP(4); if (UISS == 1) u_in(); }
#pragma unroll
        for (int ks = 0; ks < 8; ks++) kd = mfma32(cat(fx[2 * ks], fx[2 * ks + 1]), cat(fy[2 * ks], fy[2 * ks + 1]), kd);
    }
    STAMP(6);
    constexpr int LR = 4096;
    {
        const int j = l & 31;
        ldsw<u32x2>(KB + kaddr(1052 - 8 * j - 4 * h), u32x2{pk(kd[3], kd[2]), pk(kd[1], l == 0 ? kd[0] + dsk : kd[0])});
        ldsw<u32x2>(KB + kaddr(1052 - 256 - 8 * j - 4 * h), u32x2{pk(kd[6], kd[5]), pk(kd[4], kd[4])});
        const u32 cc = pk(kd[9], kd[9]);
        ldsw<u32x4>(KB + kaddr(1048 - 512 - 256 * h - 8 * j), u32x4{cc, pk(kd[8], kd[8]), pk(kd[8], kd[7]), pk(kd[7], kd[7])});
        if (l >= 60) ldsw<u32x4>(KB + (l - 60) * 16, l == 63 ? u32x4{0u, cc & 0xffff0000u, cc, cc} : u32x4{0u, 0u, 0u, 0u});
        const u32 far = pk(kd[10], kd[11]);
        ldsw<u16>(LB + LR + (127 - 64 * h - j) * 2, (u16)far);
        ldsw<u16>(LB + LR + (95 - 64 * h - j) * 2, (u16)(far >> 16));
        if (l < 16) ldsw<u32>(LB + LR + 256 + l * 4, 0u);
        u32x4 v[3]; u32x2 n[3];
        u32 a0[3];
#pragma unroll
        for (int it = 0; it < 3; it++) {
            const u32 i = it < 2 ? 64 * it + l : 128 + (l & 3);
            a0[it] = KB + kaddr(8 * i);
            v[it] = ldsr<u32x4>(a0[it]); n[it] = ldsr<u32x2>(KB + kaddr(8 * i + 8));
        }
#pragma unroll
        for (int it = 0; it < 3; it++) {
            const u32 s0 = __builtin_amdgcn_alignbit(v[it][1], v[it][0], 16), s1 = __builtin_amdgcn_alignbit(v[it][2], v[it][1], 16),
                      s2 = __builtin_amdgcn_alignbit(v[it][3], v[it][2], 16), s3 = __builtin_amdgcn_alignbit(n[it][0], v[it][3], 16),
                      s4 = __builtin_amdgcn_alignbit(n[it][1], n[it][0], 16);
            if (it < 2 || l < 4) {
                ldsw<u32x4>(a0[it] + 64, u32x4{s0, s1, s2, s3});
                ldsw<u32x4>(a0[it] + 128, u32x4{v[it][1], v[it][2], v[it][3], n[it][0]});
                ldsw<u32x4>(a0[it] + 192, u32x4{s1, s2, s3, s4});
            }
        }
    }
    STAMP(5);
    {
        const u32 a = LU + (256 * ((w + ia) & 7) + 4 * l) * 2;
#pragma unroll
        for (int c2 = 0; c2 < 4; c2++) {
            ldsw<u32x2>(a + 2 * c2 * 4096, u32x2{__builtin_amdgcn_perm(ur[1][c2], ur[0][c2], 0x05040100u), __builtin_amdgcn_perm(ur[3][c2], ur[2][c2], 0x05040100u)});
            ldsw<u32x2>(a + (2 * c2 + 1) * 4096, u32x2{__builtin_amdgcn_perm(ur[1][c2], ur[0][c2], 0x07060302u), __builtin_amdgcn_perm(ur[3][c2], ur[2][c2], 0x07060302u)});
        }
    }
    STAMP(3);
    bar();
    STAMP(10);
    const int rr = l & 15;
    f32x4 y[4] = {};
    {
        float sm[36];
        sm[0] = 0.f;
#pragma unroll
        for (int i = 0; i < 5; i++) {
            u32x4 t = ldsr<u32x4>(UB + l * 32 - 48 + i * 16);
            if (i < 3 && 2 * l + i < 3) t = u32x4{0, 0, 0, 0};
#pragma unroll
            for (int e = 0; e < 8; e++)
                if (8 * i + e < 35) sm[8 * i + e + 1] = sm[8 * i + e] + ((e & 1) ? hi2f(t[e >> 1]) : lo2f(t[e >> 1]));
        }
        const u32 wb = LB + l * 64, sw = (l >> 1) & 3;
#pragma unroll
        for (int ch = 0; ch < 4; ch++) {
            const int o1 = ch < 2 ? 13 : 20, o0 = ch < 2 ? 4 : 13, b0 = 8 * (ch & 1);
            ldsw<u32x4>(wb + ((ch ^ sw) << 4), u32x4{pk(sm[b0 + o1] - sm[b0 + o0], sm[b0 + 1 + o1] - sm[b0 + 1 + o0]), pk(sm[b0 + 2 + o1] - sm[b0 + 2 + o0], sm[b0 + 3 + o1] - sm[b0 + 3 + o0]),
                                                     pk(sm[b0 + 4 + o1] - sm[b0 + 4 + o0], sm[b0 + 5 + o1] - sm[b0 + 5 + o0]), pk(sm[b0 + 6 + o1] - sm[b0 + 6 + o0], sm[b0 + 7 + o1] - sm[b0 + 7 + o0])});
        }
        s16x8 fw[4], ft[4];
#pragma unroll
        for (int s = 0; s < 4; s++) {
            s16x4 t[2];
#pragma unroll
            for (int rd = 0; rd < 2; rd++) {
                const int blk = 4 * g + 2 * rd + (q >> 1);
                t[rd] = trd(LB + s * 1024 + blk * 64 + (((2 * (q & 1) + (p >> 1)) ^ ((blk >> 1) & 3)) << 4) + (p & 1) * 8);
            }
            fw[s] = cat(t[0], t[1]);
            const u32 a = LB + LR + (126 - 32 * s - 2 * rr + 8 * g) * 2;
            ft[s] = frag(ldsr<u32>(a), ldsr<u32>(a + 4), ldsr<u32>(a + 8), ldsr<u32>(a + 12));
        }
#pragma unroll
        for (int s = 0; s < 4; s++)
#pragma unroll
            for (int ct = s; ct < 4; ct++) y[ct] = mfma16(fw[s], ft[ct - s], y[ct]);
    }
    STAMP(13);
    const u32 e0 = 1055 - 16 - rr + 8 * g, sh = e0 & 3;
    auto half = [&](auto HI) {
        constexpr bool hi = decltype(HI)::value;
        constexpr int T0 = hi ? 4 : 0, B0 = hi ? 1 : 0, NB = hi ? 7 : 4, NRD = NB + 8;
        u32 ka0 = KB + kaddr(e0 - sh) + sh * 64 - 3 * 2048, ka1 = KB + kaddr(e0 - sh + 4) + sh * 64 - 3 * 2048;
        u32 ua = UB + 32 * rr + 16 * g - 32;
        u32 mk[4];
#pragma unroll
        for (int i = 0; i < 4; i++) mk[i] = (2 * i > 16 + rr - 8 * g ? 0u : 0xffffu) | (2 * i + 1 > 16 + rr - 8 * g ? 0u : 0xffff0000u);
        int lim = 2 * rr + g - 2;
        u32x2 A[2][8]; u32x4 B[2][NB];
        auto rdc = [&](int s, int n) {
            if (n < 12) {
                const int m = n / 3, k = n % 3;
                if (k == 0) B[s][m] = ldsr<u32x4>(ua + (B0 + m) * 512);
                else if (k == 1) A[s][2 * (3 - m)] = ldsr<u32x2>(ka0 + m * 2048);
                else A[s][2 * (3 - m) + 1] = ldsr<u32x2>(ka1 + m * 2048);
            } else B[s][n - 8] = ldsr<u32x4>(ua + (B0 + n - 8) * 512);
        };
        auto group = [&](int s, bool next) {
            A[s][0] &= u32x2{mk[0], mk[1]}; A[s][1] &= u32x2{mk[2], mk[3]};
            if (!hi && lim < 0) B[s][0] = u32x4{0, 0, 0, 0};
            ka0 -= 256; ka1 -= 256; ua -= 64; lim -= 4;
            mk[0] = mk[1] = mk[2] = mk[3] = 0xffffffffu;
            asm volatile("" : "+v"(ka0), "+v"(ka1), "+v"(ua));
            int n = 0;
#pragma unroll
            for (int m = 3; m >= 0; m--)
#pragma unroll
                for (int t = 0; t < 4; t++) {
                    const int ct = T0 + t - m;
                    if (ct < 0) continue;
                    SB;
                    y[t] = mfma16(frag(A[s][2 * m][0], A[s][2 * m][1], A[s][2 * m + 1][0], A[s][2 * m + 1][1]), __builtin_bit_cast(s16x8, B[s][ct - B0]), y[t]);
                    SB;
                    if (next && n < NRD) rdc(s ^ 1, n);
                    n++;
                    if (!hi && next && (n == 1 || n == 6)) rdc(s ^ 1, n++);
                }
        };
        asm volatile("" : "+v"(ka0), "+v"(ka1), "+v"(ua));
#pragma unroll
        for (int n = 0; n < NRD; n++) rdc(0, n);
        if (hi) {
            const u32x2 x0 = ldsr<u32x2>(ka0 - 2048), x1 = ldsr<u32x2>(ka1 - 2048);
            u32x4 b0 = ldsr<u32x4>(ua);
            if (lim < 0) b0 = u32x4{0, 0, 0, 0};
            const s16x8 xa = frag(x0[0], x0[1], x1[0], x1[1]);
            y[0] = mfma16(xa, __builtin_bit_cast(s16x8, b0), y[0]);
#pragma unroll
            for (int t = 1; t < 4; t++) y[t] = mfma16(xa, __builtin_bit_cast(s16x8, B[0][t - 1]), y[t]);
        }
        if (hi) {
#pragma unroll 1
            for (int gg = 0; gg < 3; gg++) { group(0, true); group(1, true); }
            group(0, true); group(1, false);
        } else {
            u32 v[8];
#pragma unroll
            for (int c2 = 0; c2 < 8; c2++) v[c2] = ldsr<u32>(LU + c2 * 4096 + 2048 + tid * 4);
            const u32 op = (1024 + 2 * tid) * 1280 + 8 * ia;
            const u32 idx = tid, row = (idx * 6554u) >> 16, wof = row * 1120 + idx * 16;
            const char* wob = (const char*)wo + ((sbid() >> 3) & 7) * 160;
#pragma unroll
            for (int gg = 0; gg < 8; gg++) {
                if (gg == 0 || gg == 4) {
                    const u32 sel = gg ? 0x07060302u : 0x05040100u;
                    out16(out, op + (gg ? 1280 : 0), u32x4{__builtin_amdgcn_perm(v[1], v[0], sel), __builtin_amdgcn_perm(v[3], v[2], sel),
                                                           __builtin_amdgcn_perm(v[5], v[4], sel), __builtin_amdgcn_perm(v[7], v[6], sel)});
                }
                if (w < 5) {
                    constexpr int G0[9] = {0, 0, 2, 4, 6, 6, 8, 10, 11};
#pragma unroll
                    for (int gq = G0[gg]; gq < G0[gg + 1]; gq++) dma(gq * 5120 + w * 1024, wof, wob + gq * (32 * 1280));
                }
                group(gg & 1, gg < 7);
            }
        }
#pragma unroll
        for (int ct = 0; ct < 4; ct++) {
            ldsw<u32x2>(UB + (T0 + ct) * 512 + rr * 32 + g * 8, u32x2{pk(y[ct][0], y[ct][1]), pk(y[ct][2], y[ct][3])});
            y[ct] = f32x4{0.f, 0.f, 0.f, 0.f};
        }
        if (hi) STAMP(14); else STAMP(16);
        bar();
        if (hi) STAMP(15); else STAMP(17);
        if (!hi) {
            load_wo(wo, (sbid() >> 3) & 7, 11 + w, 20, 8, l);
            if (w >= 4 && !YLATE) store_rows(out, ia, 0, tid - 256);
        }
    };
    half(Flag<true>{});
    STAMP(11);
    half(Flag<false>{});
    STAMP(12);
    if (w >= 4 && !YLATE) waitv<4>(); else waitv<0>();
    STAMP(7);
    return tag;
}

__global__ void __launch_bounds__(512, 2)
fused_kernel(const u16* __restrict__ x, const u16* __restrict__ W, const u16* __restrict__ wphi, const u16* __restrict__ wo,
             u16* __restrict__ out, char* __restrict__ ws, const u16* __restrict__ u, const u16* __restrict__ Are,
             const u16* __restrict__ Aim, const u16* __restrict__ Bre, const u16* __restrict__ Bim, const u16* __restrict__ Cre,
             const u16* __restrict__ Cim, const u16* __restrict__ logdt, const u16* __restrict__ Dskip) {
    __shared__ __attribute__((aligned(16))) char smem[LDS_BYTES];
    asm volatile("" ::"s"((u32)(size_t)(LAS char*)smem));
    u32 pfd = 0;
    if (threadIdx.x < 12) {
        u64 pc; asm volatile("s_getpc_b64 %0" : "=s"(pc));
        asm volatile("global_load_dword %0, %1, %2" : "+v"(pfd) : "v"(threadIdx.x * 128), "s"(pc + min((sbid() >> 3) * 1536, CODE_PF_BYTES - 1536)));
    }
    asm volatile("" ::"s"(x), "s"(W), "s"(wphi), "s"(wo), "s"(out), "s"(ws), "s"(u), "s"(Are), "s"(Aim), "s"(Bre), "s"(Bim), "s"(Cre), "s"(Cim),
                 "s"(logdt), "s"(Dskip));
    const Role R = role_of(sbid());
    u64 tag;
    if (!R.is_a) {
        if (__builtin_amdgcn_readfirstlane(threadIdx.x) >= 384) loader_b<true>(x, W, wo, ws, R.r, R.c, __builtin_amdgcn_readfirstlane(threadIdx.x >> 6) - 6, pfd, tag STAMP_PASS);
    } else prologue_a(R.ia, Are, Aim, Bre, Bim, Cre, Cim, logdt, Dskip);
    u32 sel = R.nb;
    asm volatile("" : "+s"(sel));
    if ((sbid() >> 3) >= sel) {
        tag = path_a(u, wo, out, ws, R.ia, pfd STAMP_PASS);
    } else {
        tag = path_b(x, W, wphi, wo, out, ws, R.r, R.c, pfd STAMP_PASS);
    }
    out_proj(out, ws, tag, R.is_a ? R.ia : -1 STAMP_PASS);
}

extern "C" void kernel_launch(void* const* d_in, const int* in_sizes, int n_in, void* d_out, int out_size, void* d_ws, size_t ws_size,
                              hipStream_t stream) {
    const u16* const* in = (const u16* const*)d_in;
    fused_kernel<<<256, 512, 0, stream>>>(in[9], in[10], in[11], in[12], (u16*)d_out, (char*)d_ws, in[0], in[1], in[2], in[3], in[4], in[5],
                                          in[6], in[7], in[8]);
}
```

```cpp
#include <hip/hip_runtime.h>
#include <hip/hip_bf16.h>

typedef unsigned short u16;
typedef unsigned u32;
typedef unsigned long long u64;
typedef __attribute__((ext_vector_type(8))) short s16x8;
typedef __attribute__((ext_vector_type(4))) short s16x4;
typedef __attribute__((ext_vector_type(16))) float f32x16;
typedef __attribute__((ext_vector_type(4))) float f32x4;
typedef __attribute__((ext_vector_type(2))) float f32x2;
typedef __attribute__((ext_vector_type(4))) u32 u32x4;
typedef __attribute__((ext_vector_type(2))) u32 u32x2;
typedef __attribute__((ext_vector_type(2))) __bf16 bf16x2;
#define LAS __attribute__((address_space(3)))
#define GAS __attribute__((address_space(1)))
#define DI __device__ __forceinline__
#define SB __builtin_amdgcn_sched_barrier(0)
DI unsigned sbid() { return __builtin_amdgcn_workgroup_id_x(); }

#define PSLEEP 4
#define STW 0
#define YSLEEP 12
#define AUXA 0
#define AUXB 0
#define RMAP 1
#define YLATE 0
#define NPRE_ 8
#define NLD0 8
#define TLBW 0
#define PRE 6
#define LATE 1
#define UISS 1
#define UPOS 1
#define WOPOS 0
#define YMODE 0
constexpr int LDS_BYTES = 163840;
constexpr u32 WS_EPOCH = 0, WS_ACK = 4096, WS_F1 = 65536, WS_F2 = 131072, WS_PART = 524288, WS_O = 2u << 20, WS_END = 5u << 20;
constexpr u32 WS_STAMP = 16u << 20;
constexpr u32 kSpinCap = 1u << 18;
constexpr u32 CODE_PF_BYTES = 44032;

DI u32 pk(float a, float b) { f32x2 v = {a, b}; return __builtin_bit_cast(u32, __builtin_convertvector(v, bf16x2)); }
DI float lo2f(u32 x) { return __uint_as_float(x << 16); }
DI float hi2f(u32 x) { return __uint_as_float(x & 0xffff0000u); }
DI float b2f(u16 h) { return __uint_as_float((u32)h << 16); }
DI s16x4 trd(u32 a) { return __builtin_amdgcn_ds_read_tr16_b64_v4i16((LAS s16x4*)(size_t)a); }
DI s16x8 cat(s16x4 a, s16x4 b) { return __builtin_shufflevector(a, b, 0, 1, 2, 3, 4, 5, 6, 7); }
DI s16x8 frag(u32 a, u32 b, u32 c, u32 d) { u32x4 v = {a, b, c, d}; return __builtin_bit_cast(s16x8, v); }
template <class T> DI T ldsr(u32 a) { return *(LAS T*)(size_t)a; }
template <class T> DI void ldsw(u32 a, T v) { *(LAS T*)(size_t)a = v; }
template <int N> DI void waitv() { asm volatile("s_waitcnt vmcnt(%0)" ::"i"(N) : "memory"); }
DI void bar() { asm volatile("s_waitcnt lgkmcnt(0)\n\ts_barrier" ::: "memory"); }
DI f32x16 mfma32(s16x8 a, s16x8 b, f32x16 c) { return __builtin_amdgcn_mfma_f32_32x32x16_bf16(a, b, c, 0, 0, 0); }
DI f32x4 mfma16(s16x8 a, s16x8 b, f32x4 c) { return __builtin_amdgcn_mfma_f32_16x16x32_bf16(a, b, c, 0, 0, 0); }
DI float max3(float a, float b, float c) { float r; asm("v_max3_f32 %0, %1, %2, %3" : "=v"(r) : "v"(a), "v"(b), "v"(c)); return r; }
DI float min3(float a, float b, float c) { float r; asm("v_min3_f32 %0, %1, %2, %3" : "=v"(r) : "v"(a), "v"(b), "v"(c)); return r; }
DI float dot2(u32 x, u32 sel, float c) { return __builtin_amdgcn_fdot2_f32_bf16(__builtin_bit_cast(bf16x2, sel), __builtin_bit_cast(bf16x2, x), c, false); }
template <class R> DI void st128(u32x4 v, R rs, u32 voff, u32 soff, int) { __builtin_amdgcn_raw_buffer_store_b128(v, rs, voff + soff, 0, 16); }
DI void out16(u16* out, u32 off, u32x4 v) {
    __builtin_amdgcn_raw_buffer_store_b128(v, __builtin_amdgcn_make_buffer_rsrc((void*)out, 0, 2048 * 2560, 0x00020000), off * 2, 0, AUXA);
}
DI void out8(u16* out, u32 off, u32x2 v) {
    __builtin_amdgcn_raw_buffer_store_b64(v, __builtin_amdgcn_make_buffer_rsrc((void*)out, 0, 2048 * 2560, 0x00020000), off * 2, 0, AUXB);
}
DI void dma(u32 m0v, u32 voff, const void* sbase) {
    asm volatile("s_mov_b32 m0, %0\n\ts_nop 0\n\tglobal_load_lds_dwordx4 %1, %2" ::"s"(m0v), "v"(voff), "s"(sbase) : "memory");
}
DI void dma_m(u32 m0v, u32 voff, const void* sbase, u32 mlo, u32 mhi) {
    asm volatile("s_mov_b32 exec_lo, %3\n\ts_mov_b32 exec_hi, %4\n\ts_mov_b32 m0, %0\n\ts_nop 0\n\tglobal_load_lds_dwordx4 %1, %2\n\ts_mov_b64 exec, -1"
                 ::"s"(m0v), "v"(voff), "s"(sbase), "i"(mlo), "i"(mhi) : "memory");
}
#define STAMP(i)
#define STAMP_ARG
#define STAMP_PASS

DI void post_flag(char* fl, u64 tag, int tid, int n, int me) {
    if (tid < n) __hip_atomic_store((GAS u64*)(fl + tid * 256 + me * 8), tag, __ATOMIC_RELAXED, __HIP_MEMORY_SCOPE_AGENT);
}
DI void wait_flags(const char* fl, u64 tag, int l, int n) {
    const GAS u64* p = (const GAS u64*)(fl + (l < n ? l : 0) * 8);
    u64 v0 = __hip_atomic_load(p, __ATOMIC_RELAXED, __HIP_MEMORY_SCOPE_AGENT);
#pragma unroll 1
    for (u32 s = 0; s < kSpinCap; s++) {
        __builtin_amdgcn_s_sleep(PSLEEP);
        const u64 v1 = __hip_atomic_load(p, __ATOMIC_RELAXED, __HIP_MEMORY_SCOPE_AGENT);
        if (__all(v0 == tag)) break;
        v0 = v1;
    }
}

template <class F> DI void features(const float (&v)[20], const s16x8 (&wf)[2][3], u32 (&phi)[20], F filler) {
    u32 b[10];
#pragma unroll
    for (int i = 0; i < 10; i++) b[i] = pk(v[2 * i], v[2 * i + 1]);
    const s16x8 B0 = frag(b[0], b[1], b[2], b[3]), B1 = frag(b[4], b[5], b[6], b[7]), B2 = frag(b[8], b[9], 0, 0);
    f32x16 p0 = {}, p1 = {};
    p0 = mfma32(wf[0][0], B0, p0); p1 = mfma32(wf[1][0], B0, p1);
    p0 = mfma32(wf[0][1], B1, p0); p1 = mfma32(wf[1][1], B1, p1);
    p0 = mfma32(wf[0][2], B2, p0); p1 = mfma32(wf[1][2], B2, p1);
    SB; filler(); SB;
    asm volatile("s_nop 11" : "+v"(p0), "+v"(p1));
    float p[20];
#pragma unroll
    for (int i = 0; i < 20; i++) p[i] = i < 16 ? p0[i] : p1[i - 16];
    float mxa = max3(p[0], p[1], p[2]), mna = min3(p[0], p[1], p[2]), mxb = max3(p[3], p[4], p[5]), mnb = min3(p[3], p[4], p[5]);
#pragma unroll
    for (int i = 6; i < 18; i += 4) {
        mxa = max3(mxa, p[i], p[i + 1]); mna = min3(mna, p[i], p[i + 1]);
        mxb = max3(mxb, p[i + 2], p[i + 3]); mnb = min3(mnb, p[i + 2], p[i + 3]);
    }
    mxa = max3(mxa, p[18], p[19]); mna = min3(mna, p[18], p[19]);
    const float mx = max3(mxa, mxb, mxb), mn = min3(mna, mnb, mnb);
    const float L2E = 1.4426950408889634f;
    const float cx = -mx * L2E, cn = mn * L2E;
    float e[40], s1, s2;
#pragma unroll
    for (int i = 0; i < 20; i++) {
        e[i] = __builtin_amdgcn_exp2f(__builtin_fmaf(p[i], L2E, cx));
        e[20 + i] = __builtin_amdgcn_exp2f(__builtin_fmaf(p[i], -L2E, cn));
        if (i == 0) { s1 = e[0]; s2 = e[20]; } else { s1 += e[i]; s2 += e[20 + i]; }
    }
    const float r1 = __builtin_amdgcn_rcpf(s1), r2 = __builtin_amdgcn_rcpf(s2);
#pragma unroll
    for (int i = 0; i < 10; i++) { phi[i] = pk(e[2 * i] * r1, e[2 * i + 1] * r1); phi[10 + i] = pk(e[20 + 2 * i] * r2, e[21 + 2 * i] * r2); }
}

constexpr int WBLK = 960, WSLOT = 8 * WBLK, XSLOT = 24576;
constexpr int LW = XSLOT, LIMG = 102400, LMISC = LIMG + 2 * XSLOT, LZERO = LMISC + 3200, LTAB = LZERO + 64, TROW = 56, THEAD = 40 * TROW;
DI int xslot(int j) { return j % 3 == 0 ? 0 : LIMG + (j % 3 - 1) * XSLOT; }
static_assert(LTAB + 80 * TROW <= LDS_BYTES, "LDS map");

template <bool V> struct Flag { static constexpr bool value = V; };

struct Role { bool is_a; int r, c, ia; u32 nb; };
DI Role role_of(u32 bid) {
    const int xcd = bid & 7, j = bid >> 3;
    Role R;
    const int e = xcd & 1, m = xcd >> 1, nb = e ? 20 : 24, cq = e ? (j * 52) >> 8 : (j * 43) >> 8;
    R.r = __builtin_amdgcn_readfirstlane(e ? 6 + j - 5 * cq : j - 6 * cq); R.c = __builtin_amdgcn_readfirstlane(4 * m + cq);
    R.ia = __builtin_amdgcn_readfirstlane((RMAP == 1 ? (e ? 32 + 12 * m : 8 * m) : 20 * m + (e ? 8 : 0)) + j - nb);
    R.is_a = j >= nb; R.nb = nb;
    return R;
}

DI void load_wo(const u16* wo, int cb, int wi, int end, int nw, int l) {
    u32 off[5];
#pragma unroll
    for (int i = 0; i < 5; i++) { const u32 idx = 64 * i + l, row = (idx * 6554u) >> 16; off[i] = row * 1120 + idx * 16; }
#pragma unroll 1
    for (int gq = wi; gq < end; gq += nw) {
        const char* gp = (const char*)wo + cb * 160 + gq * (32 * 1280);
#pragma unroll
        for (int i = 0; i < 5; i++) dma(gq * 5120 + i * 1024, off[i], gp);
    }
}

DI void store_rows(u16* __restrict__ out, int ia, int t0, int tt) {
    u32x2 v[8];
#pragma unroll
    for (int c2 = 0; c2 < 8; c2++) v[c2] = ldsr<u32x2>(131072 + c2 * 4096 + t0 * 2 + tt * 8);
    const u32 op = (t0 + 4 * tt) * 1280 + 8 * ia;
#pragma unroll
    for (int i = 0; i < 4; i++) {
        const u32 sel = (i & 1) ? 0x07060302u : 0x05040100u;
        out16(out, op + i * 1280, u32x4{__builtin_amdgcn_perm(v[1][i >> 1], v[0][i >> 1], sel), __builtin_amdgcn_perm(v[3][i >> 1], v[2][i >> 1], sel),
                                        __builtin_amdgcn_perm(v[5][i >> 1], v[4][i >> 1], sel), __builtin_amdgcn_perm(v[7][i >> 1], v[6][i >> 1], sel)});
    }
}

DI void out_proj(u16* __restrict__ out, char* __restrict__ ws, u64 tag, int ia STAMP_ARG) {
    const int bid = sbid(), tid = threadIdx.x, l = tid & 63, w = __builtin_amdgcn_readfirstlane(tid >> 6);
    const int t = 4 * (bid & 7) + (bid >> 6), cb = (bid >> 3) & 7, rs = (t * 43) >> 7, sub = t - 3 * rs;
    bar();
    if (w >= 4) {
        if (YLATE && ia >= 0) {
            bar();
            __builtin_amdgcn_s_sleep(YSLEEP);
            store_rows(out, ia, 0, tid - 256);
        }
        return;
    }
    constexpr int NPRE = NPRE_;
    const auto rsw = __builtin_amdgcn_make_buffer_rsrc((void*)ws, 0, WS_END, 0x00020000);
    const int g = l >> 4, q = (l >> 2) & 3, p = l & 3, hp = g & 1, P = g >> 1;
    u32 b1 = (20 * hp + 8 * P + q) * 160 + p * 8, b2 = (80 * P + 20 * hp + 16 + q) * 160 + p * 8;
    u32 b1h = b1 + 51200, b2h = b2 + 51200;
    asm volatile("" : "+v"(b1), "+v"(b2), "+v"(b1h), "+v"(b2h));
    s16x4 wfr[NPRE][10];
    auto rdw = [&](int ks, int n) {
        const int G = ks / 5, i = ks % 5, tt = n >> 1, rd = n & 1;
        const u32 so = (G & 1) * 25600;
        wfr[ks % NPRE][n] = i < 4 ? trd((G >> 1 ? b1h : b1) + so + (40 * i + 4 * rd) * 160 + 32 * tt) : trd((G >> 1 ? b2h : b2) + so + 40 * rd * 160 + 32 * tt);
    };
#pragma unroll
    for (int ks = 0; ks < NPRE; ks++)
#pragma unroll
        for (int n = 0; n < 10; n++) rdw(ks, n);
    if (w == 0) wait_flags(ws + WS_F2 + (rs * 24 + sub * 8 + cb) * 256, tag, l, 16);
    bar();
    STAMP(8);
    const int ti = 16 * (w & 1) + (l & 15);
    const u32 ob = WS_O + (rs * 96 + 2 * sub + (w >> 1)) * 2560;
    const u32 v1 = P * 1024 + (hp * 32 + ti) * 16, v2 = 2048 + 2 * P * 15360 + (hp * 32 + ti) * 8;
    u32x4 of[16]; u32x2 ot[8];
    auto ldo = [&](int n) {
        const int G = n / 6, i = n % 6;
        if (i < 4) of[4 * G + i] = __builtin_amdgcn_raw_buffer_load_b128(rsw, v1, ob + (4 * G + i) * 15360, 16);
        else ot[2 * G + i - 4] = __builtin_amdgcn_raw_buffer_load_b64(rsw, v2, ob + (4 * G + i - 4) * 15360, 16);
    };
#pragma unroll
    for (int n = 0; n < NLD0; n++) ldo(n);
    if (bid == 0 && tid == 0) __hip_atomic_store((GAS u64*)(ws + WS_EPOCH), tag, __ATOMIC_RELAXED, __HIP_MEMORY_SCOPE_AGENT);
    f32x4 oc[5] = {};
#pragma unroll
    for (int ks = 0; ks < 20; ks++) {
        const int G = ks / 5, i = ks % 5;
        const s16x8 B = i < 4 ? __builtin_bit_cast(s16x8, of[4 * G + i]) : frag(ot[2 * G][0], ot[2 * G][1], ot[2 * G + 1][0], ot[2 * G + 1][1]);
#pragma unroll
        for (int tt = 0; tt < 5; tt++) {
            SB;
            oc[tt] = mfma16(cat(wfr[ks % NPRE][2 * tt], wfr[ks % NPRE][2 * tt + 1]), B, oc[tt]);
            SB;
            if (ks + NPRE < 20) { rdw(ks + NPRE, 2 * tt); rdw(ks + NPRE, 2 * tt + 1); }
            if ((tt == 0 || tt == 2) && NLD0 + 2 * ks + (tt >> 1) < 24) ldo(NLD0 + 2 * ks + (tt >> 1));
        }
    }
    SB;
    const u32 op = (64 * t + 16 * w + (l & 15)) * 1280 + 640 + 80 * cb + 4 * g;
#pragma unroll
    for (int tt = 0; tt < 5; tt++) out8(out, op + 16 * tt, u32x2{pk(oc[tt][0], oc[tt][1]), pk(oc[tt][2], oc[tt][3])});
    STAMP(9);
}

template <bool FIRST>
DI void loader_b(const u16* __restrict__ x, const u16* __restrict__ W, const u16* __restrict__ wo, char* __restrict__ ws, int r, int c, int lw, u32& pfd, u64& tag STAMP_ARG) {
    const int l = threadIdx.x & 63;
    int ph = r + (((c & 3) * 10) >> 2); if (ph >= 10) ph -= 10; if (ph >= 10) ph -= 10;
    const int pl = l >> 4, idx = l & 15, q3 = (idx * 11) >> 5;
    const int q = pl < 3 ? idx >> 2 : q3 & 3, sg = pl < 3 ? pl : idx - 3 * q3, ch = pl < 3 ? (idx & 3) + ((idx >> 1) & 1) : 2;
    const u32 wl = q * 3840 + sg * 1280 + ch * 16 + (sg == 0 ? ((c & 1) ? 0 : 160) : 80);
    const u32 xo0 = (l >> 3) * 1280 + (((l & 7) ^ (l >> 4)) << 4), xo1 = ((l >> 3) + 8) * 1280 + (((l & 7) ^ ((l >> 4) | 4)) << 4);
    u32 xr[2][3];
#pragma unroll
    for (int i = 0; i < 6; i++) { const int row = 192 * r + 96 * (i / 3) + 48 * lw + 16 * (i % 3); xr[i / 3][i % 3] = (row < 2048 ? row : row - 64) * 1280; }
    const char* wb = (const char*)W + c * 80 - 80 + lw * (16 * 3840);
    auto ld_x = [&](int hx) {
        const int jx = hx >> 1, hh = hx & 1;
        int jr = jx + ph; if (jr >= 10) jr -= 10;
        const char* g = (const char*)x + jr * 128;
        const u32 d = xslot(jx) + (96 * hh + 48 * lw) * 128;
#pragma unroll
        for (int i = 0; i < 3; i++) { dma(d + i * 2048, xo0, g + xr[hh][i]); dma(d + i * 2048 + 1024, xo1, g + xr[hh][i]); }
    };
    auto ld_w = [&](int i) {
        int ir = i + 2 * ph; if (ir >= 20) ir -= 20;
        const u32 d = LW + (i & 3) * WSLOT + lw * 4 * WBLK;
        const char* g = wb + ir * (32 * 3840);
#pragma unroll
        for (int b = 0; b < 4; b++) dma_m(d + b * WBLK, wl, g + b * (4 * 3840), 0xffffffffu, 0x0fffffffu);
    };
    if (FIRST) { STAMP(4); ld_w(0); STAMP(14); ld_x(0); ld_x(1); STAMP(15); return; }
    if (PRE >= 4) ld_w(1);
    if (PRE >= 5) ld_x(2);
    if (PRE >= 6) ld_x(3);
    if (PRE >= 7) ld_w(2);
    STAMP(19);
    asm volatile("s_load_dwordx2 %0, %1, 0\n\ts_waitcnt vmcnt(%2) lgkmcnt(0)" : "=&s"(tag) : "s"(ws + WS_EPOCH),
                 "i"(PRE == 3 ? 0 : PRE == 4 ? 4 : PRE == 5 ? 10 : PRE == 6 ? 16 : 20) : "memory");
    STAMP(1);
    bar();
    if (PRE < 4) ld_w(1);
    if (PRE < 5) ld_x(2);
    if (PRE < 6) ld_x(3);
    if (PRE < 7) ld_w(2);
    if (LATE == 0) { ld_w(3); ld_x(4); }
#pragma unroll
    for (int i = 0; i < 19; i++) {
        if (i == 0) waitv<LATE ? 16 : 26>(); else if (i == 2) waitv<26>();
        else if (i <= 15) waitv<20>(); else if (i == 16) waitv<14>(); else if (i == 17) waitv<4>(); else waitv<2>();
        bar();
        if (i == 0 && LATE) { ld_w(3); ld_x(4); }
        if (i + 5 < 20) ld_x(i + 5);
        if (i + 4 < 20) ld_w(i + 4);
        if (i == 17) {
            const u32 ln = __builtin_amdgcn_readfirstlane(min((sbid() >> 3) * 256 + lw * 64, 6208u)) + l;
            asm volatile("global_load_dword %0, %1, %2\n\tglobal_load_dword %0, %1, %3" : "+v"(pfd) : "v"(ln * 128), "s"(wo), "s"((const char*)wo + 128 * 128));
        }
    }
    bar();
    load_wo(wo, (sbid() >> 3) & 7, lw, 20, 2, l);
}

DI u64 path_b(const u16* __restrict__ x, const u16* __restrict__ W, const u16* __restrict__ wphi, const u16* __restrict__ wo,
               u16* __restrict__ out, char* __restrict__ ws, int r, int c, u32& pfd STAMP_ARG) {
    const int tid = threadIdx.x, l = tid & 63;
    const int w = __builtin_amdgcn_readfirstlane(tid >> 6);
    const auto rsw = __builtin_amdgcn_make_buffer_rsrc((void*)ws, 0, WS_END, 0x00020000);
    const int g = l >> 4, q = (l >> 2) & 3, p = l & 3, h = l >> 5;
    const int rq = 2 * (g & 1) + (p >> 1), hk = g >> 1;
    const bool same = hk == (p & 1);
    u64 tag;
    u32 phiq[20];
    f32x16 acc[4] = {};
    s16x8 wfq[2][3];
    if (w >= 6) {
        loader_b<false>(x, W, wo, ws, r, c, w - 6, pfd, tag STAMP_PASS);
        if (sbid() == 0) wait_flags(ws + WS_ACK + (w - 6) * 320, tag + 1, l, 40);
    } else {
        if (w < 4) dma_m(LMISC + w * 800, l * 16, (const char*)wphi + ((c ^ (w >> 1)) * 2 + (w & 1)) * 800, 0xffffffffu, 0x3ffffu);
        if (tid < 2) ldsw<u64>(LZERO + tid * 8, 0ull);
        u32 bA = LW + (g >> 1) * 2 * WBLK + q * 64 + (p & 1) * 32 + (g & 1) * 16 + (p >> 1) * 8;
        u32 bD = LW + (g >> 1) * 2 * WBLK + 768 + (3 * q + 2 * (g & 1) + (p >> 1)) * 16 + (p & 1) * 8;
        asm volatile("" : "+v"(bA), "+v"(bD));
        const int trow = 32 * w + (l & 31);
        s16x8 wfk[2][3];
        auto load_wf = [&](s16x8 (&wf)[2][3], u32 base) {
#pragma unroll
            for (int to = 0; to < 2; to++)
#pragma unroll
                for (int s = 0; s < 3; s++) {
                    s16x4 t[2];
#pragma unroll
                    for (int rd = 0; rd < 2; rd++) {
                        const int mu = 2 * s + rd;
                        if (mu < 5) {
                            int dq = mu + hk; if (mu == 4) dq = hk ? 0 : 4;
                            const bool ok = same && (to == 0 || rq == 0);
                            t[rd] = trd(ok ? base + hk * 800 + (4 * dq + q) * 40 + (4 * to + rq) * 8 : LZERO);
                        } else t[rd] = s16x4{0, 0, 0, 0};
                    }
                    wf[to][s] = cat(t[0], t[1]);
                }
        };
        asm volatile("s_load_dwordx2 %0, %1, 0\n\ts_waitcnt vmcnt(0) lgkmcnt(0)" : "=&s"(tag) : "s"(ws + WS_EPOCH), "v"(pfd) : "memory");
        bar();
        STAMP(1);
        const u32 sw = ((trow & 2) << 1) | ((trow & 4) >> 1) | (trow & 1);
        const u32 rowa = LIMG + h * XSLOT + trow * 128;
        auto gemm = [&](auto HV) {
            constexpr bool hv = decltype(HV)::value;
            constexpr int NB = hv ? 3 : 1, NM = hv ? 6 : 3, NR = hv ? 11 : 7;
            f32x16 vc[3] = {};
            u32 aX[4], aXh[4];
            const int tx = hv ? 32 * (w - 2) + (l & 31) : trow;
#pragma unroll
            for (int k = 0; k < 4; k++) {
                aX[k] = tx * 128 + (((2 * k + h) ^ ((tx >> 1) & 7)) << 4); aXh[k] = aX[k] + LIMG;
                asm volatile("" : "+v"(aX[k]), "+v"(aXh[k]));
            }
            s16x4 fa[2][8]; s16x8 fb[2][NB];
            auto rd = [&](int s, int i, int ks, int n) {
                const int nb = hv ? (n == 0 ? 1 : n == 9 ? 0 : n == 10 ? 2 : -1) : (n == 0 ? 0 : -1);
                if (nb >= 0) {
                    const int sl = (i >> 1) % 3, k = (i & 1) * 2 + ks;
                    fb[s][nb] = sl == 0 ? ldsr<s16x8>(aX[k] + nb * 8192) : ldsr<s16x8>(aXh[k] + (sl - 1) * XSLOT + nb * 8192);
                } else {
                    const int na = n <= 4 ? n - 1 : n <= 6 ? n + 1 : n - 3;
                    const u32 o = (i & 3) * WSLOT + ks * 4 * WBLK + (na & 1) * WBLK;
                    fa[s][na] = na < 6 ? trd(bA + o + (na >> 1) * 256) : trd(bD + o);
                }
            };
            auto mm = [&](int s, int t) {
                if (t < 3) { const int tt = t == 2 ? 3 : t; acc[tt] = mfma32(cat(fa[s][2 * tt], fa[s][2 * tt + 1]), fb[s][hv ? 1 : 0], acc[tt]); }
                else { const int nb = t == 3 ? 1 : t == 4 ? 0 : 2; vc[nb] = mfma32(cat(fa[s][4], fa[s][5]), fb[s][hv ? nb : 0], vc[nb]); }
            };
            auto halfstep = [&](int s, int i, int ks, bool next) {
#pragma unroll
                for (int t = 0; t < NM; t++) {
                    SB; mm(s, t); SB;
                    if (t == 0 && ks == 0) bar();
                    if (next)
#pragma unroll
                        for (int n = 3 * t; n < 3 * t + 3 && n < NR; n++) rd(s ^ 1, i, ks, n);
                }
            };
#pragma unroll
            for (int n = 0; n < NR; n++) rd(0, 0, 0, n);
#pragma unroll
            for (int i = 0; i < 20; i++) {
                halfstep(0, i, 1, true);
                if (i == 1) load_wf(wfk, LMISC);
                if (i == 2) load_wf(wfq, LMISC + 1600);
                halfstep(1, i + 1, 0, i < 19);
            }
            if (hv) {
#pragma unroll
                for (int nb = 0; nb < 3; nb++)
#pragma unroll
                    for (int mu = 0; mu < 4; mu++) {
                        const int pc = 10 + mu + h;
                        ldsw<u32x2>(rowa + (nb - 1) * 8192 + (((pc >> 1) ^ sw) << 4) + (pc & 1) * 8,
                                    u32x2{pk(vc[nb][4 * mu], vc[nb][4 * mu + 1]), pk(vc[nb][4 * mu + 2], vc[nb][4 * mu + 3])});
                    }
            }
        };
        if ((w & 3) >= 2) gemm(Flag<true>{}); else gemm(Flag<false>{});
        STAMP(2);
        float kr[20]; u32 phik[20];
#pragma unroll
        for (int i = 0; i < 20; i++) kr[i] = i < 16 ? acc[1][i] : acc[3][i - 12];
        features(kr, wfk, phik, [&]() {
            const int pc = h ? 10 : 14;
            ldsw<u32x2>(rowa + (((pc >> 1) ^ sw) << 4) + (pc & 1) * 8, u32x2{pk(acc[3][8], acc[3][9]), pk(acc[3][10], acc[3][11])});
            ldsw<u32x2>(rowa + ((7 ^ sw) << 4) + 8, u32x2{0x3f80u, 0u});
        });
        const u32 keep = 192 * r + 32 * w < 2048 ? 0xffffffffu : 0u;
#pragma unroll
        for (int ch = 0; ch < 5; ch++) ldsw<u32x4>(rowa + ((ch ^ sw) << 4), u32x4{phik[4 * ch], phik[4 * ch + 1], phik[4 * ch + 2], phik[4 * ch + 3]} & keep);
        STAMP(10);
    }
    tag += 1;
    bar();
    if (w < 4) {
        const int pcn = 4 * (g & 1) + p;
        const int pa = 10 + (pcn < 6 ? pcn : 5), pb = 5 * (w & 1) + (pcn < 5 ? pcn : 4);
        u32 aa[2], ab[2];
#pragma unroll
        for (int rd = 0; rd < 2; rd++) {
            const int row = 8 * (g >> 1) + 4 * rd + q;
            const u32 sw = ((row & 2) << 1) | ((row & 4) >> 1) | (row & 1);
            const u32 base = LIMG + (w >> 1) * XSLOT + row * 128;
            aa[rd] = base + (((pa >> 1) ^ sw) << 4) + (pa & 1) * 8;
            ab[rd] = base + (((pb >> 1) ^ sw) << 4) + (pb & 1) * 8;
            asm volatile("" : "+v"(aa[rd]), "+v"(ab[rd]));
        }
        s16x4 ta[24], tb[24];
#pragma unroll
        for (int ks = 0; ks < 12; ks++) {
            ta[2 * ks] = trd(aa[0] + ks * 2048); ta[2 * ks + 1] = trd(aa[1] + ks * 2048);
            tb[2 * ks] = trd(ab[0] + ks * 2048); tb[2 * ks + 1] = trd(ab[1] + ks * 2048);
        }
        SB;
        f32x16 kv = {};
#pragma unroll
        for (int ks = 0; ks < 12; ks++) kv = mfma32(cat(ta[2 * ks], ta[2 * ks + 1]), cat(tb[2 * ks], tb[2 * ks + 1]), kv);
        if ((l & 31) < 20) {
            const u32 lam = h * 20 + (l & 31), o = WS_PART + (c * 16 + r) * 4096 + w * 960;
            st128(u32x4{pk(kv[0], kv[1]), pk(kv[2], kv[3]), pk(kv[4], kv[5]), pk(kv[6], kv[7])}, rsw, lam * 16, o, 16);
            __builtin_amdgcn_raw_buffer_store_b64(u32x2{pk(kv[8], kv[9]), pk(kv[10], kv[11])}, rsw, 640 + lam * 8, o, 16);
        }
    }
    STAMP(11);
    auto qfeat = [&]() {
        float qr[20];
#pragma unroll
        for (int i = 0; i < 20; i++) qr[i] = i < 16 ? acc[0][i] : acc[3][i - 16];
        features(qr, wfq, phiq, []() {});
#pragma unroll
        for (int i = 0; i < 20; i++) asm volatile("" : "+v"(phiq[i]));
    };
#pragma unroll 1
    for (int st = 0; st < 2; st++) {
        if (w < 6 && (w < 4) == (st == 1)) qfeat();
        if (st == 1) break;
        if (w < 4) waitv<0>();
        bar();
        post_flag(ws + WS_F1 + c * 4096, tag, tid, 11, r);
        STAMP(3);
    }
    STAMP(17);
    if (tid < 80) ldsw<u64>(LTAB + tid * TROW + 48, 0ull);
    if (w == 6) wait_flags(ws + WS_F1 + (c ^ 1) * 4096 + r * 256, tag, l, 11);
    bar();
    STAMP(5);
    if (w < 4) {
        const int t = tid < 240 ? tid : 239;
        const int un = (t * 1093) >> 16, it = t - un * 60;
        u32x4 pv[11];
#pragma unroll
        for (int i = 0; i < 11; i++) pv[i] = __builtin_amdgcn_raw_buffer_load_b128(rsw, t * 16, WS_PART + (c ^ 1) * 65536 + i * 4096, 16);
        float s[8] = {};
        u32 slo = 0x00003f80u, shi = 0x3f800000u;
        asm volatile("" : "+s"(slo), "+s"(shi));
#pragma unroll
        for (int i = 0; i < 11; i++)
#pragma unroll
            for (int e = 0; e < 4; e++) { s[2 * e] = dot2(pv[i][e], slo, s[2 * e]); s[2 * e + 1] = dot2(pv[i][e], shi, s[2 * e + 1]); }
        const int lam = it < 40 ? it : 2 * (it - 40);
        const int hh = lam >= 20, f = lam - 20 * hh;
        const u32 a0 = LTAB + (un >> 1) * THEAD + ((un & 1) * 20 + f) * TROW + hh * 8 + (it < 40 ? 0 : 32);
        ldsw<u32x2>(a0, u32x2{pk(s[0], s[1]), pk(s[2], s[3])});
        ldsw<u32x2>(a0 + (it < 40 ? 16 : TROW), u32x2{pk(s[4], s[5]), pk(s[6], s[7])});
    }
    bar();
    STAMP(6);
    if (w < 6 && 192 * r + 32 * w < 2048) {
        const u32 tb = LTAB + hk * THEAD + q * TROW;
        u32 t0 = tb + (same ? rq : 6) * 8, t1 = tb + (same && rq < 2 ? 4 + rq : 6) * 8;
        asm volatile("" : "+v"(t0), "+v"(t1));
        f32x16 o0 = {}, o1 = {};
#pragma unroll
        for (int s = 0; s < 5; s++) {
            const s16x8 B = frag(phiq[4 * s], phiq[4 * s + 1], phiq[4 * s + 2], phiq[4 * s + 3]);
            o0 = mfma32(cat(trd(t0 + 8 * s * TROW), trd(t0 + (8 * s + 4) * TROW)), B, o0);
            o1 = mfma32(cat(trd(t1 + 8 * s * TROW), trd(t1 + (8 * s + 4) * TROW)), B, o1);
        }
        const float rden = __builtin_amdgcn_rcpf(o1[4] + 1e-6f);
        u32 ob[10];
#pragma unroll
        for (int i = 0; i < 10; i++) ob[i] = i < 8 ? pk(o0[2 * i] * rden, o0[2 * i + 1] * rden) : pk(o1[2 * i - 16] * rden, o1[2 * i - 15] * rden);
        const u32 o = WS_O + ((r * 16 + (c ^ 1)) * 6 + w) * 2560;
        st128(u32x4{ob[0], ob[1], ob[2], ob[3]}, rsw, l * 16, o, 16);
        st128(u32x4{ob[4], ob[5], ob[6], ob[7]}, rsw, l * 16 + 1024, o, 16);
        __builtin_amdgcn_raw_buffer_store_b64(u32x2{ob[8], ob[9]}, rsw, l * 8 + 2048, o, 16);
    }
    STAMP(12);
    waitv<0>();
    asm volatile("" ::"v"(pfd));
    bar();
    post_flag(ws + WS_F2 + r * (24 * 256), tag, tid, 24, c);
    STAMP(7);
    return tag;
}

DI f32x2 cmul(f32x2 a, f32x2 b) {
    f32x2 d;
    asm("v_pk_mul_f32 %0, %1, %2 op_sel_hi:[0,1]\n\tv_pk_fma_f32 %0, %1, %2, %0 op_sel:[1,1,0] op_sel_hi:[1,0,1] neg_lo:[0,1,0]" : "=&v"(d) : "v"(a), "v"(b));
    return d;
}
DI f32x2 cmulc(f32x2 a, f32x2 b) {
    f32x2 d;
    asm("v_pk_mul_f32 %0, %1, %2 op_sel_hi:[0,1] neg_hi:[0,1]\n\tv_pk_fma_f32 %0, %1, %2, %0 op_sel:[1,1,0] op_sel_hi:[1,0,1]" : "=&v"(d) : "v"(a), "v"(b));
    return d;
}
DI f32x2 csq(f32x2 a) { return cmul(a, a); }
DI f32x2 cexp(float a, float b) {
    const float m = __builtin_amdgcn_exp2f(a * 1.4426950408889634f);
    const float c1 = 0.15915494309189535f, c2 = 6.4206382e-9f;
    const float n = __builtin_rintf(b * c1);
    const float f = __builtin_fmaf(b, c2, __builtin_fmaf(b, c1, -n));
    return f32x2{m * __builtin_amdgcn_cosf(f), m * __builtin_amdgcn_sinf(f)};
}
constexpr int LOWB = 7168, LQ = 8 * LOWB, QB = 8448, LU = 131072, LPRM = LQ + 8 * QB;
static_assert(LPRM + 6144 <= LU, "LDS map");
DI u32 kaddr(u32 e) { return ((e >> 5) << 8) + ((e & 31) << 1); }

DI void prologue_a(int ia, const u16* __restrict__ Are, const u16* __restrict__ Aim, const u16* __restrict__ Bre, const u16* __restrict__ Bim,
                   const u16* __restrict__ Cre, const u16* __restrict__ Cim, const u16* __restrict__ logdt, const u16* __restrict__ Dskip) {
    const int l = threadIdx.x & 63, w = __builtin_amdgcn_readfirstlane(threadIdx.x >> 6);
    const u16* src = w == 0 ? Are : w == 1 ? Aim : w == 2 ? Bre : w == 3 ? Bim : w == 4 ? Cre : w == 5 ? Cim : w == 6 ? logdt : Dskip;
    if (w < 6) { dma(w * 2048, ia * 2048 + l * 16, src); dma(w * 2048 + 1024, ia * 2048 + 1024 + l * 16, src); }
    else dma_m(w * 2048, ia * 16, src, 1u, 0u);
}

DI u64 path_a(const u16* __restrict__ u, const u16* __restrict__ wo, u16* __restrict__ out, char* __restrict__ ws, int ia, u32 pfd STAMP_ARG) {
    const int tid = threadIdx.x, l = tid & 63;
    const int w = __builtin_amdgcn_readfirstlane(tid >> 6);
    const u32 LB = w * LOWB, KB = LQ + w * QB, UB = LU + w * 4096;
    const int g = l >> 4, q = (l >> 2) & 3, p = l & 3, h = l >> 5;
    STAMP(1);
    u64 tag;
    asm volatile("s_load_dwordx2 %0, %1, 0\n\ts_waitcnt vmcnt(0) lgkmcnt(0)" : "=&s"(tag) : "s"(ws + WS_EPOCH), "v"(pfd) : "memory");
    tag += 1;
    bar();
    if (tid == 0) __hip_atomic_store((GAS u64*)(ws + WS_ACK + ia * 8), tag, __ATOMIC_RELAXED, __HIP_MEMORY_SCOPE_AGENT);
    const float ldt = b2f(ldsr<u16>(6 * 2048 + w * 2)), dsk = b2f(ldsr<u16>(7 * 2048 + w * 2));
    u32 prm[6];
#pragma unroll
    for (int i = 0; i < 6; i++) prm[i] = ldsr<u32>(i * 2048 + w * 256 + l * 4);
    bar();
    u32x4 ur[4];
    auto u_in = [&]() {
#pragma unroll
        for (int i = 0; i < 4; i++) ur[i] = *(const u32x4*)((const char*)u + (size_t)(256 * ((w + ia) & 7) + 4 * l + i) * 1280 + ia * 16);
    };
    if (UISS == 0) u_in();
    const float dt = lo2f(pk(expf(ldt), 0.f));
    STAMP(2);
    f32x16 kd = {};
    u32 rx, ry[2];
    {
        const int pc = 4 * (g & 1) + p;
        rx = LB + (8 * (g >> 1) + q) * 48 + (pc < 6 ? pc : 5) * 8;
#pragma unroll
        for (int rd = 0; rd < 2; rd++) ry[rd] = KB + (8 * (g >> 1) + 4 * rd + q) * 64 + (((pc >> 1) ^ ((2 * rd + (q >> 1)) & 3)) << 4) + (p & 1) * 8;
    }
    const u32 wx = LB + l * 48, wy = KB + l * 64, wsw = (l >> 1) & 3;
#pragma unroll
    for (int ps = 0; ps < 2; ps++) {
        float pf[6];
#pragma unroll
        for (int i = 0; i < 6; i++) pf[i] = ps ? hi2f(prm[i]) : lo2f(prm[i]);
        const float a = dt * pf[0], b = dt * pf[1];
        const f32x2 Wc = cmulc(f32x2{pf[4], -pf[5]}, f32x2{dt * pf[2], dt * pf[3]});
        const f32x2 e1 = cexp(a, b), e16 = cexp(16.f * a, 16.f * b), e256 = cexp(256.f * a, 256.f * b);
        const f32x2 e2 = csq(e1), e4 = csq(e2), e8 = csq(e4), e32 = csq(e16), e64 = csq(e32), e128 = csq(e64), e512 = csq(e256), e1024 = csq(e512);
        f32x2 X[24];
        X[0] = Wc; X[1] = cmulc(Wc, e1); X[2] = cmulc(Wc, e2); X[3] = cmulc(X[1], e2);
#pragma unroll
        for (int i = 0; i < 4; i++) X[4 + i] = cmulc(X[i], e4);
        X[8] = cmulc(Wc, e256); X[9] = cmulc(X[8], e2); X[10] = cmulc(X[8], e4);
        X[12] = X[10]; X[13] = cmulc(X[9], e4); X[14] = cmulc(X[8], e8);
        X[11] = cmulc(Wc, e512); X[16] = cmulc(X[11], e4); X[17] = cmulc(X[11], e8);
        X[15] = cmulc(X[8], e512); X[20] = cmulc(X[15], e4); X[21] = cmulc(X[15], e8);
        X[18] = cmulc(cmulc(Wc, e8), e1024); X[19] = cmulc(X[14], e1024);
        X[22] = cmulc(X[17], e1024); X[23] = cmulc(X[21], e1024);
        f32x2 Y[8];
        Y[0] = f32x2{1.f, 0.f}; Y[1] = e8; Y[2] = e16; Y[3] = cmul(e8, e16); Y[4] = e32; Y[5] = cmul(e8, e32); Y[6] = cmul(e16, e32); Y[7] = cmul(Y[3], e32);
        const f32x2 e192 = cmul(e64, e128);
#pragma unroll
        for (int ch = 0; ch < 4; ch++) {
            if (ch < 3) {
                const f32x2* Xc = X + 8 * ch;
                ldsw<u32x4>(wx + ch * 16, u32x4{pk(Xc[0].x, Xc[1].x), pk(Xc[2].x, Xc[3].x), pk(Xc[4].x, Xc[5].x), pk(Xc[6].x, Xc[7].x)});
                ldsw<u32x4>(wx + ch * 16 + 3072, u32x4{pk(Xc[0].y, Xc[1].y), pk(Xc[2].y, Xc[3].y), pk(Xc[4].y, Xc[5].y), pk(Xc[6].y, Xc[7].y)});
            }
            f32x2 Z[8];
            const f32x2 m = ch == 1 ? e64 : ch == 2 ? e128 : e192;
#pragma unroll
            for (int i = 0; i < 8; i++) Z[i] = ch == 0 ? Y[i] : i == 0 ? m : cmul(Y[i], m);
            const u32 o = wy + ((ch ^ wsw) << 4);
            ldsw<u32x4>(o, u32x4{pk(Z[0].x, Z[1].x), pk(Z[2].x, Z[3].x), pk(Z[4].x, Z[5].x), pk(Z[6].x, Z[7].x)});
            ldsw<u32x4>(o + 4096, u32x4{pk(Z[0].y, Z[1].y), pk(Z[2].y, Z[3].y), pk(Z[4].y, Z[5].y), pk(Z[6].y, Z[7].y)});
        }
        s16x4 fx[16], fy[16];
#pragma unroll
        for (int ks = 0; ks < 8; ks++) {
            fx[2 * ks] = trd(rx + ks * 768); fx[2 * ks + 1] = trd(rx + ks * 768 + 192);
            fy[2 * ks] = trd(ry[0] + ks * 1024); fy[2 * ks + 1] = trd(ry[1] + ks * 1024);
        }
        if (ps == 0) { STAMP(4); if (UISS == 1) u_in(); }
#pragma unroll
        for (int ks = 0; ks < 8; ks++) kd = mfma32(cat(fx[2 * ks], fx[2 * ks + 1]), cat(fy[2 * ks], fy[2 * ks + 1]), kd);
    }
    STAMP(6);
    constexpr int LR = 4096;
    {
        const int j = l & 31;
        ldsw<u32x2>(KB + kaddr(1052 - 8 * j - 4 * h), u32x2{pk(kd[3], kd[2]), pk(kd[1], l == 0 ? kd[0] + dsk : kd[0])});
        ldsw<u32x2>(KB + kaddr(1052 - 256 - 8 * j - 4 * h), u32x2{pk(kd[6], kd[5]), pk(kd[4], kd[4])});
        const u32 cc = pk(kd[9], kd[9]);
        ldsw<u32x4>(KB + kaddr(1048 - 512 - 256 * h - 8 * j), u32x4{cc, pk(kd[8], kd[8]), pk(kd[8], kd[7]), pk(kd[7], kd[7])});
        if (l >= 60) ldsw<u32x4>(KB + (l - 60) * 16, l == 63 ? u32x4{0u, cc & 0xffff0000u, cc, cc} : u32x4{0u, 0u, 0u, 0u});
        const u32 far = pk(kd[10], kd[11]);
        ldsw<u16>(LB + LR + (127 - 64 * h - j) * 2, (u16)far);
        ldsw<u16>(LB + LR + (95 - 64 * h - j) * 2, (u16)(far >> 16));
        if (l < 16) ldsw<u32>(LB + LR + 256 + l * 4, 0u);
        u32x4 v[3]; u32x2 n[3];
        u32 a0[3];
#pragma unroll
        for (int it = 0; it < 3; it++) {
            const u32 i = it < 2 ? 64 * it + l : 128 + (l & 3);
            a0[it] = KB + kaddr(8 * i);
            v[it] = ldsr<u32x4>(a0[it]); n[it] = ldsr<u32x2>(KB + kaddr(8 * i + 8));
        }
#pragma unroll
        for (int it = 0; it < 3; it++) {
            const u32 s0 = __builtin_amdgcn_alignbit(v[it][1], v[it][0], 16), s1 = __builtin_amdgcn_alignbit(v[it][2], v[it][1], 16),
                      s2 = __builtin_amdgcn_alignbit(v[it][3], v[it][2], 16), s3 = __builtin_amdgcn_alignbit(n[it][0], v[it][3], 16),
                      s4 = __builtin_amdgcn_alignbit(n[it][1], n[it][0], 16);
            if (it < 2 || l < 4) {
                ldsw<u32x4>(a0[it] + 64, u32x4{s0, s1, s2, s3});
                ldsw<u32x4>(a0[it] + 128, u32x4{v[it][1], v[it][2], v[it][3], n[it][0]});
                ldsw<u32x4>(a0[it] + 192, u32x4{s1, s2, s3, s4});
            }
        }
    }
    STAMP(5);
    {
        const u32 a = LU + (256 * ((w + ia) & 7) + 4 * l) * 2;
#pragma unroll
        for (int c2 = 0; c2 < 4; c2++) {
            ldsw<u32x2>(a + 2 * c2 * 4096, u32x2{__builtin_amdgcn_perm(ur[1][c2], ur[0][c2], 0x05040100u), __builtin_amdgcn_perm(ur[3][c2], ur[2][c2], 0x05040100u)});
            ldsw<u32x2>(a + (2 * c2 + 1) * 4096, u32x2{__builtin_amdgcn_perm(ur[1][c2], ur[0][c2], 0x07060302u), __builtin_amdgcn_perm(ur[3][c2], ur[2][c2], 0x07060302u)});
        }
    }
    STAMP(3);
    bar();
    STAMP(10);
    const int rr = l & 15;
    f32x4 y[4] = {};
    {
        float sm[36];
        sm[0] = 0.f;
#pragma unroll
        for (int i = 0; i < 5; i++) {
            u32x4 t = ldsr<u32x4>(UB + l * 32 - 48 + i * 16);
            if (i < 3 && 2 * l + i < 3) t = u32x4{0, 0, 0, 0};
#pragma unroll
            for (int e = 0; e < 8; e++)
                if (8 * i + e < 35) sm[8 * i + e + 1] = sm[8 * i + e] + ((e & 1) ? hi2f(t[e >> 1]) : lo2f(t[e >> 1]));
        }
        const u32 wb = LB + l * 64, sw = (l >> 1) & 3;
#pragma unroll
        for (int ch = 0; ch < 4; ch++) {
            const int o1 = ch < 2 ? 13 : 20, o0 = ch < 2 ? 4 : 13, b0 = 8 * (ch & 1);
            ldsw<u32x4>(wb + ((ch ^ sw) << 4), u32x4{pk(sm[b0 + o1] - sm[b0 + o0], sm[b0 + 1 + o1] - sm[b0 + 1 + o0]), pk(sm[b0 + 2 + o1] - sm[b0 + 2 + o0], sm[b0 + 3 + o1] - sm[b0 + 3 + o0]),
                                                     pk(sm[b0 + 4 + o1] - sm[b0 + 4 + o0], sm[b0 + 5 + o1] - sm[b0 + 5 + o0]), pk(sm[b0 + 6 + o1] - sm[b0 + 6 + o0], sm[b0 + 7 + o1] - sm[b0 + 7 + o0])});
        }
        s16x8 fw[4], ft[4];
#pragma unroll
        for (int s = 0; s < 4; s++) {
            s16x4 t[2];
#pragma unroll
            for (int rd = 0; rd < 2; rd++) {
                const int blk = 4 * g + 2 * rd + (q >> 1);
                t[rd] = trd(LB + s * 1024 + blk * 64 + (((2 * (q & 1) + (p >> 1)) ^ ((blk >> 1) & 3)) << 4) + (p & 1) * 8);
            }
            fw[s] = cat(t[0], t[1]);
            const u32 a = LB + LR + (126 - 32 * s - 2 * rr + 8 * g) * 2;
            ft[s] = frag(ldsr<u32>(a), ldsr<u32>(a + 4), ldsr<u32>(a + 8), ldsr<u32>(a + 12));
        }
#pragma unroll
        for (int s = 0; s < 4; s++)
#pragma unroll
            for (int ct = s; ct < 4; ct++) y[ct] = mfma16(fw[s], ft[ct - s], y[ct]);
    }
    STAMP(13);
    const u32 e0 = 1055 - 16 - rr + 8 * g, sh = e0 & 3;
    auto half = [&](auto HI) {
        constexpr bool hi = decltype(HI)::value;
        constexpr int T0 = hi ? 4 : 0, B0 = hi ? 1 : 0, NB = hi ? 7 : 4, NRD = NB + 8;
        u32 ka0 = KB + kaddr(e0 - sh) + sh * 64 - 3 * 2048, ka1 = KB + kaddr(e0 - sh + 4) + sh * 64 - 3 * 2048;
        u32 ua = UB + 32 * rr + 16 * g - 32;
        u32 mk[4];
#pragma unroll
        for (int i = 0; i < 4; i++) mk[i] = (2 * i > 16 + rr - 8 * g ? 0u : 0xffffu) | (2 * i + 1 > 16 + rr - 8 * g ? 0u : 0xffff0000u);
        int lim = 2 * rr + g - 2;
        u32x2 A[2][8]; u32x4 B[2][NB];
        auto rdc = [&](int s, int n) {
            if (n < 12) {
                const int m = n / 3, k = n % 3;
                if (k == 0) B[s][m] = ldsr<u32x4>(ua + (B0 + m) * 512);
                else if (k == 1) A[s][2 * (3 - m)] = ldsr<u32x2>(ka0 + m * 2048);
                else A[s][2 * (3 - m) + 1] = ldsr<u32x2>(ka1 + m * 2048);
            } else B[s][n - 8] = ldsr<u32x4>(ua + (B0 + n - 8) * 512);
        };
        auto group = [&](int s, bool next) {
            A[s][0] &= u32x2{mk[0], mk[1]}; A[s][1] &= u32x2{mk[2], mk[3]};
            if (!hi && lim < 0) B[s][0] = u32x4{0, 0, 0, 0};
            ka0 -= 256; ka1 -= 256; ua -= 64; lim -= 4;
            mk[0] = mk[1] = mk[2] = mk[3] = 0xffffffffu;
            asm volatile("" : "+v"(ka0), "+v"(ka1), "+v"(ua));
            int n = 0;
#pragma unroll
            for (int m = 3; m >= 0; m--)
#pragma unroll
                for (int t = 0; t < 4; t++) {
                    const int ct = T0 + t - m;
                    if (ct < 0) continue;
                    SB;
                    y[t] = mfma16(frag(A[s][2 * m][0], A[s][2 * m][1], A[s][2 * m + 1][0], A[s][2 * m + 1][1]), __builtin_bit_cast(s16x8, B[s][ct - B0]), y[t]);
                    SB;
                    if (next && n < NRD) rdc(s ^ 1, n);
                    n++;
                    if (!hi && next && (n == 1 || n == 6)) rdc(s ^ 1, n++);
                }
        };
        asm volatile("" : "+v"(ka0), "+v"(ka1), "+v"(ua));
#pragma unroll
        for (int n = 0; n < NRD; n++) rdc(0, n);
        if (hi) {
            const u32x2 x0 = ldsr<u32x2>(ka0 - 2048), x1 = ldsr<u32x2>(ka1 - 2048);
            u32x4 b0 = ldsr<u32x4>(ua);
            if (lim < 0) b0 = u32x4{0, 0, 0, 0};
            const s16x8 xa = frag(x0[0], x0[1], x1[0], x1[1]);
            y[0] = mfma16(xa, __builtin_bit_cast(s16x8, b0), y[0]);
#pragma unroll
            for (int t = 1; t < 4; t++) y[t] = mfma16(xa, __builtin_bit_cast(s16x8, B[0][t - 1]), y[t]);
        }
        if (hi) {
#pragma unroll 1
            for (int gg = 0; gg < 3; gg++) { group(0, true); group(1, true); }
            group(0, true); group(1, false);
        } else {
            u32 v[8];
#pragma unroll
            for (int c2 = 0; c2 < 8; c2++) v[c2] = ldsr<u32>(LU + c2 * 4096 + 2048 + tid * 4);
            const u32 op = (1024 + 2 * tid) * 1280 + 8 * ia;
            const u32 idx = tid, row = (idx * 6554u) >> 16, wof = row * 1120 + idx * 16;
            const char* wob = (const char*)wo + ((sbid() >> 3) & 7) * 160;
#pragma unroll
            for (int gg = 0; gg < 8; gg++) {
                if (gg == 0 || gg == 4) {
                    const u32 sel = gg ? 0x07060302u : 0x05040100u;
                    out16(out, op + (gg ? 1280 : 0), u32x4{__builtin_amdgcn_perm(v[1], v[0], sel), __builtin_amdgcn_perm(v[3], v[2], sel),
                                                           __builtin_amdgcn_perm(v[5], v[4], sel), __builtin_amdgcn_perm(v[7], v[6], sel)});
                }
                if (w < 5) {
                    constexpr int G0[9] = {0, 0, 2, 4, 6, 6, 8, 10, 11};
#pragma unroll
                    for (int gq = G0[gg]; gq < G0[gg + 1]; gq++) dma(gq * 5120 + w * 1024, wof, wob + gq * (32 * 1280));
                }
                group(gg & 1, gg < 7);
            }
        }
#pragma unroll
        for (int ct = 0; ct < 4; ct++) {
            ldsw<u32x2>(UB + (T0 + ct) * 512 + rr * 32 + g * 8, u32x2{pk(y[ct][0], y[ct][1]), pk(y[ct][2], y[ct][3])});
            y[ct] = f32x4{0.f, 0.f, 0.f, 0.f};
        }
        if (hi) STAMP(14); else STAMP(16);
        bar();
        if (hi) STAMP(15); else STAMP(17);
        if (!hi) {
            load_wo(wo, (sbid() >> 3) & 7, 11 + w, 20, 8, l);
            if (w >= 4 && !YLATE) store_rows(out, ia, 0, tid - 256);
        }
    };
    half(Flag<true>{});
    STAMP(11);
    half(Flag<false>{});
    STAMP(12);
    if (w >= 4 && !YLATE) waitv<4>(); else waitv<0>();
    STAMP(7);
    return tag;
}

__global__ void __launch_bounds__(512, 2)
fused_kernel(const u16* __restrict__ x, const u16* __restrict__ W, const u16* __restrict__ wphi, const u16* __restrict__ wo,
             u16* __restrict__ out, char* __restrict__ ws, const u16* __restrict__ u, const u16* __restrict__ Are,
             const u16* __restrict__ Aim, const u16* __restrict__ Bre, const u16* __restrict__ Bim, const u16* __restrict__ Cre,
             const u16* __restrict__ Cim, const u16* __restrict__ logdt, const u16* __restrict__ Dskip) {
    __shared__ __attribute__((aligned(16))) char smem[LDS_BYTES];
    asm volatile("" ::"s"((u32)(size_t)(LAS char*)smem));
    u64 tst; asm volatile("s_memrealtime %0\n\ts_waitcnt lgkmcnt(0)" : "=s"(tst));
    if (threadIdx.x == 64) __hip_atomic_store((GAS u64*)(ws + (32u << 20) + sbid() * 8), tst, __ATOMIC_RELAXED, __HIP_MEMORY_SCOPE_AGENT);
    u32 pfd = 0;
    if (threadIdx.x < 12) {
        u64 pc; asm volatile("s_getpc_b64 %0" : "=s"(pc));
        asm volatile("global_load_dword %0, %1, %2" : "+v"(pfd) : "v"(threadIdx.x * 128), "s"(pc + min((sbid() >> 3) * 1536, CODE_PF_BYTES - 1536)));
    }
    asm volatile("" ::"s"(x), "s"(W), "s"(wphi), "s"(wo), "s"(out), "s"(ws), "s"(u), "s"(Are), "s"(Aim), "s"(Bre), "s"(Bim), "s"(Cre), "s"(Cim),
                 "s"(logdt), "s"(Dskip));
    const Role R = role_of(sbid());
    u64 tag;
    if (!R.is_a) {
        if (__builtin_amdgcn_readfirstlane(threadIdx.x) >= 384) loader_b<true>(x, W, wo, ws, R.r, R.c, __builtin_amdgcn_readfirstlane(threadIdx.x >> 6) - 6, pfd, tag STAMP_PASS);
    } else prologue_a(R.ia, Are, Aim, Bre, Bim, Cre, Cim, logdt, Dskip);
    u32 sel = R.nb;
    asm volatile("" : "+s"(sel));
    if ((sbid() >> 3) >= sel) {
        tag = path_a(u, wo, out, ws, R.ia, pfd STAMP_PASS);
    } else {
        tag = path_b(x, W, wphi, wo, out, ws, R.r, R.c, pfd STAMP_PASS);
    }
    out_proj(out, ws, tag, R.is_a ? R.ia : -1 STAMP_PASS);
    if (sbid() == 0 && threadIdx.x < 64) {
        u64 mn = ~0ull, mx = 0;
        for (int i = 0; i < 4; i++) { const u64 v = __hip_atomic_load((GAS u64*)(ws + (32u << 20) + (i * 64 + threadIdx.x) * 8), __ATOMIC_RELAXED, __HIP_MEMORY_SCOPE_AGENT); mn = v < mn ? v : mn; mx = v > mx ? v : mx; }
        for (int o = 32; o; o >>= 1) { const u64 a = __shfl_xor(mn, o), b = __shfl_xor(mx, o); mn = a < mn ? a : mn; mx = b > mx ? b : mx; }
        u64 now; asm volatile("s_memrealtime %0\n\ts_waitcnt lgkmcnt(0)" : "=s"(now)::"memory");
        const u64 until = now + 500 + 10 * (0);
        do { asm volatile("s_memrealtime %0\n\ts_waitcnt lgkmcnt(0)" : "=s"(now)::"memory"); } while (now < until && now - tst < 100000);
    }
}

extern "C" void kernel_launch(void* const* d_in, const int* in_sizes, int n_in, void* d_out, int out_size, void* d_ws, size_t ws_size,
                              hipStream_t stream) {
    const u16* const* in = (const u16* const*)d_in;
    fused_kernel<<<256, 512, 0, stream>>>(in[9], in[10], in[11], in[12], (u16*)d_out, (char*)d_ws, in[0], in[1], in[2], in[3], in[4], in[5],
                                          in[6], in[7], in[8]);
}
```

```cpp
#include <hip/hip_runtime.h>
#include <hip/hip_bf16.h>

typedef unsigned short u16;
typedef unsigned u32;
typedef unsigned long long u64;
typedef __attribute__((ext_vector_type(8))) short s16x8;
typedef __attribute__((ext_vector_type(4))) short s16x4;
typedef __attribute__((ext_vector_type(16))) float f32x16;
typedef __attribute__((ext_vector_type(4))) float f32x4;
typedef __attribute__((ext_vector_type(2))) float f32x2;
typedef __attribute__((ext_vector_type(4))) u32 u32x4;
typedef __attribute__((ext_vector_type(2))) u32 u32x2;
typedef __attribute__((ext_vector_type(2))) __bf16 bf16x2;
#define LAS __attribute__((address_space(3)))
#define GAS __attribute__((address_space(1)))
#define DI __device__ __forceinline__
#define SB __builtin_amdgcn_sched_barrier(0)
DI unsigned sbid() { return __builtin_amdgcn_workgroup_id_x(); }

#define PSLEEP 4
#define STW 0
#define YSLEEP 12
#define AUXA 0
#define AUXB 0
#define RMAP 1
#define YLATE 0
#define NPRE_ 8
#define NLD0 8
#define WBL 0
#define PRE 6
#define LATE 1
#define UISS 1
#define UPOS 1
#define WOPOS 0
#define YMODE 0
constexpr int LDS_BYTES = 163840;
constexpr u32 WS_EPOCH = 0, WS_ACK = 4096, WS_F1 = 65536, WS_F2 = 131072, WS_PART = 524288, WS_O = 2u << 20, WS_END = 5u << 20;
constexpr u32 WS_STAMP = 16u << 20;
constexpr u32 kSpinCap = 1u << 18;
constexpr u32 CODE_PF_BYTES = 44032;

DI u32 pk(float a, float b) { f32x2 v = {a, b}; return __builtin_bit_cast(u32, __builtin_convertvector(v, bf16x2)); }
DI float lo2f(u32 x) { return __uint_as_float(x << 16); }
DI float hi2f(u32 x) { return __uint_as_float(x & 0xffff0000u); }
DI float b2f(u16 h) { return __uint_as_float((u32)h << 16); }
DI s16x4 trd(u32 a) { return __builtin_amdgcn_ds_read_tr16_b64_v4i16((LAS s16x4*)(size_t)a); }
DI s16x8 cat(s16x4 a, s16x4 b) { return __builtin_shufflevector(a, b, 0, 1, 2, 3, 4, 5, 6, 7); }
DI s16x8 frag(u32 a, u32 b, u32 c, u32 d) { u32x4 v = {a, b, c, d}; return __builtin_bit_cast(s16x8, v); }
template <class T> DI T ldsr(u32 a) { return *(LAS T*)(size_t)a; }
template <class T> DI void ldsw(u32 a, T v) { *(LAS T*)(size_t)a = v; }
template <int N> DI void waitv() { asm volatile("s_waitcnt vmcnt(%0)" ::"i"(N) : "memory"); }
DI void bar() { asm volatile("s_waitcnt lgkmcnt(0)\n\ts_barrier" ::: "memory"); }
DI f32x16 mfma32(s16x8 a, s16x8 b, f32x16 c) { return __builtin_amdgcn_mfma_f32_32x32x16_bf16(a, b, c, 0, 0, 0); }
DI f32x4 mfma16(s16x8 a, s16x8 b, f32x4 c) { return __builtin_amdgcn_mfma_f32_16x16x32_bf16(a, b, c, 0, 0, 0); }
DI float max3(float a, float b, float c) { float r; asm("v_max3_f32 %0, %1, %2, %3" : "=v"(r) : "v"(a), "v"(b), "v"(c)); return r; }
DI float min3(float a, float b, float c) { float r; asm("v_min3_f32 %0, %1, %2, %3" : "=v"(r) : "v"(a), "v"(b), "v"(c)); return r; }
DI float dot2(u32 x, u32 sel, float c) { return __builtin_amdgcn_fdot2_f32_bf16(__builtin_bit_cast(bf16x2, sel), __builtin_bit_cast(bf16x2, x), c, false); }
template <class R> DI void st128(u32x4 v, R rs, u32 voff, u32 soff, int) { __builtin_amdgcn_raw_buffer_store_b128(v, rs, voff + soff, 0, 16); }
DI void out16(u16* out, u32 off, u32x4 v) {
    __builtin_amdgcn_raw_buffer_store_b128(v, __builtin_amdgcn_make_buffer_rsrc((void*)out, 0, 2048 * 2560, 0x00020000), off * 2, 0, AUXA);
}
DI void out8(u16* out, u32 off, u32x2 v) {
    __builtin_amdgcn_raw_buffer_store_b64(v, __builtin_amdgcn_make_buffer_rsrc((void*)out, 0, 2048 * 2560, 0x00020000), off * 2, 0, AUXB);
}
DI void dma(u32 m0v, u32 voff, const void* sbase) {
    asm volatile("s_mov_b32 m0, %0\n\ts_nop 0\n\tglobal_load_lds_dwordx4 %1, %2" ::"s"(m0v), "v"(voff), "s"(sbase) : "memory");
}
DI void dma_m(u32 m0v, u32 voff, const void* sbase, u32 mlo, u32 mhi) {
    asm volatile("s_mov_b32 exec_lo, %3\n\ts_mov_b32 exec_hi, %4\n\ts_mov_b32 m0, %0\n\ts_nop 0\n\tglobal_load_lds_dwordx4 %1, %2\n\ts_mov_b64 exec, -1"
                 ::"s"(m0v), "v"(voff), "s"(sbase), "i"(mlo), "i"(mhi) : "memory");
}
#define STAMP(i)
#define STAMP_ARG
#define STAMP_PASS

DI void post_flag(char* fl, u64 tag, int tid, int n, int me) {
    if (tid < n) __hip_atomic_store((GAS u64*)(fl + tid * 256 + me * 8), tag, __ATOMIC_RELAXED, __HIP_MEMORY_SCOPE_AGENT);
}
DI void wait_flags(const char* fl, u64 tag, int l, int n) {
    const GAS u64* p = (const GAS u64*)(fl + (l < n ? l : 0) * 8);
    u64 v0 = __hip_atomic_load(p, __ATOMIC_RELAXED, __HIP_MEMORY_SCOPE_AGENT);
#pragma unroll 1
    for (u32 s = 0; s < kSpinCap; s++) {
        __builtin_amdgcn_s_sleep(PSLEEP);
        const u64 v1 = __hip_atomic_load(p, __ATOMIC_RELAXED, __HIP_MEMORY_SCOPE_AGENT);
        if (__all(v0 == tag)) break;
        v0 = v1;
    }
}

template <class F> DI void features(const float (&v)[20], const s16x8 (&wf)[2][3], u32 (&phi)[20], F filler) {
    u32 b[10];
#pragma unroll
    for (int i = 0; i < 10; i++) b[i] = pk(v[2 * i], v[2 * i + 1]);
    const s16x8 B0 = frag(b[0], b[1], b[2], b[3]), B1 = frag(b[4], b[5], b[6], b[7]), B2 = frag(b[8], b[9], 0, 0);
    f32x16 p0 = {}, p1 = {};
    p0 = mfma32(wf[0][0], B0, p0); p1 = mfma32(wf[1][0], B0, p1);
    p0 = mfma32(wf[0][1], B1, p0); p1 = mfma32(wf[1][1], B1, p1);
    p0 = mfma32(wf[0][2], B2, p0); p1 = mfma32(wf[1][2], B2, p1);
    SB; filler(); SB;
    asm volatile("s_nop 11" : "+v"(p0), "+v"(p1));
    float p[20];
#pragma unroll
    for (int i = 0; i < 20; i++) p[i] = i < 16 ? p0[i] : p1[i - 16];
    float mxa = max3(p[0], p[1], p[2]), mna = min3(p[0], p[1], p[2]), mxb = max3(p[3], p[4], p[5]), mnb = min3(p[3], p[4], p[5]);
#pragma unroll
    for (int i = 6; i < 18; i += 4) {
        mxa = max3(mxa, p[i], p[i + 1]); mna = min3(mna, p[i], p[i + 1]);
        mxb = max3(mxb, p[i + 2], p[i + 3]); mnb = min3(mnb, p[i + 2], p[i + 3]);
    }
    mxa = max3(mxa, p[18], p[19]); mna = min3(mna, p[18], p[19]);
    const float mx = max3(mxa, mxb, mxb), mn = min3(mna, mnb, mnb);
    const float L2E = 1.4426950408889634f;
    const float cx = -mx * L2E, cn = mn * L2E;
    float e[40], s1, s2;
#pragma unroll
    for (int i = 0; i < 20; i++) {
        e[i] = __builtin_amdgcn_exp2f(__builtin_fmaf(p[i], L2E, cx));
        e[20 + i] = __builtin_amdgcn_exp2f(__builtin_fmaf(p[i], -L2E, cn));
        if (i == 0) { s1 = e[0]; s2 = e[20]; } else { s1 += e[i]; s2 += e[20 + i]; }
    }
    const float r1 = __builtin_amdgcn_rcpf(s1), r2 = __builtin_amdgcn_rcpf(s2);
#pragma unroll
    for (int i = 0; i < 10; i++) { phi[i] = pk(e[2 * i] * r1, e[2 * i + 1] * r1); phi[10 + i] = pk(e[20 + 2 * i] * r2, e[21 + 2 * i] * r2); }
}

constexpr int WBLK = 960, WSLOT = 8 * WBLK, XSLOT = 24576;
constexpr int LW = XSLOT, LIMG = 102400, LMISC = LIMG + 2 * XSLOT, LZERO = LMISC + 3200, LTAB = LZERO + 64, TROW = 56, THEAD = 40 * TROW;
DI int xslot(int j) { return j % 3 == 0 ? 0 : LIMG + (j % 3 - 1) * XSLOT; }
static_assert(LTAB + 80 * TROW <= LDS_BYTES, "LDS map");

template <bool V> struct Flag { static constexpr bool value = V; };

struct Role { bool is_a; int r, c, ia; u32 nb; };
DI Role role_of(u32 bid) {
    const int xcd = bid & 7, j = bid >> 3;
    Role R;
    const int e = xcd & 1, m = xcd >> 1, nb = e ? 20 : 24, cq = e ? (j * 52) >> 8 : (j * 43) >> 8;
    R.r = __builtin_amdgcn_readfirstlane(e ? 6 + j - 5 * cq : j - 6 * cq); R.c = __builtin_amdgcn_readfirstlane(4 * m + cq);
    R.ia = __builtin_amdgcn_readfirstlane((RMAP == 1 ? (e ? 32 + 12 * m : 8 * m) : 20 * m + (e ? 8 : 0)) + j - nb);
    R.is_a = j >= nb; R.nb = nb;
    return R;
}

DI void load_wo(const u16* wo, int cb, int wi, int end, int nw, int l) {
    u32 off[5];
#pragma unroll
    for (int i = 0; i < 5; i++) { const u32 idx = 64 * i + l, row = (idx * 6554u) >> 16; off[i] = row * 1120 + idx * 16; }
#pragma unroll 1
    for (int gq = wi; gq < end; gq += nw) {
        const char* gp = (const char*)wo + cb * 160 + gq * (32 * 1280);
#pragma unroll
        for (int i = 0; i < 5; i++) dma(gq * 5120 + i * 1024, off[i], gp);
    }
}

DI void store_rows(u16* __restrict__ out, int ia, int t0, int tt) {
    u32x2 v[8];
#pragma unroll
    for (int c2 = 0; c2 < 8; c2++) v[c2] = ldsr<u32x2>(131072 + c2 * 4096 + t0 * 2 + tt * 8);
    const u32 op = (t0 + 4 * tt) * 1280 + 8 * ia;
#pragma unroll
    for (int i = 0; i < 4; i++) {
        const u32 sel = (i & 1) ? 0x07060302u : 0x05040100u;
        out16(out, op + i * 1280, u32x4{__builtin_amdgcn_perm(v[1][i >> 1], v[0][i >> 1], sel), __builtin_amdgcn_perm(v[3][i >> 1], v[2][i >> 1], sel),
                                        __builtin_amdgcn_perm(v[5][i >> 1], v[4][i >> 1], sel), __builtin_amdgcn_perm(v[7][i >> 1], v[6][i >> 1], sel)});
    }
}

DI void out_proj(u16* __restrict__ out, char* __restrict__ ws, u64 tag, int ia STAMP_ARG) {
    const int bid = sbid(), tid = threadIdx.x, l = tid & 63, w = __builtin_amdgcn_readfirstlane(tid >> 6);
    const int t = 4 * (bid & 7) + (bid >> 6), cb = (bid >> 3) & 7, rs = (t * 43) >> 7, sub = t - 3 * rs;
    bar();
    if (w >= 4) {
        if (YLATE && ia >= 0) {
            bar();
            __builtin_amdgcn_s_sleep(YSLEEP);
            store_rows(out, ia, 0, tid - 256);
        }
        if ((WBL == 1 && ia >= 0 && w == 7) || (WBL == 2 && ia >= 0 && (ia & 3) == 0 && w == 7)) asm volatile("s_waitcnt vmcnt(0)\n\ts_sleep 4\n\tbuffer_wbl2 sc1" ::: "memory");
        if (WBL == 3 && ia >= 0 && w == 7) asm volatile("s_waitcnt vmcnt(0)\n\ts_sleep 4\n\tbuffer_wbl2 sc0 sc1" ::: "memory");
        return;
    }
    constexpr int NPRE = NPRE_;
    const auto rsw = __builtin_amdgcn_make_buffer_rsrc((void*)ws, 0, WS_END, 0x00020000);
    const int g = l >> 4, q = (l >> 2) & 3, p = l & 3, hp = g & 1, P = g >> 1;
    u32 b1 = (20 * hp + 8 * P + q) * 160 + p * 8, b2 = (80 * P + 20 * hp + 16 + q) * 160 + p * 8;
    u32 b1h = b1 + 51200, b2h = b2 + 51200;
    asm volatile("" : "+v"(b1), "+v"(b2), "+v"(b1h), "+v"(b2h));
    s16x4 wfr[NPRE][10];
    auto rdw = [&](int ks, int n) {
        const int G = ks / 5, i = ks % 5, tt = n >> 1, rd = n & 1;
        const u32 so = (G & 1) * 25600;
        wfr[ks % NPRE][n] = i < 4 ? trd((G >> 1 ? b1h : b1) + so + (40 * i + 4 * rd) * 160 + 32 * tt) : trd((G >> 1 ? b2h : b2) + so + 40 * rd * 160 + 32 * tt);
    };
#pragma unroll
    for (int ks = 0; ks < NPRE; ks++)
#pragma unroll
        for (int n = 0; n < 10; n++) rdw(ks, n);
    if (w == 0) wait_flags(ws + WS_F2 + (rs * 24 + sub * 8 + cb) * 256, tag, l, 16);
    bar();
    STAMP(8);
    const int ti = 16 * (w & 1) + (l & 15);
    const u32 ob = WS_O + (rs * 96 + 2 * sub + (w >> 1)) * 2560;
    const u32 v1 = P * 1024 + (hp * 32 + ti) * 16, v2 = 2048 + 2 * P * 15360 + (hp * 32 + ti) * 8;
    u32x4 of[16]; u32x2 ot[8];
    auto ldo = [&](int n) {
        const int G = n / 6, i = n % 6;
        if (i < 4) of[4 * G + i] = __builtin_amdgcn_raw_buffer_load_b128(rsw, v1, ob + (4 * G + i) * 15360, 16);
        else ot[2 * G + i - 4] = __builtin_amdgcn_raw_buffer_load_b64(rsw, v2, ob + (4 * G + i - 4) * 15360, 16);
    };
#pragma unroll
    for (int n = 0; n < NLD0; n++) ldo(n);
    if (bid == 0 && tid == 0) __hip_atomic_store((GAS u64*)(ws + WS_EPOCH), tag, __ATOMIC_RELAXED, __HIP_MEMORY_SCOPE_AGENT);
    f32x4 oc[5] = {};
#pragma unroll
    for (int ks = 0; ks < 20; ks++) {
        const int G = ks / 5, i = ks % 5;
        const s16x8 B = i < 4 ? __builtin_bit_cast(s16x8, of[4 * G + i]) : frag(ot[2 * G][0], ot[2 * G][1], ot[2 * G + 1][0], ot[2 * G + 1][1]);
#pragma unroll
        for (int tt = 0; tt < 5; tt++) {
            SB;
            oc[tt] = mfma16(cat(wfr[ks % NPRE][2 * tt], wfr[ks % NPRE][2 * tt + 1]), B, oc[tt]);
            SB;
            if (ks + NPRE < 20) { rdw(ks + NPRE, 2 * tt); rdw(ks + NPRE, 2 * tt + 1); }
            if ((tt == 0 || tt == 2) && NLD0 + 2 * ks + (tt >> 1) < 24) ldo(NLD0 + 2 * ks + (tt >> 1));
        }
    }
    SB;
    const u32 op = (64 * t + 16 * w + (l & 15)) * 1280 + 640 + 80 * cb + 4 * g;
#pragma unroll
    for (int tt = 0; tt < 5; tt++) out8(out, op + 16 * tt, u32x2{pk(oc[tt][0], oc[tt][1]), pk(oc[tt][2], oc[tt][3])});
    STAMP(9);
}

template <bool FIRST>
DI void loader_b(const u16* __restrict__ x, const u16* __restrict__ W, const u16* __restrict__ wo, char* __restrict__ ws, int r, int c, int lw, u32& pfd, u64& tag STAMP_ARG) {
    const int l = threadIdx.x & 63;
    int ph = r + (((c & 3) * 10) >> 2); if (ph >= 10) ph -= 10; if (ph >= 10) ph -= 10;
    const int pl = l >> 4, idx = l & 15, q3 = (idx * 11) >> 5;
    const int q = pl < 3 ? idx >> 2 : q3 & 3, sg = pl < 3 ? pl : idx - 3 * q3, ch = pl < 3 ? (idx & 3) + ((idx >> 1) & 1) : 2;
    const u32 wl = q * 3840 + sg * 1280 + ch * 16 + (sg == 0 ? ((c & 1) ? 0 : 160) : 80);
    const u32 xo0 = (l >> 3) * 1280 + (((l & 7) ^ (l >> 4)) << 4), xo1 = ((l >> 3) + 8) * 1280 + (((l & 7) ^ ((l >> 4) | 4)) << 4);
    u32 xr[2][3];
#pragma unroll
    for (int i = 0; i < 6; i++) { const int row = 192 * r + 96 * (i / 3) + 48 * lw + 16 * (i % 3); xr[i / 3][i % 3] = (row < 2048 ? row : row - 64) * 1280; }
    const char* wb = (const char*)W + c * 80 - 80 + lw * (16 * 3840);
    auto ld_x = [&](int hx) {
        const int jx = hx >> 1, hh = hx & 1;
        int jr = jx + ph; if (jr >= 10) jr -= 10;
        const char* g = (const char*)x + jr * 128;
        const u32 d = xslot(jx) + (96 * hh + 48 * lw) * 128;
#pragma unroll
        for (int i = 0; i < 3; i++) { dma(d + i * 2048, xo0, g + xr[hh][i]); dma(d + i * 2048 + 1024, xo1, g + xr[hh][i]); }
    };
    auto ld_w = [&](int i) {
        int ir = i + 2 * ph; if (ir >= 20) ir -= 20;
        const u32 d = LW + (i & 3) * WSLOT + lw * 4 * WBLK;
        const char* g = wb + ir * (32 * 3840);
#pragma unroll
        for (int b = 0; b < 4; b++) dma_m(d + b * WBLK, wl, g + b * (4 * 3840), 0xffffffffu, 0x0fffffffu);
    };
    if (FIRST) { STAMP(4); ld_w(0); STAMP(14); ld_x(0); ld_x(1); STAMP(15); return; }
    if (PRE >= 4) ld_w(1);
    if (PRE >= 5) ld_x(2);
    if (PRE >= 6) ld_x(3);
    if (PRE >= 7) ld_w(2);
    STAMP(19);
    asm volatile("s_load_dwordx2 %0, %1, 0\n\ts_waitcnt vmcnt(%2) lgkmcnt(0)" : "=&s"(tag) : "s"(ws + WS_EPOCH),
                 "i"(PRE == 3 ? 0 : PRE == 4 ? 4 : PRE == 5 ? 10 : PRE == 6 ? 16 : 20) : "memory");
    STAMP(1);
    bar();
    if (PRE < 4) ld_w(1);
    if (PRE < 5) ld_x(2);
    if (PRE < 6) ld_x(3);
    if (PRE < 7) ld_w(2);
    if (LATE == 0) { ld_w(3); ld_x(4); }
#pragma unroll
    for (int i = 0; i < 19; i++) {
        if (i == 0) waitv<LATE ? 16 : 26>(); else if (i == 2) waitv<26>();
        else if (i <= 15) waitv<20>(); else if (i == 16) waitv<14>(); else if (i == 17) waitv<4>(); else waitv<2>();
        bar();
        if (i == 0 && LATE) { ld_w(3); ld_x(4); }
        if (i + 5 < 20) ld_x(i + 5);
        if (i + 4 < 20) ld_w(i + 4);
        if (i == 17) {
            const u32 ln = __builtin_amdgcn_readfirstlane(min((sbid() >> 3) * 256 + lw * 64, 6208u)) + l;
            asm volatile("global_load_dword %0, %1, %2\n\tglobal_load_dword %0, %1, %3" : "+v"(pfd) : "v"(ln * 128), "s"(wo), "s"((const char*)wo + 128 * 128));
        }
    }
    bar();
    load_wo(wo, (sbid() >> 3) & 7, lw, 20, 2, l);
}

DI u64 path_b(const u16* __restrict__ x, const u16* __restrict__ W, const u16* __restrict__ wphi, const u16* __restrict__ wo,
               u16* __restrict__ out, char* __restrict__ ws, int r, int c, u32& pfd STAMP_ARG) {
    const int tid = threadIdx.x, l = tid & 63;
    const int w = __builtin_amdgcn_readfirstlane(tid >> 6);
    const auto rsw = __builtin_amdgcn_make_buffer_rsrc((void*)ws, 0, WS_END, 0x00020000);
    const int g = l >> 4, q = (l >> 2) & 3, p = l & 3, h = l >> 5;
    const int rq = 2 * (g & 1) + (p >> 1), hk = g >> 1;
    const bool same = hk == (p & 1);
    u64 tag;
    u32 phiq[20];
    f32x16 acc[4] = {};
    s16x8 wfq[2][3];
    if (w >= 6) {
        loader_b<false>(x, W, wo, ws, r, c, w - 6, pfd, tag STAMP_PASS);
        if (sbid() == 0) wait_flags(ws + WS_ACK + (w - 6) * 320, tag + 1, l, 40);
    } else {
        if (w < 4) dma_m(LMISC + w * 800, l * 16, (const char*)wphi + ((c ^ (w >> 1)) * 2 + (w & 1)) * 800, 0xffffffffu, 0x3ffffu);
        if (tid < 2) ldsw<u64>(LZERO + tid * 8, 0ull);
        u32 bA = LW + (g >> 1) * 2 * WBLK + q * 64 + (p & 1) * 32 + (g & 1) * 16 + (p >> 1) * 8;
        u32 bD = LW + (g >> 1) * 2 * WBLK + 768 + (3 * q + 2 * (g & 1) + (p >> 1)) * 16 + (p & 1) * 8;
        asm volatile("" : "+v"(bA), "+v"(bD));
        const int trow = 32 * w + (l & 31);
        s16x8 wfk[2][3];
        auto load_wf = [&](s16x8 (&wf)[2][3], u32 base) {
#pragma unroll
            for (int to = 0; to < 2; to++)
#pragma unroll
                for (int s = 0; s < 3; s++) {
                    s16x4 t[2];
#pragma unroll
                    for (int rd = 0; rd < 2; rd++) {
                        const int mu = 2 * s + rd;
                        if (mu < 5) {
                            int dq = mu + hk; if (mu == 4) dq = hk ? 0 : 4;
                            const bool ok = same && (to == 0 || rq == 0);
                            t[rd] = trd(ok ? base + hk * 800 + (4 * dq + q) * 40 + (4 * to + rq) * 8 : LZERO);
                        } else t[rd] = s16x4{0, 0, 0, 0};
                    }
                    wf[to][s] = cat(t[0], t[1]);
                }
        };
        asm volatile("s_load_dwordx2 %0, %1, 0\n\ts_waitcnt vmcnt(0) lgkmcnt(0)" : "=&s"(tag) : "s"(ws + WS_EPOCH), "v"(pfd) : "memory");
        bar();
        STAMP(1);
        const u32 sw = ((trow & 2) << 1) | ((trow & 4) >> 1) | (trow & 1);
        const u32 rowa = LIMG + h * XSLOT + trow * 128;
        auto gemm = [&](auto HV) {
            constexpr bool hv = decltype(HV)::value;
            constexpr int NB = hv ? 3 : 1, NM = hv ? 6 : 3, NR = hv ? 11 : 7;
            f32x16 vc[3] = {};
            u32 aX[4], aXh[4];
            const int tx = hv ? 32 * (w - 2) + (l & 31) : trow;
#pragma unroll
            for (int k = 0; k < 4; k++) {
                aX[k] = tx * 128 + (((2 * k + h) ^ ((tx >> 1) & 7)) << 4); aXh[k] = aX[k] + LIMG;
                asm volatile("" : "+v"(aX[k]), "+v"(aXh[k]));
            }
            s16x4 fa[2][8]; s16x8 fb[2][NB];
            auto rd = [&](int s, int i, int ks, int n) {
                const int nb = hv ? (n == 0 ? 1 : n == 9 ? 0 : n == 10 ? 2 : -1) : (n == 0 ? 0 : -1);
                if (nb >= 0) {
                    const int sl = (i >> 1) % 3, k = (i & 1) * 2 + ks;
                    fb[s][nb] = sl == 0 ? ldsr<s16x8>(aX[k] + nb * 8192) : ldsr<s16x8>(aXh[k] + (sl - 1) * XSLOT + nb * 8192);
                } else {
                    const int na = n <= 4 ? n - 1 : n <= 6 ? n + 1 : n - 3;
                    const u32 o = (i & 3) * WSLOT + ks * 4 * WBLK + (na & 1) * WBLK;
                    fa[s][na] = na < 6 ? trd(bA + o + (na >> 1) * 256) : trd(bD + o);
                }
            };
            auto mm = [&](int s, int t) {
                if (t < 3) { const int tt = t == 2 ? 3 : t; acc[tt] = mfma32(cat(fa[s][2 * tt], fa[s][2 * tt + 1]), fb[s][hv ? 1 : 0], acc[tt]); }
                else { const int nb = t == 3 ? 1 : t == 4 ? 0 : 2; vc[nb] = mfma32(cat(fa[s][4], fa[s][5]), fb[s][hv ? nb : 0], vc[nb]); }
            };
            auto halfstep = [&](int s, int i, int ks, bool next) {
#pragma unroll
                for (int t = 0; t < NM; t++) {
                    SB; mm(s, t); SB;
                    if (t == 0 && ks == 0) bar();
                    if (next)
#pragma unroll
                        for (int n = 3 * t; n < 3 * t + 3 && n < NR; n++) rd(s ^ 1, i, ks, n);
                }
            };
#pragma unroll
            for (int n = 0; n < NR; n++) rd(0, 0, 0, n);
#pragma unroll
            for (int i = 0; i < 20; i++) {
                halfstep(0, i, 1, true);
                if (i == 1) load_wf(wfk, LMISC);
                if (i == 2) load_wf(wfq, LMISC + 1600);
                halfstep(1, i + 1, 0, i < 19);
            }
            if (hv) {
#pragma unroll
                for (int nb = 0; nb < 3; nb++)
#pragma unroll
                    for (int mu = 0; mu < 4; mu++) {
                        const int pc = 10 + mu + h;
                        ldsw<u32x2>(rowa + (nb - 1) * 8192 + (((pc >> 1) ^ sw) << 4) + (pc & 1) * 8,
                                    u32x2{pk(vc[nb][4 * mu], vc[nb][4 * mu + 1]), pk(vc[nb][4 * mu + 2], vc[nb][4 * mu + 3])});
                    }
            }
        };
        if ((w & 3) >= 2) gemm(Flag<true>{}); else gemm(Flag<false>{});
        STAMP(2);
        float kr[20]; u32 phik[20];
#pragma unroll
        for (int i = 0; i < 20; i++) kr[i] = i < 16 ? acc[1][i] : acc[3][i - 12];
        features(kr, wfk, phik, [&]() {
            const int pc = h ? 10 : 14;
            ldsw<u32x2>(rowa + (((pc >> 1) ^ sw) << 4) + (pc & 1) * 8, u32x2{pk(acc[3][8], acc[3][9]), pk(acc[3][10], acc[3][11])});
            ldsw<u32x2>(rowa + ((7 ^ sw) << 4) + 8, u32x2{0x3f80u, 0u});
        });
        const u32 keep = 192 * r + 32 * w < 2048 ? 0xffffffffu : 0u;
#pragma unroll
        for (int ch = 0; ch < 5; ch++) ldsw<u32x4>(rowa + ((ch ^ sw) << 4), u32x4{phik[4 * ch], phik[4 * ch + 1], phik[4 * ch + 2], phik[4 * ch + 3]} & keep);
        STAMP(10);
    }
    tag += 1;
    bar();
    if (w < 4) {
        const int pcn = 4 * (g & 1) + p;
        const int pa = 10 + (pcn < 6 ? pcn : 5), pb = 5 * (w & 1) + (pcn < 5 ? pcn : 4);
        u32 aa[2], ab[2];
#pragma unroll
        for (int rd = 0; rd < 2; rd++) {
            const int row = 8 * (g >> 1) + 4 * rd + q;
            const u32 sw = ((row & 2) << 1) | ((row & 4) >> 1) | (row & 1);
            const u32 base = LIMG + (w >> 1) * XSLOT + row * 128;
            aa[rd] = base + (((pa >> 1) ^ sw) << 4) + (pa & 1) * 8;
            ab[rd] = base + (((pb >> 1) ^ sw) << 4) + (pb & 1) * 8;
            asm volatile("" : "+v"(aa[rd]), "+v"(ab[rd]));
        }
        s16x4 ta[24], tb[24];
#pragma unroll
        for (int ks = 0; ks < 12; ks++) {
            ta[2 * ks] = trd(aa[0] + ks * 2048); ta[2 * ks + 1] = trd(aa[1] + ks * 2048);
            tb[2 * ks] = trd(ab[0] + ks * 2048); tb[2 * ks + 1] = trd(ab[1] + ks * 2048);
        }
        SB;
        f32x16 kv = {};
#pragma unroll
        for (int ks = 0; ks < 12; ks++) kv = mfma32(cat(ta[2 * ks], ta[2 * ks + 1]), cat(tb[2 * ks], tb[2 * ks + 1]), kv);
        if ((l & 31) < 20) {
            const u32 lam = h * 20 + (l & 31), o = WS_PART + (c * 16 + r) * 4096 + w * 960;
            st128(u32x4{pk(kv[0], kv[1]), pk(kv[2], kv[3]), pk(kv[4], kv[5]), pk(kv[6], kv[7])}, rsw, lam * 16, o, 16);
            __builtin_amdgcn_raw_buffer_store_b64(u32x2{pk(kv[8], kv[9]), pk(kv[10], kv[11])}, rsw, 640 + lam * 8, o, 16);
        }
    }
    STAMP(11);
    auto qfeat = [&]() {
        float qr[20];
#pragma unroll
        for (int i = 0; i < 20; i++) qr[i] = i < 16 ? acc[0][i] : acc[3][i - 16];
        features(qr, wfq, phiq, []() {});
#pragma unroll
        for (int i = 0; i < 20; i++) asm volatile("" : "+v"(phiq[i]));
    };
#pragma unroll 1
    for (int st = 0; st < 2; st++) {
        if (w < 6 && (w < 4) == (st == 1)) qfeat();
        if (st == 1) break;
        if (w < 4) waitv<0>();
        bar();
        post_flag(ws + WS_F1 + c * 4096, tag, tid, 11, r);
        STAMP(3);
    }
    STAMP(17);
    if (tid < 80) ldsw<u64>(LTAB + tid * TROW + 48, 0ull);
    if (w == 6) wait_flags(ws + WS_F1 + (c ^ 1) * 4096 + r * 256, tag, l, 11);
    bar();
    STAMP(5);
    if (w < 4) {
        const int t = tid < 240 ? tid : 239;
        const int un = (t * 1093) >> 16, it = t - un * 60;
        u32x4 pv[11];
#pragma unroll
        for (int i = 0; i < 11; i++) pv[i] = __builtin_amdgcn_raw_buffer_load_b128(rsw, t * 16, WS_PART + (c ^ 1) * 65536 + i * 4096, 16);
        float s[8] = {};
        u32 slo = 0x00003f80u, shi = 0x3f800000u;
        asm volatile("" : "+s"(slo), "+s"(shi));
#pragma unroll
        for (int i = 0; i < 11; i++)
#pragma unroll
            for (int e = 0; e < 4; e++) { s[2 * e] = dot2(pv[i][e], slo, s[2 * e]); s[2 * e + 1] = dot2(pv[i][e], shi, s[2 * e + 1]); }
        const int lam = it < 40 ? it : 2 * (it - 40);
        const int hh = lam >= 20, f = lam - 20 * hh;
        const u32 a0 = LTAB + (un >> 1) * THEAD + ((un & 1) * 20 + f) * TROW + hh * 8 + (it < 40 ? 0 : 32);
        ldsw<u32x2>(a0, u32x2{pk(s[0], s[1]), pk(s[2], s[3])});
        ldsw<u32x2>(a0 + (it < 40 ? 16 : TROW), u32x2{pk(s[4], s[5]), pk(s[6], s[7])});
    }
    bar();
    STAMP(6);
    if (w < 6 && 192 * r + 32 * w < 2048) {
        const u32 tb = LTAB + hk * THEAD + q * TROW;
        u32 t0 = tb + (same ? rq : 6) * 8, t1 = tb + (same && rq < 2 ? 4 + rq : 6) * 8;
        asm volatile("" : "+v"(t0), "+v"(t1));
        f32x16 o0 = {}, o1 = {};
#pragma unroll
        for (int s = 0; s < 5; s++) {
            const s16x8 B = frag(phiq[4 * s], phiq[4 * s + 1], phiq[4 * s + 2], phiq[4 * s + 3]);
            o0 = mfma32(cat(trd(t0 + 8 * s * TROW), trd(t0 + (8 * s + 4) * TROW)), B, o0);
            o1 = mfma32(cat(trd(t1 + 8 * s * TROW), trd(t1 + (8 * s + 4) * TROW)), B, o1);
        }
        const float rden = __builtin_amdgcn_rcpf(o1[4] + 1e-6f);
        u32 ob[10];
#pragma unroll
        for (int i = 0; i < 10; i++) ob[i] = i < 8 ? pk(o0[2 * i] * rden, o0[2 * i + 1] * rden) : pk(o1[2 * i - 16] * rden, o1[2 * i - 15] * rden);
        const u32 o = WS_O + ((r * 16 + (c ^ 1)) * 6 + w) * 2560;
        st128(u32x4{ob[0], ob[1], ob[2], ob[3]}, rsw, l * 16, o, 16);
        st128(u32x4{ob[4], ob[5], ob[6], ob[7]}, rsw, l * 16 + 1024, o, 16);
        __builtin_amdgcn_raw_buffer_store_b64(u32x2{ob[8], ob[9]}, rsw, l * 8 + 2048, o, 16);
    }
    STAMP(12);
    waitv<0>();
    asm volatile("" ::"v"(pfd));
    bar();
    post_flag(ws + WS_F2 + r * (24 * 256), tag, tid, 24, c);
    STAMP(7);
    return tag;
}

DI f32x2 cmul(f32x2 a, f32x2 b) {
    f32x2 d;
    asm("v_pk_mul_f32 %0, %1, %2 op_sel_hi:[0,1]\n\tv_pk_fma_f32 %0, %1, %2, %0 op_sel:[1,1,0] op_sel_hi:[1,0,1] neg_lo:[0,1,0]" : "=&v"(d) : "v"(a), "v"(b));
    return d;
}
DI f32x2 cmulc(f32x2 a, f32x2 b) {
    f32x2 d;
    asm("v_pk_mul_f32 %0, %1, %2 op_sel_hi:[0,1] neg_hi:[0,1]\n\tv_pk_fma_f32 %0, %1, %2, %0 op_sel:[1,1,0] op_sel_hi:[1,0,1]" : "=&v"(d) : "v"(a), "v"(b));
    return d;
}
DI f32x2 csq(f32x2 a) { return cmul(a, a); }
DI f32x2 cexp(float a, float b) {
    const float m = __builtin_amdgcn_exp2f(a * 1.4426950408889634f);
    const float c1 = 0.15915494309189535f, c2 = 6.4206382e-9f;
    const float n = __builtin_rintf(b * c1);
    const float f = __builtin_fmaf(b, c2, __builtin_fmaf(b, c1, -n));
    return f32x2{m * __builtin_amdgcn_cosf(f), m * __builtin_amdgcn_sinf(f)};
}
constexpr int LOWB = 7168, LQ = 8 * LOWB, QB = 8448, LU = 131072, LPRM = LQ + 8 * QB;
static_assert(LPRM + 6144 <= LU, "LDS map");
DI u32 kaddr(u32 e) { return ((e >> 5) << 8) + ((e & 31) << 1); }

DI void prologue_a(int ia, const u16* __restrict__ Are, const u16* __restrict__ Aim, const u16* __restrict__ Bre, const u16* __restrict__ Bim,
                   const u16* __restrict__ Cre, const u16* __restrict__ Cim, const u16* __restrict__ logdt, const u16* __restrict__ Dskip) {
    const int l = threadIdx.x & 63, w = __builtin_amdgcn_readfirstlane(threadIdx.x >> 6);
    const u16* src = w == 0 ? Are : w == 1 ? Aim : w == 2 ? Bre : w == 3 ? Bim : w == 4 ? Cre : w == 5 ? Cim : w == 6 ? logdt : Dskip;
    if (w < 6) { dma(w * 2048, ia * 2048 + l * 16, src); dma(w * 2048 + 1024, ia * 2048 + 1024 + l * 16, src); }
    else dma_m(w * 2048, ia * 16, src, 1u, 0u);
}

DI u64 path_a(const u16* __restrict__ u, const u16* __restrict__ wo, u16* __restrict__ out, char* __restrict__ ws, int ia, u32 pfd STAMP_ARG) {
    const int tid = threadIdx.x, l = tid & 63;
    const int w = __builtin_amdgcn_readfirstlane(tid >> 6);
    const u32 LB = w * LOWB, KB = LQ + w * QB, UB = LU + w * 4096;
    const int g = l >> 4, q = (l >> 2) & 3, p = l & 3, h = l >> 5;
    STAMP(1);
    u64 tag;
    asm volatile("s_load_dwordx2 %0, %1, 0\n\ts_waitcnt vmcnt(0) lgkmcnt(0)" : "=&s"(tag) : "s"(ws + WS_EPOCH), "v"(pfd) : "memory");
    tag += 1;
    bar();
    if (tid == 0) __hip_atomic_store((GAS u64*)(ws + WS_ACK + ia * 8), tag, __ATOMIC_RELAXED, __HIP_MEMORY_SCOPE_AGENT);
    const float ldt = b2f(ldsr<u16>(6 * 2048 + w * 2)), dsk = b2f(ldsr<u16>(7 * 2048 + w * 2));
    u32 prm[6];
#pragma unroll
    for (int i = 0; i < 6; i++) prm[i] = ldsr<u32>(i * 2048 + w * 256 + l * 4);
    bar();
    u32x4 ur[4];
    auto u_in = [&]() {
#pragma unroll
        for (int i = 0; i < 4; i++) ur[i] = *(const u32x4*)((const char*)u + (size_t)(256 * ((w + ia) & 7) + 4 * l + i) * 1280 + ia * 16);
    };
    if (UISS == 0) u_in();
    const float dt = lo2f(pk(expf(ldt), 0.f));
    STAMP(2);
    f32x16 kd = {};
    u32 rx, ry[2];
    {
        const int pc = 4 * (g & 1) + p;
        rx = LB + (8 * (g >> 1) + q) * 48 + (pc < 6 ? pc : 5) * 8;
#pragma unroll
        for (int rd = 0; rd < 2; rd++) ry[rd] = KB + (8 * (g >> 1) + 4 * rd + q) * 64 + (((pc >> 1) ^ ((2 * rd + (q >> 1)) & 3)) << 4) + (p & 1) * 8;
    }
    const u32 wx = LB + l * 48, wy = KB + l * 64, wsw = (l >> 1) & 3;
#pragma unroll
    for (int ps = 0; ps < 2; ps++) {
        float pf[6];
#pragma unroll
        for (int i = 0; i < 6; i++) pf[i] = ps ? hi2f(prm[i]) : lo2f(prm[i]);
        const float a = dt * pf[0], b = dt * pf[1];
        const f32x2 Wc = cmulc(f32x2{pf[4], -pf[5]}, f32x2{dt * pf[2], dt * pf[3]});
        const f32x2 e1 = cexp(a, b), e16 = cexp(16.f * a, 16.f * b), e256 = cexp(256.f * a, 256.f * b);
        const f32x2 e2 = csq(e1), e4 = csq(e2), e8 = csq(e4), e32 = csq(e16), e64 = csq(e32), e128 = csq(e64), e512 = csq(e256), e1024 = csq(e512);
        f32x2 X[24];
        X[0] = Wc; X[1] = cmulc(Wc, e1); X[2] = cmulc(Wc, e2); X[3] = cmulc(X[1], e2);
#pragma unroll
        for (int i = 0; i < 4; i++) X[4 + i] = cmulc(X[i], e4);
        X[8] = cmulc(Wc, e256); X[9] = cmulc(X[8], e2); X[10] = cmulc(X[8], e4);
        X[12] = X[10]; X[13] = cmulc(X[9], e4); X[14] = cmulc(X[8], e8);
        X[11] = cmulc(Wc, e512); X[16] = cmulc(X[11], e4); X[17] = cmulc(X[11], e8);
        X[15] = cmulc(X[8], e512); X[20] = cmulc(X[15], e4); X[21] = cmulc(X[15], e8);
        X[18] = cmulc(cmulc(Wc, e8), e1024); X[19] = cmulc(X[14], e1024);
        X[22] = cmulc(X[17], e1024); X[23] = cmulc(X[21], e1024);
        f32x2 Y[8];
        Y[0] = f32x2{1.f, 0.f}; Y[1] = e8; Y[2] = e16; Y[3] = cmul(e8, e16); Y[4] = e32; Y[5] = cmul(e8, e32); Y[6] = cmul(e16, e32); Y[7] = cmul(Y[3], e32);
        const f32x2 e192 = cmul(e64, e128);
#pragma unroll
        for (int ch = 0; ch < 4; ch++) {
            if (ch < 3) {
                const f32x2* Xc = X + 8 * ch;
                ldsw<u32x4>(wx + ch * 16, u32x4{pk(Xc[0].x, Xc[1].x), pk(Xc[2].x, Xc[3].x), pk(Xc[4].x, Xc[5].x), pk(Xc[6].x, Xc[7].x)});
                ldsw<u32x4>(wx + ch * 16 + 3072, u32x4{pk(Xc[0].y, Xc[1].y), pk(Xc[2].y, Xc[3].y), pk(Xc[4].y, Xc[5].y), pk(Xc[6].y, Xc[7].y)});
            }
            f32x2 Z[8];
            const f32x2 m = ch == 1 ? e64 : ch == 2 ? e128 : e192;
#pragma unroll
            for (int i = 0; i < 8; i++) Z[i] = ch == 0 ? Y[i] : i == 0 ? m : cmul(Y[i], m);
            const u32 o = wy + ((ch ^ wsw) << 4);
            ldsw<u32x4>(o, u32x4{pk(Z[0].x, Z[1].x), pk(Z[2].x, Z[3].x), pk(Z[4].x, Z[5].x), pk(Z[6].x, Z[7].x)});
            ldsw<u32x4>(o + 4096, u32x4{pk(Z[0].y, Z[1].y), pk(Z[2].y, Z[3].y), pk(Z[4].y, Z[5].y), pk(Z[6].y, Z[7].y)});
        }
        s16x4 fx[16], fy[16];
#pragma unroll
        for (int ks = 0; ks < 8; ks++) {
            fx[2 * ks] = trd(rx + ks * 768); fx[2 * ks + 1] = trd(rx + ks * 768 + 192);
            fy[2 * ks] = trd(ry[0] + ks * 1024); fy[2 * ks + 1] = trd(ry[1] + ks * 1024);
        }
        if (ps == 0) { STAMP(4); if (UISS == 1) u_in(); }
#pragma unroll
        for (int ks = 0; ks < 8; ks++) kd = mfma32(cat(fx[2 * ks], fx[2 * ks + 1]), cat(fy[2 * ks], fy[2 * ks + 1]), kd);
    }
    STAMP(6);
    constexpr int LR = 4096;
    {
        const int j = l & 31;
        ldsw<u32x2>(KB + kaddr(1052 - 8 * j - 4 * h), u32x2{pk(kd[3], kd[2]), pk(kd[1], l == 0 ? kd[0] + dsk : kd[0])});
        ldsw<u32x2>(KB + kaddr(1052 - 256 - 8 * j - 4 * h), u32x2{pk(kd[6], kd[5]), pk(kd[4], kd[4])});
        const u32 cc = pk(kd[9], kd[9]);
        ldsw<u32x4>(KB + kaddr(1048 - 512 - 256 * h - 8 * j), u32x4{cc, pk(kd[8], kd[8]), pk(kd[8], kd[7]), pk(kd[7], kd[7])});
        if (l >= 60) ldsw<u32x4>(KB + (l - 60) * 16, l == 63 ? u32x4{0u, cc & 0xffff0000u, cc, cc} : u32x4{0u, 0u, 0u, 0u});
        const u32 far = pk(kd[10], kd[11]);
        ldsw<u16>(LB + LR + (127 - 64 * h - j) * 2, (u16)far);
        ldsw<u16>(LB + LR + (95 - 64 * h - j) * 2, (u16)(far >> 16));
        if (l < 16) ldsw<u32>(LB + LR + 256 + l * 4, 0u);
        u32x4 v[3]; u32x2 n[3];
        u32 a0[3];
#pragma unroll
        for (int it = 0; it < 3; it++) {
            const u32 i = it < 2 ? 64 * it + l : 128 + (l & 3);
            a0[it] = KB + kaddr(8 * i);
            v[it] = ldsr<u32x4>(a0[it]); n[it] = ldsr<u32x2>(KB + kaddr(8 * i + 8));
        }
#pragma unroll
        for (int it = 0; it < 3; it++) {
            const u32 s0 = __builtin_amdgcn_alignbit(v[it][1], v[it][0], 16), s1 = __builtin_amdgcn_alignbit(v[it][2], v[it][1], 16),
                      s2 = __builtin_amdgcn_alignbit(v[it][3], v[it][2], 16), s3 = __builtin_amdgcn_alignbit(n[it][0], v[it][3], 16),
                      s4 = __builtin_amdgcn_alignbit(n[it][1], n[it][0], 16);
            if (it < 2 || l < 4) {
                ldsw<u32x4>(a0[it] + 64, u32x4{s0, s1, s2, s3});
                ldsw<u32x4>(a0[it] + 128, u32x4{v[it][1], v[it][2], v[it][3], n[it][0]});
                ldsw<u32x4>(a0[it] + 192, u32x4{s1, s2, s3, s4});
            }
        }
    }
    STAMP(5);
    {
        const u32 a = LU + (256 * ((w + ia) & 7) + 4 * l) * 2;
#pragma unroll
        for (int c2 = 0; c2 < 4; c2++) {
            ldsw<u32x2>(a + 2 * c2 * 4096, u32x2{__builtin_amdgcn_perm(ur[1][c2], ur[0][c2], 0x05040100u), __builtin_amdgcn_perm(ur[3][c2], ur[2][c2], 0x05040100u)});
            ldsw<u32x2>(a + (2 * c2 + 1) * 4096, u32x2{__builtin_amdgcn_perm(ur[1][c2], ur[0][c2], 0x07060302u), __builtin_amdgcn_perm(ur[3][c2], ur[2][c2], 0x07060302u)});
        }
    }
    STAMP(3);
    bar();
    STAMP(10);
    const int rr = l & 15;
    f32x4 y[4] = {};
    {
        float sm[36];
        sm[0] = 0.f;
#pragma unroll
        for (int i = 0; i < 5; i++) {
            u32x4 t = ldsr<u32x4>(UB + l * 32 - 48 + i * 16);
            if (i < 3 && 2 * l + i < 3) t = u32x4{0, 0, 0, 0};
#pragma unroll
            for (int e = 0; e < 8; e++)
                if (8 * i + e < 35) sm[8 * i + e + 1] = sm[8 * i + e] + ((e & 1) ? hi2f(t[e >> 1]) : lo2f(t[e >> 1]));
        }
        const u32 wb = LB + l * 64, sw = (l >> 1) & 3;
#pragma unroll
        for (int ch = 0; ch < 4; ch++) {
            const int o1 = ch < 2 ? 13 : 20, o0 = ch < 2 ? 4 : 13, b0 = 8 * (ch & 1);
            ldsw<u32x4>(wb + ((ch ^ sw) << 4), u32x4{pk(sm[b0 + o1] - sm[b0 + o0], sm[b0 + 1 + o1] - sm[b0 + 1 + o0]), pk(sm[b0 + 2 + o1] - sm[b0 + 2 + o0], sm[b0 + 3 + o1] - sm[b0 + 3 + o0]),
                                                     pk(sm[b0 + 4 + o1] - sm[b0 + 4 + o0], sm[b0 + 5 + o1] - sm[b0 + 5 + o0]), pk(sm[b0 + 6 + o1] - sm[b0 + 6 + o0], sm[b0 + 7 + o1] - sm[b0 + 7 + o0])});
        }
        s16x8 fw[4], ft[4];
#pragma unroll
        for (int s = 0; s < 4; s++) {
            s16x4 t[2];
#pragma unroll
            for (int rd = 0; rd < 2; rd++) {
                const int blk = 4 * g + 2 * rd + (q >> 1);
                t[rd] = trd(LB + s * 1024 + blk * 64 + (((2 * (q & 1) + (p >> 1)) ^ ((blk >> 1) & 3)) << 4) + (p & 1) * 8);
            }
            fw[s] = cat(t[0], t[1]);
            const u32 a = LB + LR + (126 - 32 * s - 2 * rr + 8 * g) * 2;
            ft[s] = frag(ldsr<u32>(a), ldsr<u32>(a + 4), ldsr<u32>(a + 8), ldsr<u32>(a + 12));
        }
#pragma unroll
        for (int s = 0; s < 4; s++)
#pragma unroll
            for (int ct = s; ct < 4; ct++) y[ct] = mfma16(fw[s], ft[ct - s], y[ct]);
    }
    STAMP(13);
    const u32 e0 = 1055 - 16 - rr + 8 * g, sh = e0 & 3;
    auto half = [&](auto HI) {
        constexpr bool hi = decltype(HI)::value;
        constexpr int T0 = hi ? 4 : 0, B0 = hi ? 1 : 0, NB = hi ? 7 : 4, NRD = NB + 8;
        u32 ka0 = KB + kaddr(e0 - sh) + sh * 64 - 3 * 2048, ka1 = KB + kaddr(e0 - sh + 4) + sh * 64 - 3 * 2048;
        u32 ua = UB + 32 * rr + 16 * g - 32;
        u32 mk[4];
#pragma unroll
        for (int i = 0; i < 4; i++) mk[i] = (2 * i > 16 + rr - 8 * g ? 0u : 0xffffu) | (2 * i + 1 > 16 + rr - 8 * g ? 0u : 0xffff0000u);
        int lim = 2 * rr + g - 2;
        u32x2 A[2][8]; u32x4 B[2][NB];
        auto rdc = [&](int s, int n) {
            if (n < 12) {
                const int m = n / 3, k = n % 3;
                if (k == 0) B[s][m] = ldsr<u32x4>(ua + (B0 + m) * 512);
                else if (k == 1) A[s][2 * (3 - m)] = ldsr<u32x2>(ka0 + m * 2048);
                else A[s][2 * (3 - m) + 1] = ldsr<u32x2>(ka1 + m * 2048);
            } else B[s][n - 8] = ldsr<u32x4>(ua + (B0 + n - 8) * 512);
        };
        auto group = [&](int s, bool next) {
            A[s][0] &= u32x2{mk[0], mk[1]}; A[s][1] &= u32x2{mk[2], mk[3]};
            if (!hi && lim < 0) B[s][0] = u32x4{0, 0, 0, 0};
            ka0 -= 256; ka1 -= 256; ua -= 64; lim -= 4;
            mk[0] = mk[1] = mk[2] = mk[3] = 0xffffffffu;
            asm volatile("" : "+v"(ka0), "+v"(ka1), "+v"(ua));
            int n = 0;
#pragma unroll
            for (int m = 3; m >= 0; m--)
#pragma unroll
                for (int t = 0; t < 4; t++) {
                    const int ct = T0 + t - m;
                    if (ct < 0) continue;
                    SB;
                    y[t] = mfma16(frag(A[s][2 * m][0], A[s][2 * m][1], A[s][2 * m + 1][0], A[s][2 * m + 1][1]), __builtin_bit_cast(s16x8, B[s][ct - B0]), y[t]);
                    SB;
                    if (next && n < NRD) rdc(s ^ 1, n);
                    n++;
                    if (!hi && next && (n == 1 || n == 6)) rdc(s ^ 1, n++);
                }
        };
        asm volatile("" : "+v"(ka0), "+v"(ka1), "+v"(ua));
#pragma unroll
        for (int n = 0; n < NRD; n++) rdc(0, n);
        if (hi) {
            const u32x2 x0 = ldsr<u32x2>(ka0 - 2048), x1 = ldsr<u32x2>(ka1 - 2048);
            u32x4 b0 = ldsr<u32x4>(ua);
            if (lim < 0) b0 = u32x4{0, 0, 0, 0};
            const s16x8 xa = frag(x0[0], x0[1], x1[0], x1[1]);
            y[0] = mfma16(xa, __builtin_bit_cast(s16x8, b0), y[0]);
#pragma unroll
            for (int t = 1; t < 4; t++) y[t] = mfma16(xa, __builtin_bit_cast(s16x8, B[0][t - 1]), y[t]);
        }
        if (hi) {
#pragma unroll 1
            for (int gg = 0; gg < 3; gg++) { group(0, true); group(1, true); }
            group(0, true); group(1, false);
        } else {
            u32 v[8];
#pragma unroll
            for (int c2 = 0; c2 < 8; c2++) v[c2] = ldsr<u32>(LU + c2 * 4096 + 2048 + tid * 4);
            const u32 op = (1024 + 2 * tid) * 1280 + 8 * ia;
            const u32 idx = tid, row = (idx * 6554u) >> 16, wof = row * 1120 + idx * 16;
            const char* wob = (const char*)wo + ((sbid() >> 3) & 7) * 160;
#pragma unroll
            for (int gg = 0; gg < 8; gg++) {
                if (gg == 0 || gg == 4) {
                    const u32 sel = gg ? 0x07060302u : 0x05040100u;
                    out16(out, op + (gg ? 1280 : 0), u32x4{__builtin_amdgcn_perm(v[1], v[0], sel), __builtin_amdgcn_perm(v[3], v[2], sel),
                                                           __builtin_amdgcn_perm(v[5], v[4], sel), __builtin_amdgcn_perm(v[7], v[6], sel)});
                }
                if (w < 5) {
                    constexpr int G0[9] = {0, 0, 2, 4, 6, 6, 8, 10, 11};
#pragma unroll
                    for (int gq = G0[gg]; gq < G0[gg + 1]; gq++) dma(gq * 5120 + w * 1024, wof, wob + gq * (32 * 1280));
                }
                group(gg & 1, gg < 7);
            }
        }
#pragma unroll
        for (int ct = 0; ct < 4; ct++) {
            ldsw<u32x2>(UB + (T0 + ct) * 512 + rr * 32 + g * 8, u32x2{pk(y[ct][0], y[ct][1]), pk(y[ct][2], y[ct][3])});
            y[ct] = f32x4{0.f, 0.f, 0.f, 0.f};
        }
        if (hi) STAMP(14); else STAMP(16);
        bar();
        if (hi) STAMP(15); else STAMP(17);
        if (!hi) {
            load_wo(wo, (sbid() >> 3) & 7, 11 + w, 20, 8, l);
            if (w >= 4 && !YLATE) store_rows(out, ia, 0, tid - 256);
        }
    };
    half(Flag<true>{});
    STAMP(11);
    half(Flag<false>{});
    STAMP(12);
    if (w >= 4 && !YLATE) waitv<4>(); else waitv<0>();
    STAMP(7);
    return tag;
}

__global__ void __launch_bounds__(512, 2)
fused_kernel(const u16* __restrict__ x, const u16* __restrict__ W, const u16* __restrict__ wphi, const u16* __restrict__ wo,
             u16* __restrict__ out, char* __restrict__ ws, const u16* __restrict__ u, const u16* __restrict__ Are,
             const u16* __restrict__ Aim, const u16* __restrict__ Bre, const u16* __restrict__ Bim, const u16* __restrict__ Cre,
             const u16* __restrict__ Cim, const u16* __restrict__ logdt, const u16* __restrict__ Dskip) {
    __shared__ __attribute__((aligned(16))) char smem[LDS_BYTES];
    asm volatile("" ::"s"((u32)(size_t)(LAS char*)smem));
    u32 pfd = 0;
    if (threadIdx.x < 12) {
        u64 pc; asm volatile("s_getpc_b64 %0" : "=s"(pc));
        asm volatile("global_load_dword %0, %1, %2" : "+v"(pfd) : "v"(threadIdx.x * 128), "s"(pc + min((sbid() >> 3) * 1536, CODE_PF_BYTES - 1536)));
    }
    asm volatile("" ::"s"(x), "s"(W), "s"(wphi), "s"(wo), "s"(out), "s"(ws), "s"(u), "s"(Are), "s"(Aim), "s"(Bre), "s"(Bim), "s"(Cre), "s"(Cim),
                 "s"(logdt), "s"(Dskip));
    const Role R = role_of(sbid());
    u64 tag;
    if (!R.is_a) {
        if (__builtin_amdgcn_readfirstlane(threadIdx.x) >= 384) loader_b<true>(x, W, wo, ws, R.r, R.c, __builtin_amdgcn_readfirstlane(threadIdx.x >> 6) - 6, pfd, tag STAMP_PASS);
    } else prologue_a(R.ia, Are, Aim, Bre, Bim, Cre, Cim, logdt, Dskip);
    u32 sel = R.nb;
    asm volatile("" : "+s"(sel));
    if ((sbid() >> 3) >= sel) {
        tag = path_a(u, wo, out, ws, R.ia, pfd STAMP_PASS);
    } else {
        tag = path_b(x, W, wphi, wo, out, ws, R.r, R.c, pfd STAMP_PASS);
    }
    out_proj(out, ws, tag, R.is_a ? R.ia : -1 STAMP_PASS);
}

extern "C" void kernel_launch(void* const* d_in, const int* in_sizes, int n_in, void* d_out, int out_size, void* d_ws, size_t ws_size,
                              hipStream_t stream) {
    const u16* const* in = (const u16* const*)d_in;
    fused_kernel<<<256, 512, 0, stream>>>(in[9], in[10], in[11], in[12], (u16*)d_out, (char*)d_ws, in[0], in[1], in[2], in[3], in[4], in[5],
                                          in[6], in[7], in[8]);
}
```

```cpp
#include <hip/hip_runtime.h>
#include <hip/hip_bf16.h>

typedef unsigned short u16;
typedef unsigned u32;
typedef unsigned long long u64;
typedef __attribute__((ext_vector_type(8))) short s16x8;
typedef __attribute__((ext_vector_type(4))) short s16x4;
typedef __attribute__((ext_vector_type(16))) float f32x16;
typedef __attribute__((ext_vector_type(4))) float f32x4;
typedef __attribute__((ext_vector_type(2))) float f32x2;
typedef __attribute__((ext_vector_type(4))) u32 u32x4;
typedef __attribute__((ext_vector_type(2))) u32 u32x2;
typedef __attribute__((ext_vector_type(2))) __bf16 bf16x2;
#define LAS __attribute__((address_space(3)))
#define GAS __attribute__((address_space(1)))
#define DI __device__ __forceinline__
#define SB __builtin_amdgcn_sched_barrier(0)
DI unsigned sbid() { return __builtin_amdgcn_workgroup_id_x(); }

constexpr int LDS_BYTES = 163840;
constexpr u32 WS_EPOCH = 0, WS_ACK = 4096, WS_F1 = 65536, WS_F2 = 131072, WS_PART = 524288, WS_O = 2u << 20, WS_END = 5u << 20;
constexpr u32 kSpinCap = 1u << 18;
constexpr u32 CODE_PF_BYTES = 46080;

DI u32 pk(float a, float b) { f32x2 v = {a, b}; return __builtin_bit_cast(u32, __builtin_convertvector(v, bf16x2)); }
DI float lo2f(u32 x) { return __uint_as_float(x << 16); }
DI float hi2f(u32 x) { return __uint_as_float(x & 0xffff0000u); }
DI float b2f(u16 h) { return __uint_as_float((u32)h << 16); }
DI s16x4 trd(u32 a) { return __builtin_amdgcn_ds_read_tr16_b64_v4i16((LAS s16x4*)(size_t)a); }
DI s16x8 cat(s16x4 a, s16x4 b) { return __builtin_shufflevector(a, b, 0, 1, 2, 3, 4, 5, 6, 7); }
DI s16x8 frag(u32 a, u32 b, u32 c, u32 d) { u32x4 v = {a, b, c, d}; return __builtin_bit_cast(s16x8, v); }
template <class T> DI T ldsr(u32 a) { return *(LAS T*)(size_t)a; }
template <class T> DI void ldsw(u32 a, T v) { *(LAS T*)(size_t)a = v; }
template <int N> DI void waitv() { asm volatile("s_waitcnt vmcnt(%0)" ::"i"(N) : "memory"); }
DI void bar() { asm volatile("s_waitcnt lgkmcnt(0)\n\ts_barrier" ::: "memory"); }
DI f32x16 mfma32(s16x8 a, s16x8 b, f32x16 c) { return __builtin_amdgcn_mfma_f32_32x32x16_bf16(a, b, c, 0, 0, 0); }
DI f32x4 mfma16(s16x8 a, s16x8 b, f32x4 c) { return __builtin_amdgcn_mfma_f32_16x16x32_bf16(a, b, c, 0, 0, 0); }
DI float max3(float a, float b, float c) { float r; asm("v_max3_f32 %0, %1, %2, %3" : "=v"(r) : "v"(a), "v"(b), "v"(c)); return r; }
DI float min3(float a, float b, float c) { float r; asm("v_min3_f32 %0, %1, %2, %3" : "=v"(r) : "v"(a), "v"(b), "v"(c)); return r; }
DI float dot2(u32 x, u32 sel, float c) { return __builtin_amdgcn_fdot2_f32_bf16(__builtin_bit_cast(bf16x2, sel), __builtin_bit_cast(bf16x2, x), c, false); }
template <class R> DI void st128(u32x4 v, R rs, u32 voff, u32 soff) { __builtin_amdgcn_raw_buffer_store_b128(v, rs, voff + soff, 0, 16); }
DI void out16(u16* out, u32 off, u32x4 v) {
    __builtin_amdgcn_raw_buffer_store_b128(v, __builtin_amdgcn_make_buffer_rsrc((void*)out, 0, 2048 * 2560, 0x00020000), off * 2, 0, 0);
}
DI void out8(u16* out, u32 off, u32x2 v) {
    __builtin_amdgcn_raw_buffer_store_b64(v, __builtin_amdgcn_make_buffer_rsrc((void*)out, 0, 2048 * 2560, 0x00020000), off * 2, 0, 0);
}
DI void dma(u32 m0v, u32 voff, const void* sbase) {
    asm volatile("s_mov_b32 m0, %0\n\ts_nop 0\n\tglobal_load_lds_dwordx4 %1, %2" ::"s"(m0v), "v"(voff), "s"(sbase) : "memory");
}
DI void dma_m(u32 m0v, u32 voff, const void* sbase, u32 mlo, u32 mhi) {
    asm volatile("s_mov_b32 exec_lo, %3\n\ts_mov_b32 exec_hi, %4\n\ts_mov_b32 m0, %0\n\ts_nop 0\n\tglobal_load_lds_dwordx4 %1, %2\n\ts_mov_b64 exec, -1"
                 ::"s"(m0v), "v"(voff), "s"(sbase), "i"(mlo), "i"(mhi) : "memory");
}

DI void post_flag(char* fl, u64 tag, int tid, int n, int me) {
    if (tid < n) __hip_atomic_store((GAS u64*)(fl + tid * 256 + me * 8), tag, __ATOMIC_RELAXED, __HIP_MEMORY_SCOPE_AGENT);
}
DI u64 epoch(const char* ws) { return __hip_atomic_load((const GAS u64*)(ws + WS_EPOCH), __ATOMIC_RELAXED, __HIP_MEMORY_SCOPE_AGENT); }
DI void wait_flags(const char* fl, u64 tag, int l, int n) {
    const GAS u64* p = (const GAS u64*)(fl + (l < n ? l : 0) * 8);
    u64 v0 = __hip_atomic_load(p, __ATOMIC_RELAXED, __HIP_MEMORY_SCOPE_AGENT);
#pragma unroll 1
    for (u32 s = 0; s < kSpinCap; s++) {
        __builtin_amdgcn_s_sleep(2);
        const u64 v1 = __hip_atomic_load(p, __ATOMIC_RELAXED, __HIP_MEMORY_SCOPE_AGENT);
        if (__all(v0 == tag)) break;
        v0 = v1;
    }
}

template <class F> DI void features(const float (&v)[20], const s16x8 (&wf)[2][3], u32 (&phi)[20], F filler) {
    u32 b[10];
#pragma unroll
    for (int i = 0; i < 10; i++) b[i] = pk(v[2 * i], v[2 * i + 1]);
    const s16x8 B0 = frag(b[0], b[1], b[2], b[3]), B1 = frag(b[4], b[5], b[6], b[7]), B2 = frag(b[8], b[9], 0, 0);
    f32x16 p0 = {}, p1 = {};
    p0 = mfma32(wf[0][0], B0, p0); p1 = mfma32(wf[1][0], B0, p1);
    p0 = mfma32(wf[0][1], B1, p0); p1 = mfma32(wf[1][1], B1, p1);
    p0 = mfma32(wf[0][2], B2, p0); p1 = mfma32(wf[1][2], B2, p1);
    SB; filler(); SB;
    asm volatile("s_nop 11" : "+v"(p0), "+v"(p1));
    float p[20];
#pragma unroll
    for (int i = 0; i < 20; i++) p[i] = i < 16 ? p0[i] : p1[i - 16];
    float mxa = max3(p[0], p[1], p[2]), mna = min3(p[0], p[1], p[2]), mxb = max3(p[3], p[4], p[5]), mnb = min3(p[3], p[4], p[5]);
#pragma unroll
    for (int i = 6; i < 18; i += 4) {
        mxa = max3(mxa, p[i], p[i + 1]); mna = min3(mna, p[i], p[i + 1]);
        mxb = max3(mxb, p[i + 2], p[i + 3]); mnb = min3(mnb, p[i + 2], p[i + 3]);
    }
    mxa = max3(mxa, p[18], p[19]); mna = min3(mna, p[18], p[19]);
    const float mx = max3(mxa, mxb, mxb), mn = min3(mna, mnb, mnb);
    const float L2E = 1.4426950408889634f;
    const float cx = -mx * L2E, cn = mn * L2E;
    float e[40], s1, s2;
#pragma unroll
    for (int i = 0; i < 20; i++) {
        e[i] = __builtin_amdgcn_exp2f(__builtin_fmaf(p[i], L2E, cx));
        e[20 + i] = __builtin_amdgcn_exp2f(__builtin_fmaf(p[i], -L2E, cn));
        if (i == 0) { s1 = e[0]; s2 = e[20]; } else { s1 += e[i]; s2 += e[20 + i]; }
    }
    const float r1 = __builtin_amdgcn_rcpf(s1), r2 = __builtin_amdgcn_rcpf(s2);
#pragma unroll
    for (int i = 0; i < 10; i++) { phi[i] = pk(e[2 * i] * r1, e[2 * i + 1] * r1); phi[10 + i] = pk(e[20 + 2 * i] * r2, e[21 + 2 * i] * r2); }
}


constexpr int WBLK = 960, WSLOT = 8 * WBLK, XSLOT = 24576;
constexpr int LW = XSLOT, LIMG = 102400, LMISC = LIMG + 2 * XSLOT, LZERO = LMISC + 3200, LTAB = LZERO + 64, TROW = 56, THEAD = 40 * TROW;
DI int xslot(int j) { return j % 3 == 0 ? 0 : LIMG + (j % 3 - 1) * XSLOT; }
static_assert(LTAB + 80 * TROW <= LDS_BYTES, "LDS map");

template <bool V> struct Flag { static constexpr bool value = V; };

struct Role { bool is_a; int r, c, ia; u32 nb; };
DI Role role_of(u32 bid) {
    const int xcd = bid & 7, j = bid >> 3;
    Role R;
    const int e = xcd & 1, m = xcd >> 1, nb = e ? 20 : 24, cq = e ? (j * 52) >> 8 : (j * 43) >> 8;
    R.r = __builtin_amdgcn_readfirstlane(e ? 6 + j - 5 * cq : j - 6 * cq); R.c = __builtin_amdgcn_readfirstlane(4 * m + cq);
    R.ia = __builtin_amdgcn_readfirstlane((e ? 32 + 12 * m : 8 * m) + j - nb);
    R.is_a = j >= nb; R.nb = nb;
    return R;
}

DI void load_wo(const u16* wo, int cb, int wi, int end, int nw, int l) {
    u32 off[5];
#pragma unroll
    for (int i = 0; i < 5; i++) { const u32 idx = 64 * i + l, row = (idx * 6554u) >> 16; off[i] = row * 1120 + idx * 16; }
#pragma unroll 1
    for (int gq = wi; gq < end; gq += nw) {
        const char* gp = (const char*)wo + cb * 160 + gq * (32 * 1280);
#pragma unroll
        for (int i = 0; i < 5; i++) dma(gq * 5120 + i * 1024, off[i], gp);
    }
}

DI void store_rows(u16* __restrict__ out, int ia, int t0, int tt) {
    u32x2 v[8];
#pragma unroll
    for (int c2 = 0; c2 < 8; c2++) v[c2] = ldsr<u32x2>(131072 + c2 * 4096 + t0 * 2 + tt * 8);
    const u32 op = (t0 + 4 * tt) * 1280 + 8 * ia;
#pragma unroll
    for (int i = 0; i < 4; i++) {
        const u32 sel = (i & 1) ? 0x07060302u : 0x05040100u;
        out16(out, op + i * 1280, u32x4{__builtin_amdgcn_perm(v[1][i >> 1], v[0][i >> 1], sel), __builtin_amdgcn_perm(v[3][i >> 1], v[2][i >> 1], sel),
                                        __builtin_amdgcn_perm(v[5][i >> 1], v[4][i >> 1], sel), __builtin_amdgcn_perm(v[7][i >> 1], v[6][i >> 1], sel)});
    }
}

DI void out_proj(u16* __restrict__ out, char* __restrict__ ws, u64 tag) {
    const int bid = sbid(), tid = threadIdx.x, l = tid & 63, w = __builtin_amdgcn_readfirstlane(tid >> 6);
    const int t = 4 * (bid & 7) + (bid >> 6), cb = (bid >> 3) & 7, rs = (t * 43) >> 7, sub = t - 3 * rs;
    bar();
    if (w >= 4) return;
    constexpr int NPRE = 8, NLD0 = 8;
    const auto rsw = __builtin_amdgcn_make_buffer_rsrc((void*)ws, 0, WS_END, 0x00020000);
    const int g = l >> 4, q = (l >> 2) & 3, p = l & 3, hp = g & 1, P = g >> 1;
    u32 b1 = (20 * hp + 8 * P + q) * 160 + p * 8, b2 = (80 * P + 20 * hp + 16 + q) * 160 + p * 8;
    u32 b1h = b1 + 51200, b2h = b2 + 51200;
    asm volatile("" : "+v"(b1), "+v"(b2), "+v"(b1h), "+v"(b2h));
    s16x4 wfr[NPRE][10];
    auto rdw = [&](int ks, int n) {
        const int G = ks / 5, i = ks % 5, tt = n >> 1, rd = n & 1;
        const u32 so = (G & 1) * 25600;
        wfr[ks % NPRE][n] = i < 4 ? trd((G >> 1 ? b1h : b1) + so + (40 * i + 4 * rd) * 160 + 32 * tt) : trd((G >> 1 ? b2h : b2) + so + 40 * rd * 160 + 32 * tt);
    };
#pragma unroll
    for (int ks = 0; ks < NPRE; ks++)
#pragma unroll
        for (int n = 0; n < 10; n++) rdw(ks, n);
    if (w == 0) wait_flags(ws + WS_F2 + (rs * 24 + sub * 8 + cb) * 256, tag, l, 16);
    bar();
    const int ti = 16 * (w & 1) + (l & 15);
    const u32 ob = WS_O + (rs * 96 + 2 * sub + (w >> 1)) * 2560;
    const u32 v1 = P * 1024 + (hp * 32 + ti) * 16, v2 = 2048 + 2 * P * 15360 + (hp * 32 + ti) * 8;
    u32x4 of[16]; u32x2 ot[8];
    auto ldo = [&](int n) {
        const int G = n / 6, i = n % 6;
        if (i < 4) of[4 * G + i] = __builtin_amdgcn_raw_buffer_load_b128(rsw, v1, ob + (4 * G + i) * 15360, 16);
        else ot[2 * G + i - 4] = __builtin_amdgcn_raw_buffer_load_b64(rsw, v2, ob + (4 * G + i - 4) * 15360, 16);
    };
#pragma unroll
    for (int n = 0; n < NLD0; n++) ldo(n);
    if (bid == 0 && tid == 0) __hip_atomic_store((GAS u64*)(ws + WS_EPOCH), tag, __ATOMIC_RELAXED, __HIP_MEMORY_SCOPE_AGENT);
    f32x4 oc[5] = {};
#pragma unroll
    for (int ks = 0; ks < 20; ks++) {
        const int G = ks / 5, i = ks % 5;
        const s16x8 B = i < 4 ? __builtin_bit_cast(s16x8, of[4 * G + i]) : frag(ot[2 * G][0], ot[2 * G][1], ot[2 * G + 1][0], ot[2 * G + 1][1]);
#pragma unroll
        for (int tt = 0; tt < 5; tt++) {
            SB;
            oc[tt] = mfma16(cat(wfr[ks % NPRE][2 * tt], wfr[ks % NPRE][2 * tt + 1]), B, oc[tt]);
            SB;
            if (ks + NPRE < 20) { rdw(ks + NPRE, 2 * tt); rdw(ks + NPRE, 2 * tt + 1); }
            if ((tt == 0 || tt == 2) && NLD0 + 2 * ks + (tt >> 1) < 24) ldo(NLD0 + 2 * ks + (tt >> 1));
        }
    }
    SB;
    const u32 op = (64 * t + 16 * w + (l & 15)) * 1280 + 640 + 80 * cb;
#pragma unroll
    for (int tt = 0; tt < 4; tt += 2) {
        const auto s0 = __builtin_amdgcn_permlane16_swap(pk(oc[tt][0], oc[tt][1]), pk(oc[tt + 1][0], oc[tt + 1][1]), false, false);
        const auto s1 = __builtin_amdgcn_permlane16_swap(pk(oc[tt][2], oc[tt][3]), pk(oc[tt + 1][2], oc[tt + 1][3]), false, false);
        out16(out, op + 16 * (tt + (g & 1)) + 8 * (g >> 1), u32x4{s0[0], s1[0], s0[1], s1[1]});
    }
    out8(out, op + 64 + 4 * g, u32x2{pk(oc[4][0], oc[4][1]), pk(oc[4][2], oc[4][3])});
}

template <bool FIRST>
DI void loader_b(const u16* __restrict__ x, const u16* __restrict__ W, const u16* __restrict__ wo, const char* ws, int r, int c, int lw, u32& pfd, u64& ep) {
    const int l = threadIdx.x & 63;
    int ph = r + (((c & 3) * 10) >> 2); if (ph >= 10) ph -= 10; if (ph >= 10) ph -= 10;
    const int pl = l >> 4, idx = l & 15, q3 = (idx * 11) >> 5;
    const int q = pl < 3 ? idx >> 2 : q3 & 3, sg = pl < 3 ? pl : idx - 3 * q3, ch = pl < 3 ? (idx & 3) + ((idx >> 1) & 1) : 2;
    const u32 wl = q * 3840 + sg * 1280 + ch * 16 + (sg == 0 ? ((c & 1) ? 0 : 160) : 80);
    const u32 xo0 = (l >> 3) * 1280 + (((l & 7) ^ (l >> 4)) << 4), xo1 = ((l >> 3) + 8) * 1280 + (((l & 7) ^ ((l >> 4) | 4)) << 4);
    u32 xr[2][3];
#pragma unroll
    for (int i = 0; i < 6; i++) { const int row = 192 * r + 96 * (i / 3) + 48 * lw + 16 * (i % 3); xr[i / 3][i % 3] = (row < 2048 ? row : row - 64) * 1280; }
    const char* wb = (const char*)W + c * 80 - 80 + lw * (16 * 3840);
    auto ld_x = [&](int hx) {
        const int jx = hx >> 1, hh = hx & 1;
        int jr = jx + ph; if (jr >= 10) jr -= 10;
        const char* g = (const char*)x + jr * 128;
        const u32 d = xslot(jx) + (96 * hh + 48 * lw) * 128;
#pragma unroll
        for (int i = 0; i < 3; i++) { dma(d + i * 2048, xo0, g + xr[hh][i]); dma(d + i * 2048 + 1024, xo1, g + xr[hh][i]); }
    };
    auto ld_w = [&](int i) {
        int ir = i + 2 * ph; if (ir >= 20) ir -= 20;
        const u32 d = LW + (i & 3) * WSLOT + lw * 4 * WBLK;
        const char* g = wb + ir * (32 * 3840);
#pragma unroll
        for (int b = 0; b < 4; b++) dma_m(d + b * WBLK, wl, g + b * (4 * 3840), 0xffffffffu, 0x0fffffffu);
    };
    if (FIRST) { ld_w(0); ld_x(0); ld_x(1); return; }
    ld_w(1); ld_x(2); ld_x(3);
    waitv<16>();
    bar();
    ld_w(2);
#pragma unroll
    for (int i = 0; i < 19; i++) {
        if (i == 0) waitv<16>(); else if (i == 2) waitv<26>();
        else if (i <= 15) waitv<20>(); else if (i == 16) waitv<14>(); else if (i == 17) waitv<4>(); else waitv<3>();
        bar();
        if (i == 0) { ld_w(3); ld_x(4); }
        if (i + 5 < 20) ld_x(i + 5);
        if (i + 4 < 20) ld_w(i + 4);
        if (i == 17) {
            const u32 ln = __builtin_amdgcn_readfirstlane(min((sbid() >> 3) * 256 + lw * 64, 6208u)) + l;
            asm volatile("global_load_dword %0, %1, %2\n\tglobal_load_dword %0, %1, %3" : "+v"(pfd) : "v"(ln * 128), "s"(wo), "s"((const char*)wo + 128 * 128));
            asm volatile("global_load_dwordx2 %0, %1, %2 sc1" : "=v"(ep) : "v"(0u), "s"(ws + WS_EPOCH) : "memory");
        }
    }
    bar();
    asm volatile("s_waitcnt vmcnt(0)" : "+v"(ep)::"memory");
    if (threadIdx.x == 384) ldsw<u64>(LZERO + 16, ep);
    load_wo(wo, (sbid() >> 3) & 7, lw, 20, 2, l);
}

DI u64 path_b(const u16* __restrict__ x, const u16* __restrict__ W, const u16* __restrict__ wphi, const u16* __restrict__ wo,
               u16* __restrict__ out, char* __restrict__ ws, int r, int c, u32& pfd) {
    const int tid = threadIdx.x, l = tid & 63;
    const int w = __builtin_amdgcn_readfirstlane(tid >> 6);
    const auto rsw = __builtin_amdgcn_make_buffer_rsrc((void*)ws, 0, WS_END, 0x00020000);
    const int g = l >> 4, q = (l >> 2) & 3, p = l & 3, h = l >> 5;
    const int rq = 2 * (g & 1) + (p >> 1), hk = g >> 1;
    const bool same = hk == (p & 1);
    u64 tag = 0;
    u32 phiq[20];
    f32x16 acc[4] = {};
    s16x8 wfq[2][3];
    if (w >= 6) {
        __builtin_amdgcn_s_setprio(3);
        loader_b<false>(x, W, wo, ws, r, c, w - 6, pfd, tag);
        if (sbid() == 0) wait_flags(ws + WS_ACK + (w - 6) * 320, tag + 1, l, 40);
    } else {
        if (w < 4) dma_m(LMISC + w * 800, l * 16, (const char*)wphi + ((c ^ (w >> 1)) * 2 + (w & 1)) * 800, 0xffffffffu, 0x3ffffu);
        if (tid < 2) ldsw<u64>(LZERO + tid * 8, 0ull);
        u32 bA = LW + (g >> 1) * 2 * WBLK + q * 64 + (p & 1) * 32 + (g & 1) * 16 + (p >> 1) * 8;
        u32 bD = LW + (g >> 1) * 2 * WBLK + 768 + (3 * q + 2 * (g & 1) + (p >> 1)) * 16 + (p & 1) * 8;
        asm volatile("" : "+v"(bA), "+v"(bD));
        const int trow = 32 * w + (l & 31);
        s16x8 wfk[2][3];
        auto load_wf = [&](s16x8 (&wf)[2][3], u32 base) {
#pragma unroll
            for (int to = 0; to < 2; to++)
#pragma unroll
                for (int s = 0; s < 3; s++) {
                    s16x4 t[2];
#pragma unroll
                    for (int rd = 0; rd < 2; rd++) {
                        const int mu = 2 * s + rd;
                        if (mu < 5) {
                            int dq = mu + hk; if (mu == 4) dq = hk ? 0 : 4;
                            const bool ok = same && (to == 0 || rq == 0);
                            t[rd] = trd(ok ? base + hk * 800 + (4 * dq + q) * 40 + (4 * to + rq) * 8 : LZERO);
                        } else t[rd] = s16x4{0, 0, 0, 0};
                    }
                    wf[to][s] = cat(t[0], t[1]);
                }
        };
        asm volatile("s_waitcnt vmcnt(0)" ::"v"(pfd) : "memory");
        bar();
        const u32 sw = ((trow & 2) << 1) | ((trow & 4) >> 1) | (trow & 1);
        const u32 rowa = LIMG + h * XSLOT + trow * 128;
        auto gemm = [&](auto HV) {
            constexpr bool hv = decltype(HV)::value;
            constexpr int NB = hv ? 3 : 1, NM = hv ? 6 : 3, NR = hv ? 11 : 7;
            f32x16 vc[3] = {};
            u32 aX[4], aXh[4];
            const int tx = hv ? 32 * (w - 2) + (l & 31) : trow;
#pragma unroll
            for (int k = 0; k < 4; k++) {
                aX[k] = tx * 128 + (((2 * k + h) ^ ((tx >> 1) & 7)) << 4); aXh[k] = aX[k] + LIMG;
                asm volatile("" : "+v"(aX[k]), "+v"(aXh[k]));
            }
            s16x4 fa[2][8]; s16x8 fb[2][NB];
            auto rd = [&](int s, int i, int ks, int n) {
                const int nb = hv ? (n == 0 ? 1 : n == 9 ? 0 : n == 10 ? 2 : -1) : (n == 0 ? 0 : -1);
                if (nb >= 0) {
                    const int sl = (i >> 1) % 3, k = (i & 1) * 2 + ks;
                    fb[s][nb] = sl == 0 ? ldsr<s16x8>(aX[k] + nb * 8192) : ldsr<s16x8>(aXh[k] + (sl - 1) * XSLOT + nb * 8192);
                } else {
                    const int na = n <= 4 ? n - 1 : n <= 6 ? n + 1 : n - 3;
                    const u32 o = (i & 3) * WSLOT + ks * 4 * WBLK + (na & 1) * WBLK;
                    fa[s][na] = na < 6 ? trd(bA + o + (na >> 1) * 256) : trd(bD + o);
                }
            };
            auto mm = [&](int s, int t) {
                if (t < 3) { const int tt = t == 2 ? 3 : t; acc[tt] = mfma32(cat(fa[s][2 * tt], fa[s][2 * tt + 1]), fb[s][hv ? 1 : 0], acc[tt]); }
                else { const int nb = t == 3 ? 1 : t == 4 ? 0 : 2; vc[nb] = mfma32(cat(fa[s][4], fa[s][5]), fb[s][hv ? nb : 0], vc[nb]); }
            };
            auto halfstep = [&](int s, int i, int ks, bool next) {
#pragma unroll
                for (int t = 0; t < NM; t++) {
                    SB; mm(s, t); SB;
                    if (t == 0 && ks == 0) bar();
                    if (next)
#pragma unroll
                        for (int n = 3 * t; n < 3 * t + 3 && n < NR; n++) rd(s ^ 1, i, ks, n);
                }
            };
#pragma unroll
            for (int n = 0; n < NR; n++) rd(0, 0, 0, n);
#pragma unroll
            for (int i = 0; i < 20; i++) {
                halfstep(0, i, 1, true);
                if (i == 1) load_wf(wfk, LMISC);
                if (i == 2) load_wf(wfq, LMISC + 1600);
                halfstep(1, i + 1, 0, i < 19);
            }
            if (hv) {
#pragma unroll
                for (int nb = 0; nb < 3; nb++)
#pragma unroll
                    for (int mu = 0; mu < 4; mu++) {
                        const int pc = 10 + mu + h;
                        ldsw<u32x2>(rowa + (nb - 1) * 8192 + (((pc >> 1) ^ sw) << 4) + (pc & 1) * 8,
                                    u32x2{pk(vc[nb][4 * mu], vc[nb][4 * mu + 1]), pk(vc[nb][4 * mu + 2], vc[nb][4 * mu + 3])});
                    }
            }
        };
        if ((w & 3) >= 2) gemm(Flag<true>{}); else gemm(Flag<false>{});
        float kr[20]; u32 phik[20];
#pragma unroll
        for (int i = 0; i < 20; i++) kr[i] = i < 16 ? acc[1][i] : acc[3][i - 12];
        features(kr, wfk, phik, [&]() {
            const int pc = h ? 10 : 14;
            ldsw<u32x2>(rowa + (((pc >> 1) ^ sw) << 4) + (pc & 1) * 8, u32x2{pk(acc[3][8], acc[3][9]), pk(acc[3][10], acc[3][11])});
            ldsw<u32x2>(rowa + ((7 ^ sw) << 4) + 8, u32x2{0x3f80u, 0u});
        });
        if (192 * r + 32 * w >= 2048) {
#pragma unroll
            for (int i = 0; i < 20; i++) phik[i] = 0;
        }
#pragma unroll
        for (int ch = 0; ch < 5; ch++) ldsw<u32x4>(rowa + ((ch ^ sw) << 4), u32x4{phik[4 * ch], phik[4 * ch + 1], phik[4 * ch + 2], phik[4 * ch + 3]});
    }
    bar();
    tag = ldsr<u64>(LZERO + 16) + 1;
    if (w < 4) {
        const int pcn = 4 * (g & 1) + p;
        const int pa = 10 + (pcn < 6 ? pcn : 5), pb = 5 * (w & 1) + (pcn < 5 ? pcn : 4);
        u32 aa[2], ab[2];
#pragma unroll
        for (int rd = 0; rd < 2; rd++) {
            const int row = 8 * (g >> 1) + 4 * rd + q;
            const u32 sw = ((row & 2) << 1) | ((row & 4) >> 1) | (row & 1);
            const u32 base = LIMG + (w >> 1) * XSLOT + row * 128;
            aa[rd] = base + (((pa >> 1) ^ sw) << 4) + (pa & 1) * 8;
            ab[rd] = base + (((pb >> 1) ^ sw) << 4) + (pb & 1) * 8;
            asm volatile("" : "+v"(aa[rd]), "+v"(ab[rd]));
        }
        f32x16 kv = {};
#pragma unroll
        for (int st = 0; st < 2; st++) {
            constexpr int K0[3] = {0, 8, 12};
            s16x4 ta[16], tb[16];
#pragma unroll
            for (int ks = K0[st]; ks < K0[st + 1]; ks++) {
                const int n = 2 * (ks - K0[st]);
                ta[n] = trd(aa[0] + ks * 2048); ta[n + 1] = trd(aa[1] + ks * 2048);
                tb[n] = trd(ab[0] + ks * 2048); tb[n + 1] = trd(ab[1] + ks * 2048);
            }
            SB;
#pragma unroll
            for (int ks = K0[st]; ks < K0[st + 1]; ks++) { const int n = 2 * (ks - K0[st]); kv = mfma32(cat(ta[n], ta[n + 1]), cat(tb[n], tb[n + 1]), kv); }
        }
        if ((l & 31) < 20) {
            const u32 lam = h * 20 + (l & 31), o = WS_PART + (c * 16 + r) * 4096 + w * 960;
            st128(u32x4{pk(kv[0], kv[1]), pk(kv[2], kv[3]), pk(kv[4], kv[5]), pk(kv[6], kv[7])}, rsw, lam * 16, o);
            __builtin_amdgcn_raw_buffer_store_b64(u32x2{pk(kv[8], kv[9]), pk(kv[10], kv[11])}, rsw, 640 + lam * 8, o, 16);
        }
    }
    auto qfeat = [&]() {
        float qr[20];
#pragma unroll
        for (int i = 0; i < 20; i++) qr[i] = i < 16 ? acc[0][i] : acc[3][i - 16];
        features(qr, wfq, phiq, []() {});
#pragma unroll
        for (int i = 0; i < 20; i++) asm volatile("" : "+v"(phiq[i]));
    };
#pragma unroll 1
    for (int st = 0; st < 2; st++) {
        if (w < 6 && (w < 4) == (st == 1)) qfeat();
        if (st == 1) break;
        if (w < 4) waitv<0>();
        bar();
        post_flag(ws + WS_F1 + c * 4096, tag, tid, 11, r);
    }
    if (tid < 80) ldsw<u64>(LTAB + tid * TROW + 48, 0ull);
    if (w == 6) wait_flags(ws + WS_F1 + (c ^ 1) * 4096 + r * 256, tag, l, 11);
    bar();
    if (w < 4) {
        const int t = tid < 240 ? tid : 239;
        const int un = (t * 1093) >> 16, it = t - un * 60;
        u32x4 pv[11];
#pragma unroll
        for (int i = 0; i < 11; i++) pv[i] = __builtin_amdgcn_raw_buffer_load_b128(rsw, t * 16, WS_PART + (c ^ 1) * 65536 + i * 4096, 16);
        float s[8] = {};
        u32 slo = 0x00003f80u, shi = 0x3f800000u;
        asm volatile("" : "+s"(slo), "+s"(shi));
#pragma unroll
        for (int i = 0; i < 11; i++)
#pragma unroll
            for (int e = 0; e < 4; e++) { s[2 * e] = dot2(pv[i][e], slo, s[2 * e]); s[2 * e + 1] = dot2(pv[i][e], shi, s[2 * e + 1]); }
        const int lam = it < 40 ? it : 2 * (it - 40);
        const int hh = lam >= 20, f = lam - 20 * hh;
        const u32 a0 = LTAB + (un >> 1) * THEAD + ((un & 1) * 20 + f) * TROW + hh * 8 + (it < 40 ? 0 : 32);
        ldsw<u32x2>(a0, u32x2{pk(s[0], s[1]), pk(s[2], s[3])});
        ldsw<u32x2>(a0 + (it < 40 ? 16 : TROW), u32x2{pk(s[4], s[5]), pk(s[6], s[7])});
    }
    bar();
    if (w < 6 && 192 * r + 32 * w < 2048) {
        const u32 tb = LTAB + hk * THEAD + q * TROW;
        u32 t0 = tb + (same ? rq : 6) * 8, t1 = tb + (same && rq < 2 ? 4 + rq : 6) * 8;
        asm volatile("" : "+v"(t0), "+v"(t1));
        s16x4 f0[10], f1[10];
#pragma unroll
        for (int s = 0; s < 10; s++) { f0[s] = trd(t0 + 4 * s * TROW); f1[s] = trd(t1 + 4 * s * TROW); }
        SB;
        f32x16 o0 = {}, o1 = {};
#pragma unroll
        for (int s = 0; s < 5; s++) {
            const s16x8 B = frag(phiq[4 * s], phiq[4 * s + 1], phiq[4 * s + 2], phiq[4 * s + 3]);
            o1 = mfma32(cat(f1[2 * s], f1[2 * s + 1]), B, o1);
            o0 = mfma32(cat(f0[2 * s], f0[2 * s + 1]), B, o0);
        }
        const float rden = __builtin_amdgcn_rcpf(o1[4] + 1e-6f);
        u32 ob[10];
#pragma unroll
        for (int i = 0; i < 10; i++) ob[i] = i < 8 ? pk(o0[2 * i] * rden, o0[2 * i + 1] * rden) : pk(o1[2 * i - 16] * rden, o1[2 * i - 15] * rden);
        const u32 o = WS_O + ((r * 16 + (c ^ 1)) * 6 + w) * 2560;
        st128(u32x4{ob[0], ob[1], ob[2], ob[3]}, rsw, l * 16, o);
        st128(u32x4{ob[4], ob[5], ob[6], ob[7]}, rsw, l * 16 + 1024, o);
        __builtin_amdgcn_raw_buffer_store_b64(u32x2{ob[8], ob[9]}, rsw, l * 8 + 2048, o, 16);
    }
    waitv<0>();
    asm volatile("" ::"v"(pfd));
    bar();
    post_flag(ws + WS_F2 + r * (24 * 256), tag, tid, 24, c);
    return tag;
}

DI f32x2 cmul(f32x2 a, f32x2 b) {
    f32x2 d;
    asm("v_pk_mul_f32 %0, %1, %2 op_sel_hi:[0,1]\n\tv_pk_fma_f32 %0, %1, %2, %0 op_sel:[1,1,0] op_sel_hi:[1,0,1] neg_lo:[0,1,0]" : "=&v"(d) : "v"(a), "v"(b));
    return d;
}
DI f32x2 cmulc(f32x2 a, f32x2 b) {
    f32x2 d;
    asm("v_pk_mul_f32 %0, %1, %2 op_sel_hi:[0,1] neg_hi:[0,1]\n\tv_pk_fma_f32 %0, %1, %2, %0 op_sel:[1,1,0] op_sel_hi:[1,0,1]" : "=&v"(d) : "v"(a), "v"(b));
    return d;
}
DI f32x2 csq(f32x2 a) { return cmul(a, a); }
DI f32x2 cexp(float a, float b) {
    const float m = __builtin_amdgcn_exp2f(a * 1.4426950408889634f);
    const float c1 = 0.15915494309189535f, c2 = 6.4206382e-9f;
    const float n = __builtin_rintf(b * c1);
    const float f = __builtin_fmaf(b, c2, __builtin_fmaf(b, c1, -n));
    return f32x2{m * __builtin_amdgcn_cosf(f), m * __builtin_amdgcn_sinf(f)};
}
constexpr int LOWB = 7168, LQ = 8 * LOWB, QB = 8448, LU = 131072;
static_assert(LQ + 8 * QB + 512 <= LU, "LDS map");
DI u32 kaddr(u32 e) { return ((e >> 5) << 8) + ((e & 31) << 1); }

DI void prologue_a(int ia, const u16* __restrict__ Are, const u16* __restrict__ Aim, const u16* __restrict__ Bre, const u16* __restrict__ Bim,
                   const u16* __restrict__ Cre, const u16* __restrict__ Cim, const u16* __restrict__ logdt, const u16* __restrict__ Dskip) {
    const int l = threadIdx.x & 63, w = __builtin_amdgcn_readfirstlane(threadIdx.x >> 6);
    const u16* src = w == 0 ? Are : w == 1 ? Aim : w == 2 ? Bre : w == 3 ? Bim : w == 4 ? Cre : w == 5 ? Cim : w == 6 ? logdt : Dskip;
    if (w < 6) { dma(w * 2048, ia * 2048 + l * 16, src); dma(w * 2048 + 1024, ia * 2048 + 1024 + l * 16, src); }
    else dma_m(w * 2048, ia * 16, src, 1u, 0u);
}

DI u64 path_a(const u16* __restrict__ u, const u16* __restrict__ wo, u16* __restrict__ out, char* __restrict__ ws, int ia, u32 pfd) {
    const int tid = threadIdx.x, l = tid & 63;
    const int w = __builtin_amdgcn_readfirstlane(tid >> 6);
    const u32 LB = w * LOWB, KB = LQ + w * QB, UB = LU + w * 4096;
    const int g = l >> 4, q = (l >> 2) & 3, p = l & 3, h = l >> 5;
    asm volatile("s_waitcnt vmcnt(0)" ::"v"(pfd) : "memory");
    bar();
    const float ldt = b2f(ldsr<u16>(6 * 2048 + w * 2)), dsk = b2f(ldsr<u16>(7 * 2048 + w * 2));
    u32 prm[6];
#pragma unroll
    for (int i = 0; i < 6; i++) prm[i] = ldsr<u32>(i * 2048 + w * 256 + l * 4);
    bar();
    u32x4 ur[4];
    auto u_in = [&]() {
#pragma unroll
        for (int i = 0; i < 4; i++) ur[i] = *(const u32x4*)((const char*)u + (size_t)(256 * ((w + ia) & 7) + 4 * l + i) * 1280 + ia * 16);
    };
    const float dt = lo2f(pk(expf(ldt), 0.f));
    f32x16 kd = {};
    u32 rx, ry[2];
    {
        const int pc = 4 * (g & 1) + p;
        rx = LB + (8 * (g >> 1) + q) * 48 + (pc < 6 ? pc : 5) * 8;
#pragma unroll
        for (int rd = 0; rd < 2; rd++) ry[rd] = KB + (8 * (g >> 1) + 4 * rd + q) * 64 + (((pc >> 1) ^ ((2 * rd + (q >> 1)) & 3)) << 4) + (p & 1) * 8;
    }
    const u32 wx = LB + l * 48, wy = KB + l * 64, wsw = (l >> 1) & 3;
#pragma unroll
    for (int ps = 0; ps < 2; ps++) {
        float pf[6];
#pragma unroll
        for (int i = 0; i < 6; i++) pf[i] = ps ? hi2f(prm[i]) : lo2f(prm[i]);
        const float a = dt * pf[0], b = dt * pf[1];
        const f32x2 Wc = cmulc(f32x2{pf[4], -pf[5]}, f32x2{dt * pf[2], dt * pf[3]});
        const f32x2 e1 = cexp(a, b), e16 = cexp(16.f * a, 16.f * b), e256 = cexp(256.f * a, 256.f * b);
        const f32x2 e2 = csq(e1), e4 = csq(e2), e8 = csq(e4), e32 = csq(e16), e64 = csq(e32), e128 = csq(e64), e512 = csq(e256), e1024 = csq(e512);
        f32x2 X[24];
        X[0] = Wc; X[1] = cmulc(Wc, e1); X[2] = cmulc(Wc, e2); X[3] = cmulc(X[1], e2);
#pragma unroll
        for (int i = 0; i < 4; i++) X[4 + i] = cmulc(X[i], e4);
        X[8] = cmulc(Wc, e256); X[9] = cmulc(X[8], e2); X[10] = cmulc(X[8], e4);
        X[12] = X[10]; X[13] = cmulc(X[9], e4); X[14] = cmulc(X[8], e8);
        X[11] = cmulc(Wc, e512); X[16] = cmulc(X[11], e4); X[17] = cmulc(X[11], e8);
        X[15] = cmulc(X[8], e512); X[20] = cmulc(X[15], e4); X[21] = cmulc(X[15], e8);
        X[18] = cmulc(cmulc(Wc, e8), e1024); X[19] = cmulc(X[14], e1024);
        X[22] = cmulc(X[17], e1024); X[23] = cmulc(X[21], e1024);
        f32x2 Y[8];
        Y[0] = f32x2{1.f, 0.f}; Y[1] = e8; Y[2] = e16; Y[3] = cmul(e8, e16); Y[4] = e32; Y[5] = cmul(e8, e32); Y[6] = cmul(e16, e32); Y[7] = cmul(Y[3], e32);
        const f32x2 e192 = cmul(e64, e128);
#pragma unroll
        for (int ch = 0; ch < 4; ch++) {
            if (ch < 3) {
                const f32x2* Xc = X + 8 * ch;
                ldsw<u32x4>(wx + ch * 16, u32x4{pk(Xc[0].x, Xc[1].x), pk(Xc[2].x, Xc[3].x), pk(Xc[4].x, Xc[5].x), pk(Xc[6].x, Xc[7].x)});
                ldsw<u32x4>(wx + ch * 16 + 3072, u32x4{pk(Xc[0].y, Xc[1].y), pk(Xc[2].y, Xc[3].y), pk(Xc[4].y, Xc[5].y), pk(Xc[6].y, Xc[7].y)});
            }
            f32x2 Z[8];
            const f32x2 m = ch == 1 ? e64 : ch == 2 ? e128 : e192;
#pragma unroll
            for (int i = 0; i < 8; i++) Z[i] = ch == 0 ? Y[i] : i == 0 ? m : cmul(Y[i], m);
            const u32 o = wy + ((ch ^ wsw) << 4);
            ldsw<u32x4>(o, u32x4{pk(Z[0].x, Z[1].x), pk(Z[2].x, Z[3].x), pk(Z[4].x, Z[5].x), pk(Z[6].x, Z[7].x)});
            ldsw<u32x4>(o + 4096, u32x4{pk(Z[0].y, Z[1].y), pk(Z[2].y, Z[3].y), pk(Z[4].y, Z[5].y), pk(Z[6].y, Z[7].y)});
        }
        s16x4 fx[16], fy[16];
#pragma unroll
        for (int ks = 0; ks < 8; ks++) {
            fx[2 * ks] = trd(rx + ks * 768); fx[2 * ks + 1] = trd(rx + ks * 768 + 192);
            fy[2 * ks] = trd(ry[0] + ks * 1024); fy[2 * ks + 1] = trd(ry[1] + ks * 1024);
        }
        if (ps == 0) { u_in(); }
#pragma unroll
        for (int ks = 0; ks < 8; ks++) kd = mfma32(cat(fx[2 * ks], fx[2 * ks + 1]), cat(fy[2 * ks], fy[2 * ks + 1]), kd);
    }
    constexpr int LR = 4096;
    {
        const int j = l & 31;
        ldsw<u32x2>(KB + kaddr(1052 - 8 * j - 4 * h), u32x2{pk(kd[3], kd[2]), pk(kd[1], l == 0 ? kd[0] + dsk : kd[0])});
        ldsw<u32x2>(KB + kaddr(1052 - 256 - 8 * j - 4 * h), u32x2{pk(kd[6], kd[5]), pk(kd[4], kd[4])});
        const u32 cc = pk(kd[9], kd[9]);
        ldsw<u32x4>(KB + kaddr(1048 - 512 - 256 * h - 8 * j), u32x4{cc, pk(kd[8], kd[8]), pk(kd[8], kd[7]), pk(kd[7], kd[7])});
        if (l >= 60) ldsw<u32x4>(KB + (l - 60) * 16, l == 63 ? u32x4{0u, cc & 0xffff0000u, cc, cc} : u32x4{0u, 0u, 0u, 0u});
        const u32 far = pk(kd[10], kd[11]);
        ldsw<u16>(LB + LR + (127 - 64 * h - j) * 2, (u16)far);
        ldsw<u16>(LB + LR + (95 - 64 * h - j) * 2, (u16)(far >> 16));
        if (l < 16) ldsw<u32>(LB + LR + 256 + l * 4, 0u);
        u32x4 v[3]; u32x2 n[3];
        u32 a0[3];
#pragma unroll
        for (int it = 0; it < 3; it++) {
            const u32 i = it < 2 ? 64 * it + l : 128 + (l & 3);
            a0[it] = KB + kaddr(8 * i);
            v[it] = ldsr<u32x4>(a0[it]); n[it] = ldsr<u32x2>(KB + kaddr(8 * i + 8));
        }
#pragma unroll
        for (int it = 0; it < 3; it++) {
            const u32 s0 = __builtin_amdgcn_alignbit(v[it][1], v[it][0], 16), s1 = __builtin_amdgcn_alignbit(v[it][2], v[it][1], 16),
                      s2 = __builtin_amdgcn_alignbit(v[it][3], v[it][2], 16), s3 = __builtin_amdgcn_alignbit(n[it][0], v[it][3], 16),
                      s4 = __builtin_amdgcn_alignbit(n[it][1], n[it][0], 16);
            if (it < 2 || l < 4) {
                ldsw<u32x4>(a0[it] + 64, u32x4{s0, s1, s2, s3});
                ldsw<u32x4>(a0[it] + 128, u32x4{v[it][1], v[it][2], v[it][3], n[it][0]});
                ldsw<u32x4>(a0[it] + 192, u32x4{s1, s2, s3, s4});
            }
        }
    }
    u64 tag = 0;
    if (w == 0) {
        tag = epoch(ws) + 1;
        if (l == 0) __hip_atomic_store((GAS u64*)(ws + WS_ACK + ia * 8), tag, __ATOMIC_RELAXED, __HIP_MEMORY_SCOPE_AGENT);
    }
    {
        const u32 a = LU + (256 * ((w + ia) & 7) + 4 * l) * 2;
#pragma unroll
        for (int c2 = 0; c2 < 4; c2++) {
            ldsw<u32x2>(a + 2 * c2 * 4096, u32x2{__builtin_amdgcn_perm(ur[1][c2], ur[0][c2], 0x05040100u), __builtin_amdgcn_perm(ur[3][c2], ur[2][c2], 0x05040100u)});
            ldsw<u32x2>(a + (2 * c2 + 1) * 4096, u32x2{__builtin_amdgcn_perm(ur[1][c2], ur[0][c2], 0x07060302u), __builtin_amdgcn_perm(ur[3][c2], ur[2][c2], 0x07060302u)});
        }
    }
    bar();
    const int rr = l & 15;
    f32x4 y[4] = {};
    {
        float sm[36];
        sm[0] = 0.f;
#pragma unroll
        for (int i = 0; i < 5; i++) {
            u32x4 t = ldsr<u32x4>(UB + l * 32 - 48 + i * 16);
            if (i < 3 && 2 * l + i < 3) t = u32x4{0, 0, 0, 0};
#pragma unroll
            for (int e = 0; e < 8; e++)
                if (8 * i + e < 35) sm[8 * i + e + 1] = sm[8 * i + e] + ((e & 1) ? hi2f(t[e >> 1]) : lo2f(t[e >> 1]));
        }
        const u32 wb = LB + l * 64, sw = (l >> 1) & 3;
#pragma unroll
        for (int ch = 0; ch < 4; ch++) {
            const int o1 = ch < 2 ? 13 : 20, o0 = ch < 2 ? 4 : 13, b0 = 8 * (ch & 1);
            ldsw<u32x4>(wb + ((ch ^ sw) << 4), u32x4{pk(sm[b0 + o1] - sm[b0 + o0], sm[b0 + 1 + o1] - sm[b0 + 1 + o0]), pk(sm[b0 + 2 + o1] - sm[b0 + 2 + o0], sm[b0 + 3 + o1] - sm[b0 + 3 + o0]),
                                                     pk(sm[b0 + 4 + o1] - sm[b0 + 4 + o0], sm[b0 + 5 + o1] - sm[b0 + 5 + o0]), pk(sm[b0 + 6 + o1] - sm[b0 + 6 + o0], sm[b0 + 7 + o1] - sm[b0 + 7 + o0])});
        }
        s16x8 fw[4], ft[4];
#pragma unroll
        for (int s = 0; s < 4; s++) {
            s16x4 t[2];
#pragma unroll
            for (int rd = 0; rd < 2; rd++) {
                const int blk = 4 * g + 2 * rd + (q >> 1);
                t[rd] = trd(LB + s * 1024 + blk * 64 + (((2 * (q & 1) + (p >> 1)) ^ ((blk >> 1) & 3)) << 4) + (p & 1) * 8);
            }
            fw[s] = cat(t[0], t[1]);
            const u32 a = LB + LR + (126 - 32 * s - 2 * rr + 8 * g) * 2;
            ft[s] = frag(ldsr<u32>(a), ldsr<u32>(a + 4), ldsr<u32>(a + 8), ldsr<u32>(a + 12));
        }
#pragma unroll
        for (int s = 0; s < 4; s++)
#pragma unroll
            for (int ct = s; ct < 4; ct++) y[ct] = mfma16(fw[s], ft[ct - s], y[ct]);
    }
    bar();
    load_wo(wo, (sbid() >> 3) & 7, (w + 4) & 7, 11, 8, l);
    const u32 e0 = 1055 - 16 - rr + 8 * g, sh = e0 & 3;
    auto half = [&](auto HI) {
        constexpr bool hi = decltype(HI)::value;
        constexpr int T0 = hi ? 4 : 0, B0 = hi ? 1 : 0, NB = hi ? 7 : 4, NRD = NB + 8;
        u32 ka0 = KB + kaddr(e0 - sh) + sh * 64 - 3 * 2048, ka1 = KB + kaddr(e0 - sh + 4) + sh * 64 - 3 * 2048;
        u32 ua = UB + 32 * rr + 16 * g - 32;
        u32 mk[4];
#pragma unroll
        for (int i = 0; i < 4; i++) mk[i] = (2 * i > 16 + rr - 8 * g ? 0u : 0xffffu) | (2 * i + 1 > 16 + rr - 8 * g ? 0u : 0xffff0000u);
        int lim = 2 * rr + g - 2;
        u32x2 A[2][8]; u32x4 B[2][NB];
        auto rdc = [&](int s, int n) {
            if (n < 12) {
                const int m = n / 3, k = n % 3;
                if (k == 0) B[s][m] = ldsr<u32x4>(ua + (B0 + m) * 512);
                else if (k == 1) A[s][2 * (3 - m)] = ldsr<u32x2>(ka0 + m * 2048);
                else A[s][2 * (3 - m) + 1] = ldsr<u32x2>(ka1 + m * 2048);
            } else B[s][n - 8] = ldsr<u32x4>(ua + (B0 + n - 8) * 512);
        };
        auto group = [&](int s, bool next) {
            A[s][0] &= u32x2{mk[0], mk[1]}; A[s][1] &= u32x2{mk[2], mk[3]};
            if (!hi && lim < 0) B[s][0] = u32x4{0, 0, 0, 0};
            ka0 -= 256; ka1 -= 256; ua -= 64; lim -= 4;
            mk[0] = mk[1] = mk[2] = mk[3] = 0xffffffffu;
            asm volatile("" : "+v"(ka0), "+v"(ka1), "+v"(ua));
            int n = 0;
#pragma unroll
            for (int m = 3; m >= 0; m--)
#pragma unroll
                for (int t = 0; t < 4; t++) {
                    const int ct = T0 + t - m;
                    if (ct < 0) continue;
                    SB;
                    y[t] = mfma16(frag(A[s][2 * m][0], A[s][2 * m][1], A[s][2 * m + 1][0], A[s][2 * m + 1][1]), __builtin_bit_cast(s16x8, B[s][ct - B0]), y[t]);
                    SB;
                    if (next && n < NRD) rdc(s ^ 1, n);
                    n++;
                    if (!hi && next && (n == 1 || n == 6)) rdc(s ^ 1, n++);
                }
        };
        asm volatile("" : "+v"(ka0), "+v"(ka1), "+v"(ua));
#pragma unroll
        for (int n = 0; n < NRD; n++) rdc(0, n);
        if (hi) {
            const u32x2 x0 = ldsr<u32x2>(ka0 - 2048), x1 = ldsr<u32x2>(ka1 - 2048);
            u32x4 b0 = ldsr<u32x4>(ua);
            if (lim < 0) b0 = u32x4{0, 0, 0, 0};
            const s16x8 xa = frag(x0[0], x0[1], x1[0], x1[1]);
            y[0] = mfma16(xa, __builtin_bit_cast(s16x8, b0), y[0]);
#pragma unroll
            for (int t = 1; t < 4; t++) y[t] = mfma16(xa, __builtin_bit_cast(s16x8, B[0][t - 1]), y[t]);
        }
        if (hi) {
#pragma unroll 1
            for (int gg = 0; gg < 3; gg++) { group(0, true); group(1, true); }
            group(0, true); group(1, false);
        } else {
            u32 v[8];
#pragma unroll
            for (int c2 = 0; c2 < 8; c2++) v[c2] = ldsr<u32>(LU + c2 * 4096 + 2048 + tid * 4);
            const u32 op = (1024 + 2 * tid) * 1280 + 8 * ia;
#pragma unroll
            for (int gg = 0; gg < 8; gg++) {
                if (gg == 0 || gg == 4) {
                    const u32 sel = gg ? 0x07060302u : 0x05040100u;
                    out16(out, op + (gg ? 1280 : 0), u32x4{__builtin_amdgcn_perm(v[1], v[0], sel), __builtin_amdgcn_perm(v[3], v[2], sel),
                                                           __builtin_amdgcn_perm(v[5], v[4], sel), __builtin_amdgcn_perm(v[7], v[6], sel)});
                }
                group(gg & 1, gg < 7);
            }
        }
#pragma unroll
        for (int ct = 0; ct < 4; ct++) {
            ldsw<u32x2>(UB + (T0 + ct) * 512 + rr * 32 + g * 8, u32x2{pk(y[ct][0], y[ct][1]), pk(y[ct][2], y[ct][3])});
            y[ct] = f32x4{0.f, 0.f, 0.f, 0.f};
        }
        bar();
        if (!hi) {
            load_wo(wo, (sbid() >> 3) & 7, 11 + w, 20, 8, l);
            if (w >= 4) store_rows(out, ia, 0, tid - 256);
        }
    };
    half(Flag<true>{});
    half(Flag<false>{});
    if (w >= 4) waitv<4>(); else waitv<0>();
    return tag;
}

__global__ void __launch_bounds__(512, 2)
fused_kernel(const u16* __restrict__ x, const u16* __restrict__ W, const u16* __restrict__ wphi, const u16* __restrict__ wo,
             u16* __restrict__ out, char* __restrict__ ws, const u16* __restrict__ u, const u16* __restrict__ Are,
             const u16* __restrict__ Aim, const u16* __restrict__ Bre, const u16* __restrict__ Bim, const u16* __restrict__ Cre,
             const u16* __restrict__ Cim, const u16* __restrict__ logdt, const u16* __restrict__ Dskip) {
    __shared__ __attribute__((aligned(16))) char smem[LDS_BYTES];
    asm volatile("" ::"s"((u32)(size_t)(LAS char*)smem));
    u32 pfd = 0;
    if (threadIdx.x < 12) {
        u64 pc; asm volatile("s_getpc_b64 %0" : "=s"(pc));
        asm volatile("global_load_dword %0, %1, %2" : "+v"(pfd) : "v"(threadIdx.x * 128), "s"(pc + min((sbid() >> 3) * 1536, CODE_PF_BYTES - 1536)));
    }
    asm volatile("" ::"s"(x), "s"(W), "s"(wphi), "s"(wo), "s"(out), "s"(ws), "s"(u), "s"(Are), "s"(Aim), "s"(Bre), "s"(Bim), "s"(Cre), "s"(Cim),
                 "s"(logdt), "s"(Dskip));
    const Role R = role_of(sbid());
    u64 tag;
    if (!R.is_a) {
        if (__builtin_amdgcn_readfirstlane(threadIdx.x) >= 384) loader_b<true>(x, W, wo, ws, R.r, R.c, __builtin_amdgcn_readfirstlane(threadIdx.x >> 6) - 6, pfd, tag);
    } else prologue_a(R.ia, Are, Aim, Bre, Bim, Cre, Cim, logdt, Dskip);
    u32 sel = R.nb;
    asm volatile("" : "+s"(sel));
    if ((sbid() >> 3) >= sel) {
        tag = path_a(u, wo, out, ws, R.ia, pfd);
    } else {
        tag = path_b(x, W, wphi, wo, out, ws, R.r, R.c, pfd);
    }
    out_proj(out, ws, tag);
}

extern "C" void kernel_launch(void* const* d_in, const int* in_sizes, int n_in, void* d_out, int out_size, void* d_ws, size_t ws_size,
                              hipStream_t stream) {
    const u16* const* in = (const u16* const*)d_in;
    fused_kernel<<<256, 512, 0, stream>>>(in[9], in[10], in[11], in[12], (u16*)d_out, (char*)d_ws, in[0], in[1], in[2], in[3], in[4], in[5],
                                          in[6], in[7], in[8]);
}
```
